```python
import torch

def _softmax(x, axis=-1):
    return torch.softmax(x, dim=axis)
def _gelu(x):
    return torch.nn.functional.gelu(x, approximate="tanh")
def _silu(x):
    return torch.nn.functional.silu(x)
def _sigmoid(x):
    return torch.sigmoid(x)
def _layernorm(x, axis=-1, eps=1e-5):
    m = x.mean(dim=axis, keepdim=True)
    v = x.var(dim=axis, keepdim=True, unbiased=False)
    return (x - m) / torch.sqrt(v + eps)
def _scatter_add(tgt, idx, src, axis=0):
    return tgt.clone().index_add_(axis, idx, src)
def _segment_sum(src, seg, n):
    out = torch.zeros((n,) + src.shape[1:], dtype=src.dtype)
    return out.index_add_(0, seg, src)
def _pad(x, pw):
    flat = tuple(v for pair in reversed(pw) for v in pair)
    return torch.nn.functional.pad(x, flat)

kernel_name = 'standalone__mha_softmax__near_ridge'

def setup_inputs():

    _w = (lambda a: (lambda t: t.to(torch.bfloat16) if t.is_floating_point() else t)(torch.as_tensor(a)))
    B, H, Sq, Sk, D = 16, 32, 512, 512, 192
    return {
        "q": _w((lambda *s: torch.randn(*s, dtype=torch.bfloat16) * 0.8)(B, H, Sq, D)),
        "k": _w((lambda *s: torch.randn(*s, dtype=torch.bfloat16) * 0.8)(B, H, Sk, D)),
        "v": _w((lambda *s: torch.randn(*s, dtype=torch.bfloat16) * 0.05)(B, H, Sk, D)),
    }

def reference(q, k, v):
    scale = q.shape[-1] ** -0.5
    scores = torch.einsum("bhqd,bhkd->bhqk", q, k) * scale
    p = _softmax(scores, axis=-1)
    return torch.einsum("bhqk,bhkd->bhqd", p, v)
```

```cpp
#include <hip/hip_runtime.h>
#include <hip/hip_bf16.h>

typedef unsigned u32x4 __attribute__((ext_vector_type(4)));

__global__ __launch_bounds__(256, 1) void mha_fwd_kernel(const void* __restrict__ Q, const void* __restrict__ K, const void* __restrict__ V,
                                                         void* __restrict__ O, unsigned* __restrict__ ws) {
  __shared__ __attribute__((aligned(1024))) char lds[163840];
  const unsigned tid = threadIdx.x, L = tid & 63u;
  const unsigned wid = __builtin_amdgcn_readfirstlane(tid >> 6);
  const unsigned ldsb = (unsigned)(size_t)lds;
  const unsigned bx = blockIdx.x, xcd = bx & 7u, sl = bx >> 3, pair = sl >> 1, half = sl & 1u;
  const unsigned head0 = xcd * 16u + pair;
  const unsigned TOT = 16u * 32u * 512u * 192u * 2u;
  const unsigned stage = ldsb + 98304u + 16384u * wid;
  const unsigned q4 = (L >> 2) & 3u, p = L & 3u, r = L >> 3, cc = L & 7u;
  const unsigned i = L & 15u, g = L >> 4, vrow = 4u * (g & 1u) + q4;
  const unsigned kb0 = ldsb + 3072u * (i >> 3) + 128u * (i & 7u) + 16u * (g ^ ((i >> 1) & 7u));
  const unsigned vb0 = ldsb + 49152u + 3072u * (g >> 1) + 128u * vrow + 16u * ((p >> 1) ^ (((vrow >> 1) & 3u) << 1)) + 8u * (p & 1u);
  const unsigned vswz = ((r >> 1) & 3u) << 1;
  const unsigned stq = stage + 128u * i + 16u * (g ^ ((i >> 1) & 7u));
  const unsigned sto = stage + 128u * i + 16u * ((g >> 1) ^ ((i >> 1) & 7u)) + 8u * (g & 1u);
  const unsigned qdir = 384u * i + 256u + 16u * g;
  const unsigned ks0 = 384u * r + 16u * (cc ^ (r >> 1)), ks1 = 384u * r + 16u * (cc ^ (4u | (r >> 1)));
  const unsigned vsrc = 384u * r + 16u * (cc ^ vswz);
  const unsigned stw = stage + 16u * L;
  const unsigned long long pk = (unsigned long long)K, pv = (unsigned long long)V, pq = (unsigned long long)Q, po = (unsigned long long)O;
  const u32x4 srdK = {(unsigned)pk, (unsigned)(pk >> 32) & 0xffffu, TOT, 0x00020000u};
  const u32x4 srdV = {(unsigned)pv, (unsigned)(pv >> 32) & 0xffffu, TOT, 0x00020000u};
  const u32x4 srdQ = {(unsigned)pq, (unsigned)(pq >> 32) & 0xffffu, TOT, 0x00020000u};
  const u32x4 srdO = {(unsigned)po, (unsigned)(po >> 32) & 0xffffu, TOT, 0x00020000u};
  const unsigned rot = 0u;
  const unsigned r2 = (xcd + (pair >> 2)) & 3u;
  const u32x4 dtab = {6144u * r2, 6144u * (1u ^ r2), 6144u * (2u ^ r2), 6144u * (3u ^ r2)};
  const unsigned dsoff = head0 * 196608u + 6144u * wid + rot * 24576u;
  const unsigned qoff = head0 * 196608u + half * 98304u + wid * 24576u;
  const unsigned m0k = ldsb + 6144u * wid, m0v = ldsb + 49152u + 6144u * wid;
  asm volatile(
    "s_nop 4\n\t"
    "s_mov_b32 s62, 0x3dd53b95\n\t"
    "s_mov_b32 s63, 0x4299ac27\n\t"
    "s_mov_b32 s61, 4294795264\n\t"
    "s_mov_b32 s69, 0\n\t"
    "s_mov_b32 s72, 0\n\t"
    "s_mov_b32 s50, 0\n\t"
    "s_add_u32 s55, s55, 4269801472\n\t"
    "s_mov_b32 s70, 0\n\t"
    "s_mov_b32 s78, 0\n\t"
    "s_mov_b32 s79, 0\n\t"
    "s_mov_b32 s80, 0\n\t"
    "s_getpc_b64 s[84:85]\n\t"
    "global_load_dword v252, v207, s[84:85] offset:0\n\t"
    "global_load_dword v253, v206, s[84:85]\n\t"
    "s_cmp_eq_u32 s81, 0\n\t"
    "s_cbranch_scc1 NOSK\n\t"
    "SKL:\n\t"
    "s_sleep 24\n\t"
    "s_sub_u32 s81, s81, 1\n\t"
    "s_cmp_lg_u32 s81, 0\n\t"
    "s_cbranch_scc1 SKL\n\t"
    "NOSK:\n\t"
    "s_add_u32 m0, s56, 0\n\t"
    "s_nop 0\n\t"
    "buffer_load_dwordx4 v200, s[36:39], s52 offen offset:0 lds\n\t"
    "s_add_u32 m0, s56, 0\n\t"
    "s_nop 0\n\t"
    "buffer_load_dwordx4 v201, s[36:39], s52 offen offset:3072 lds\n\t"
    "s_add_u32 m0, s56, 896\n\t"
    "s_nop 0\n\t"
    "buffer_load_dwordx4 v200, s[36:39], s52 offen offset:128 lds\n\t"
    "s_add_u32 m0, s56, 896\n\t"
    "s_nop 0\n\t"
    "buffer_load_dwordx4 v201, s[36:39], s52 offen offset:3200 lds\n\t"
    "s_add_u32 m0, s56, 1792\n\t"
    "s_nop 0\n\t"
    "buffer_load_dwordx4 v200, s[36:39], s52 offen offset:256 lds\n\t"
    "s_add_u32 m0, s56, 1792\n\t"
    "s_nop 0\n\t"
    "buffer_load_dwordx4 v201, s[36:39], s52 offen offset:3328 lds\n\t"
    "s_add_u32 s52, s52, 24576\n\t"
    "s_add_u32 m0, s58, 0\n\t"
    "s_add_u32 s75, s54, s96\n\t"
    "buffer_load_dwordx4 v200, s[44:47], s75 offen offset:0 nt lds\n\t"
    "s_add_u32 m0, s58, 4294965248\n\t"
    "s_add_u32 s75, s54, s96\n\t"
    "buffer_load_dwordx4 v201, s[44:47], s75 offen offset:3072 nt lds\n\t"
    "s_add_u32 m0, s58, 2048\n\t"
    "s_add_u32 s75, s54, s97\n\t"
    "buffer_load_dwordx4 v200, s[44:47], s75 offen offset:0 nt lds\n\t"
    "s_add_u32 m0, s58, 0\n\t"
    "s_add_u32 s75, s54, s97\n\t"
    "buffer_load_dwordx4 v201, s[44:47], s75 offen offset:3072 nt lds\n\t"
    "s_add_u32 m0, s58, 4096\n\t"
    "s_add_u32 s75, s54, s98\n\t"
    "buffer_load_dwordx4 v200, s[44:47], s75 offen offset:0 nt lds\n\t"
    "s_add_u32 m0, s58, 2048\n\t"
    "s_add_u32 s75, s54, s98\n\t"
    "buffer_load_dwordx4 v201, s[44:47], s75 offen offset:3072 nt lds\n\t"
    "s_add_u32 m0, s58, 6144\n\t"
    "s_add_u32 s75, s54, s99\n\t"
    "buffer_load_dwordx4 v200, s[44:47], s75 offen offset:0 nt lds\n\t"
    "s_add_u32 m0, s58, 4096\n\t"
    "s_add_u32 s75, s54, s99\n\t"
    "buffer_load_dwordx4 v201, s[44:47], s75 offen offset:3072 nt lds\n\t"
    "s_add_u32 m0, s58, 8064\n\t"
    "s_add_u32 s75, s54, s96\n\t"
    "buffer_load_dwordx4 v200, s[44:47], s75 offen offset:128 nt lds\n\t"
    "s_add_u32 m0, s58, 6016\n\t"
    "s_add_u32 s75, s54, s96\n\t"
    "buffer_load_dwordx4 v201, s[44:47], s75 offen offset:3200 nt lds\n\t"
    "s_add_u32 m0, s58, 10112\n\t"
    "s_add_u32 s75, s54, s97\n\t"
    "buffer_load_dwordx4 v200, s[44:47], s75 offen offset:128 nt lds\n\t"
    "s_add_u32 m0, s58, 8064\n\t"
    "s_add_u32 s75, s54, s97\n\t"
    "buffer_load_dwordx4 v201, s[44:47], s75 offen offset:3200 nt lds\n\t"
    "v_mov_b32 v224, 0x44400000\n\t"
    "v_mov_b32 v225, 0x44400000\n\t"
    "v_mov_b32 v226, 0x44400000\n\t"
    "v_mov_b32 v227, 0x44400000\n\t"
    "v_mov_b32 v244, 0x3f803f80\n\t"
    "v_mov_b32 v245, 0x3f803f80\n\t"
    "v_mov_b32 v246, 0x3f803f80\n\t"
    "v_mov_b32 v247, 0x3f803f80\n\t"
    "s_waitcnt vmcnt(4)\n\t"
    "s_barrier\n\t"
    "ds_read_b128 a[128:131], v204 offset:0\n\t"
    "ds_read_b128 a[132:135], v204 offset:2048\n\t"
    "ds_read_b128 a[136:139], v204 offset:4096\n\t"
    "ds_read_b128 a[140:143], v204 offset:6144\n\t"
    "ds_read_b128 a[144:147], v205 offset:0\n\t"
    "ds_read_b128 a[148:151], v205 offset:2048\n\t"
    "ds_read_b128 a[152:155], v205 offset:4096\n\t"
    "ds_read_b128 a[156:159], v205 offset:6144\n\t"
    "ds_read_b128 a[224:227], v192 offset:0\n\t"
    "ds_read_b128 a[228:231], v192 offset:6144\n\t"
    "ds_read_b128 a[232:235], v192 offset:12288\n\t"
    "ds_read_b128 a[236:239], v192 offset:18432\n\t"
    "ds_read_b128 a[240:243], v193 offset:0\n\t"
    "ds_read_b128 a[244:247], v193 offset:6144\n\t"
    "s_waitcnt lgkmcnt(4)\n\t"
    "s_add_u32 m0, s58, 12160\n\t"
    "s_add_u32 s75, s54, s98\n\t"
    "buffer_load_dwordx4 v200, s[44:47], s75 offen offset:128 nt lds\n\t"
    "v_mfma_f32_16x16x32_bf16 v[0:3], a[224:227], a[128:131], v[224:227]\n\t"
    "v_mfma_f32_16x16x32_bf16 v[16:19], a[224:227], a[132:135], v[224:227]\n\t"
    "s_add_u32 m0, s58, 10112\n\t"
    "s_add_u32 s75, s54, s98\n\t"
    "buffer_load_dwordx4 v201, s[44:47], s75 offen offset:3200 nt lds\n\t"
    "v_mfma_f32_16x16x32_bf16 v[32:35], a[224:227], a[136:139], v[224:227]\n\t"
    "ds_read_b128 a[248:251], v193 offset:12288\n\t"
    "v_mfma_f32_16x16x32_bf16 v[48:51], a[224:227], a[140:143], v[224:227]\n\t"
    "s_add_u32 m0, s58, 14208\n\t"
    "s_add_u32 s75, s54, s99\n\t"
    "buffer_load_dwordx4 v200, s[44:47], s75 offen offset:128 nt lds\n\t"
    "v_mfma_f32_16x16x32_bf16 v[52:55], a[228:231], a[140:143], v[224:227]\n\t"
    "v_mfma_f32_16x16x32_bf16 v[36:39], a[228:231], a[136:139], v[224:227]\n\t"
    "s_add_u32 m0, s58, 12160\n\t"
    "s_add_u32 s75, s54, s99\n\t"
    "buffer_load_dwordx4 v201, s[44:47], s75 offen offset:3200 nt lds\n\t"
    "v_mfma_f32_16x16x32_bf16 v[20:23], a[228:231], a[132:135], v[224:227]\n\t"
    "ds_read_b128 a[252:255], v193 offset:18432\n\t"
    "v_mfma_f32_16x16x32_bf16 v[4:7], a[228:231], a[128:131], v[224:227]\n\t"
    "s_waitcnt lgkmcnt(4)\n\t"
    "s_add_u32 s75, s54, s96\n\t"
    "buffer_load_dwordx4 a[192:195], v220, s[44:47], s75 offen offset:0\n\t"
    "v_mfma_f32_16x16x32_bf16 v[8:11], a[232:235], a[128:131], v[224:227]\n\t"
    "v_mfma_f32_16x16x32_bf16 v[24:27], a[232:235], a[132:135], v[224:227]\n\t"
    "s_add_u32 s75, s54, s97\n\t"
    "buffer_load_dwordx4 a[196:199], v220, s[44:47], s75 offen offset:0\n\t"
    "v_mfma_f32_16x16x32_bf16 v[40:43], a[232:235], a[136:139], v[224:227]\n\t"
    "ds_read_b128 a[224:227], v192 offset:1024\n\t"
    "v_mfma_f32_16x16x32_bf16 v[56:59], a[232:235], a[140:143], v[224:227]\n\t"
    "s_add_u32 s75, s54, s98\n\t"
    "buffer_load_dwordx4 a[200:203], v220, s[44:47], s75 offen offset:0\n\t"
    "v_mfma_f32_16x16x32_bf16 v[60:63], a[236:239], a[140:143], v[224:227]\n\t"
    "v_mfma_f32_16x16x32_bf16 v[44:47], a[236:239], a[136:139], v[224:227]\n\t"
    "s_add_u32 s75, s54, s99\n\t"
    "buffer_load_dwordx4 a[204:207], v220, s[44:47], s75 offen offset:0\n\t"
    "v_mfma_f32_16x16x32_bf16 v[28:31], a[236:239], a[132:135], v[224:227]\n\t"
    "ds_read_b128 a[228:231], v192 offset:7168\n\t"
    "v_mfma_f32_16x16x32_bf16 v[12:15], a[236:239], a[128:131], v[224:227]\n\t"
    "s_waitcnt lgkmcnt(4)\n\t"
    "s_add_u32 s75, s54, s96\n\t"
    "buffer_load_dwordx4 a[208:211], v220, s[44:47], s75 offen offset:64\n\t"
    "v_mfma_f32_16x16x32_bf16 v[0:3], a[240:243], a[144:147], v[0:3]\n\t"
    "v_mfma_f32_16x16x32_bf16 v[16:19], a[240:243], a[148:151], v[16:19]\n\t"
    "s_add_u32 s75, s54, s97\n\t"
    "buffer_load_dwordx4 a[212:215], v220, s[44:47], s75 offen offset:64\n\t"
    "v_mfma_f32_16x16x32_bf16 v[32:35], a[240:243], a[152:155], v[32:35]\n\t"
    "ds_read_b128 a[232:235], v192 offset:13312\n\t"
    "v_mfma_f32_16x16x32_bf16 v[48:51], a[240:243], a[156:159], v[48:51]\n\t"
    "s_add_u32 s75, s54, s98\n\t"
    "buffer_load_dwordx4 a[216:219], v220, s[44:47], s75 offen offset:64\n\t"
    "v_mfma_f32_16x16x32_bf16 v[52:55], a[244:247], a[156:159], v[52:55]\n\t"
    "v_mfma_f32_16x16x32_bf16 v[36:39], a[244:247], a[152:155], v[36:39]\n\t"
    "s_add_u32 s75, s54, s99\n\t"
    "buffer_load_dwordx4 a[220:223], v220, s[44:47], s75 offen offset:64\n\t"
    "s_add_u32 s54, s54, 25165824\n\t"
    "v_mfma_f32_16x16x32_bf16 v[20:23], a[244:247], a[148:151], v[20:23]\n\t"
    "ds_read_b128 a[236:239], v192 offset:19456\n\t"
    "v_mfma_f32_16x16x32_bf16 v[4:7], a[244:247], a[144:147], v[4:7]\n\t"
    "s_waitcnt lgkmcnt(4)\n\t"
    "v_mfma_f32_16x16x32_bf16 v[8:11], a[248:251], a[144:147], v[8:11]\n\t"
    "v_mfma_f32_16x16x32_bf16 v[24:27], a[248:251], a[148:151], v[24:27]\n\t"
    "s_waitcnt vmcnt(8)\n\t"
    "ds_read_b128 a[160:163], v204 offset:8192\n\t"
    "ds_read_b128 a[164:167], v204 offset:10240\n\t"
    "ds_read_b128 a[168:171], v204 offset:12288\n\t"
    "ds_read_b128 a[172:175], v204 offset:14336\n\t"
    "ds_read_b128 a[176:179], v205 offset:8192\n\t"
    "ds_read_b128 a[180:183], v205 offset:10240\n\t"
    "ds_read_b128 a[184:187], v205 offset:12288\n\t"
    "ds_read_b128 a[188:191], v205 offset:14336\n\t"
    "v_mfma_f32_16x16x32_bf16 v[40:43], a[248:251], a[152:155], v[40:43]\n\t"
    "ds_read_b128 a[240:243], v193 offset:1024\n\t"
    "v_mfma_f32_16x16x32_bf16 v[56:59], a[248:251], a[156:159], v[56:59]\n\t"
    "v_mfma_f32_16x16x32_bf16 v[60:63], a[252:255], a[156:159], v[60:63]\n\t"
    "v_mfma_f32_16x16x32_bf16 v[44:47], a[252:255], a[152:155], v[44:47]\n\t"
    "v_mfma_f32_16x16x32_bf16 v[28:31], a[252:255], a[148:151], v[28:31]\n\t"
    "ds_read_b128 a[244:247], v193 offset:7168\n\t"
    "v_mfma_f32_16x16x32_bf16 v[12:15], a[252:255], a[144:147], v[12:15]\n\t"
    "s_waitcnt lgkmcnt(12)\n\t"
    "s_waitcnt lgkmcnt(2)\n\t"
    "v_mfma_f32_16x16x32_bf16 v[0:3], a[224:227], a[160:163], v[0:3]\n\t"
    "v_mfma_f32_16x16x32_bf16 v[16:19], a[224:227], a[164:167], v[16:19]\n\t"
    "s_add_u32 m0, s56, 24576\n\t"
    "s_nop 0\n\t"
    "buffer_load_dwordx4 v200, s[36:39], s52 offen offset:0 lds\n\t"
    "v_mfma_f32_16x16x32_bf16 v[32:35], a[224:227], a[168:171], v[32:35]\n\t"
    "ds_read_b128 a[248:251], v193 offset:13312\n\t"
    "v_mfma_f32_16x16x32_bf16 v[48:51], a[224:227], a[172:175], v[48:51]\n\t"
    "v_mfma_f32_16x16x32_bf16 v[52:55], a[228:231], a[172:175], v[52:55]\n\t"
    "v_mfma_f32_16x16x32_bf16 v[36:39], a[228:231], a[168:171], v[36:39]\n\t"
    "s_add_u32 m0, s56, 24576\n\t"
    "s_nop 0\n\t"
    "buffer_load_dwordx4 v201, s[36:39], s52 offen offset:3072 lds\n\t"
    "v_mfma_f32_16x16x32_bf16 v[20:23], a[228:231], a[164:167], v[20:23]\n\t"
    "ds_read_b128 a[252:255], v193 offset:19456\n\t"
    "v_mfma_f32_16x16x32_bf16 v[4:7], a[228:231], a[160:163], v[4:7]\n\t"
    "v_mfma_f32_16x16x32_bf16 v[8:11], a[232:235], a[160:163], v[8:11]\n\t"
    "v_mfma_f32_16x16x32_bf16 v[24:27], a[232:235], a[164:167], v[24:27]\n\t"
    "s_add_u32 m0, s56, 25472\n\t"
    "s_nop 0\n\t"
    "buffer_load_dwordx4 v200, s[36:39], s52 offen offset:128 lds\n\t"
    "v_mfma_f32_16x16x32_bf16 v[40:43], a[232:235], a[168:171], v[40:43]\n\t"
    "ds_read_b128 a[224:227], v192 offset:2048\n\t"
    "v_mfma_f32_16x16x32_bf16 v[56:59], a[232:235], a[172:175], v[56:59]\n\t"
    "v_mfma_f32_16x16x32_bf16 v[60:63], a[236:239], a[172:175], v[60:63]\n\t"
    "v_mfma_f32_16x16x32_bf16 v[44:47], a[236:239], a[168:171], v[44:47]\n\t"
    "s_add_u32 m0, s56, 25472\n\t"
    "s_nop 0\n\t"
    "buffer_load_dwordx4 v201, s[36:39], s52 offen offset:3200 lds\n\t"
    "v_mfma_f32_16x16x32_bf16 v[28:31], a[236:239], a[164:167], v[28:31]\n\t"
    "ds_read_b128 a[228:231], v192 offset:8192\n\t"
    "v_mfma_f32_16x16x32_bf16 v[12:15], a[236:239], a[160:163], v[12:15]\n\t"
    "s_waitcnt lgkmcnt(4)\n\t"
    "v_mfma_f32_16x16x32_bf16 v[0:3], a[240:243], a[176:179], v[0:3]\n\t"
    "v_mfma_f32_16x16x32_bf16 v[16:19], a[240:243], a[180:183], v[16:19]\n\t"
    "s_add_u32 m0, s56, 26368\n\t"
    "s_nop 0\n\t"
    "buffer_load_dwordx4 v200, s[36:39], s52 offen offset:256 lds\n\t"
    "v_mfma_f32_16x16x32_bf16 v[32:35], a[240:243], a[184:187], v[32:35]\n\t"
    "ds_read_b128 a[232:235], v192 offset:14336\n\t"
    "v_mfma_f32_16x16x32_bf16 v[48:51], a[240:243], a[188:191], v[48:51]\n\t"
    "v_mfma_f32_16x16x32_bf16 v[52:55], a[244:247], a[188:191], v[52:55]\n\t"
    "v_mfma_f32_16x16x32_bf16 v[36:39], a[244:247], a[184:187], v[36:39]\n\t"
    "s_add_u32 m0, s56, 26368\n\t"
    "s_nop 0\n\t"
    "buffer_load_dwordx4 v201, s[36:39], s52 offen offset:3328 lds\n\t"
    "v_mfma_f32_16x16x32_bf16 v[20:23], a[244:247], a[180:183], v[20:23]\n\t"
    "ds_read_b128 a[236:239], v192 offset:20480\n\t"
    "v_mfma_f32_16x16x32_bf16 v[4:7], a[244:247], a[176:179], v[4:7]\n\t"
    "s_waitcnt lgkmcnt(4)\n\t"
    "v_mfma_f32_16x16x32_bf16 v[8:11], a[248:251], a[176:179], v[8:11]\n\t"
    "v_mfma_f32_16x16x32_bf16 v[24:27], a[248:251], a[180:183], v[24:27]\n\t"
    "s_add_u32 s52, s52, 24576\n\t"
    "v_mfma_f32_16x16x32_bf16 v[40:43], a[248:251], a[184:187], v[40:43]\n\t"
    "ds_read_b128 a[240:243], v193 offset:2048\n\t"
    "v_mfma_f32_16x16x32_bf16 v[56:59], a[248:251], a[188:191], v[56:59]\n\t"
    "v_mfma_f32_16x16x32_bf16 v[60:63], a[252:255], a[188:191], v[60:63]\n\t"
    "v_mfma_f32_16x16x32_bf16 v[44:47], a[252:255], a[184:187], v[44:47]\n\t"
    "s_add_u32 m0, s57, 0\n\t"
    "s_nop 0\n\t"
    "buffer_load_dwordx4 v202, s[40:43], s53 offen offset:0 lds\n\t"
    "v_mfma_f32_16x16x32_bf16 v[28:31], a[252:255], a[180:183], v[28:31]\n\t"
    "ds_read_b128 a[244:247], v193 offset:8192\n\t"
    "v_mfma_f32_16x16x32_bf16 v[12:15], a[252:255], a[176:179], v[12:15]\n\t"
    "s_waitcnt lgkmcnt(4) vmcnt(7)\n\t"
    "v_mfma_f32_16x16x32_bf16 v[0:3], a[224:227], a[192:195], v[0:3]\n\t"
    "v_mfma_f32_16x16x32_bf16 v[16:19], a[224:227], a[196:199], v[16:19]\n\t"
    "s_add_u32 m0, s57, 0\n\t"
    "s_nop 0\n\t"
    "buffer_load_dwordx4 v202, s[40:43], s53 offen offset:3072 lds\n\t"
    "v_mfma_f32_16x16x32_bf16 v[32:35], a[224:227], a[200:203], v[32:35]\n\t"
    "ds_read_b128 a[248:251], v193 offset:14336\n\t"
    "v_mfma_f32_16x16x32_bf16 v[48:51], a[224:227], a[204:207], v[48:51]\n\t"
    "v_mfma_f32_16x16x32_bf16 v[52:55], a[228:231], a[204:207], v[52:55]\n\t"
    "v_mfma_f32_16x16x32_bf16 v[36:39], a[228:231], a[200:203], v[36:39]\n\t"
    "s_add_u32 m0, s57, 896\n\t"
    "s_nop 0\n\t"
    "buffer_load_dwordx4 v202, s[40:43], s53 offen offset:128 lds\n\t"
    "v_mfma_f32_16x16x32_bf16 v[20:23], a[228:231], a[196:199], v[20:23]\n\t"
    "ds_read_b128 a[252:255], v193 offset:20480\n\t"
    "v_mfma_f32_16x16x32_bf16 v[4:7], a[228:231], a[192:195], v[4:7]\n\t"
    "s_waitcnt lgkmcnt(4)\n\t"
    "v_mfma_f32_16x16x32_bf16 v[8:11], a[232:235], a[192:195], v[8:11]\n\t"
    "v_mfma_f32_16x16x32_bf16 v[24:27], a[232:235], a[196:199], v[24:27]\n\t"
    "s_add_u32 m0, s57, 896\n\t"
    "s_nop 0\n\t"
    "buffer_load_dwordx4 v202, s[40:43], s53 offen offset:3200 lds\n\t"
    "v_mfma_f32_16x16x32_bf16 v[40:43], a[232:235], a[200:203], v[40:43]\n\t"
    "v_mfma_f32_16x16x32_bf16 v[56:59], a[232:235], a[204:207], v[56:59]\n\t"
    "v_mfma_f32_16x16x32_bf16 v[60:63], a[236:239], a[204:207], v[60:63]\n\t"
    "v_mfma_f32_16x16x32_bf16 v[44:47], a[236:239], a[200:203], v[44:47]\n\t"
    "s_add_u32 m0, s57, 1792\n\t"
    "s_nop 0\n\t"
    "buffer_load_dwordx4 v202, s[40:43], s53 offen offset:256 lds\n\t"
    "v_mfma_f32_16x16x32_bf16 v[28:31], a[236:239], a[196:199], v[28:31]\n\t"
    "s_waitcnt lgkmcnt(0)\n\t"
    "v_mfma_f32_16x16x32_bf16 v[12:15], a[236:239], a[192:195], v[12:15]\n\t"
    "v_mfma_f32_16x16x32_bf16 v[0:3], a[240:243], a[208:211], v[0:3]\n\t"
    "s_barrier\n\t"
    "v_mfma_f32_16x16x32_bf16 v[16:19], a[240:243], a[212:215], v[16:19]\n\t"
    "s_add_u32 m0, s57, 1792\n\t"
    "s_nop 0\n\t"
    "buffer_load_dwordx4 v202, s[40:43], s53 offen offset:3328 lds\n\t"
    "v_mfma_f32_16x16x32_bf16 v[32:35], a[240:243], a[216:219], v[32:35]\n\t"
    "v_mfma_f32_16x16x32_bf16 v[48:51], a[240:243], a[220:223], v[48:51]\n\t"
    "v_mfma_f32_16x16x32_bf16 v[52:55], a[244:247], a[220:223], v[52:55]\n\t"
    "v_mfma_f32_16x16x32_bf16 v[36:39], a[244:247], a[216:219], v[36:39]\n\t"
    "s_add_u32 s53, s53, 24576\n\t"
    "v_mfma_f32_16x16x32_bf16 v[20:23], a[244:247], a[212:215], v[20:23]\n\t"
    "v_mfma_f32_16x16x32_bf16 v[4:7], a[244:247], a[208:211], v[4:7]\n\t"
    "v_mfma_f32_16x16x32_bf16 v[8:11], a[248:251], a[208:211], v[8:11]\n\t"
    "v_mfma_f32_16x16x32_bf16 v[24:27], a[248:251], a[212:215], v[24:27]\n\t"
    "v_mfma_f32_16x16x32_bf16 v[40:43], a[248:251], a[216:219], v[40:43]\n\t"
    "v_mfma_f32_16x16x32_bf16 v[56:59], a[248:251], a[220:223], v[56:59]\n\t"
    "v_mfma_f32_16x16x32_bf16 v[60:63], a[252:255], a[220:223], v[60:63]\n\t"
    "v_mfma_f32_16x16x32_bf16 v[44:47], a[252:255], a[216:219], v[44:47]\n\t"
    "v_mfma_f32_16x16x32_bf16 v[28:31], a[252:255], a[212:215], v[28:31]\n\t"
    "v_mfma_f32_16x16x32_bf16 v[12:15], a[252:255], a[208:211], v[12:15]\n\t"
    "s_nop 7\n\t"
    "s_nop 3\n\t"
    "s_add_u32 m0, s56, 0\n\t"
    "s_nop 0\n\t"
    "buffer_load_dwordx4 v200, s[36:39], s52 offen offset:0 lds\n\t"
    "v_max3_f32 v220, v0, v1, v2\n\t"
    "v_max3_f32 v221, v16, v17, v18\n\t"
    "v_max3_f32 v222, v32, v33, v34\n\t"
    "v_max3_f32 v223, v48, v49, v50\n\t"
    "v_max3_f32 v220, v220, v3, v4\n\t"
    "v_max3_f32 v221, v221, v19, v20\n\t"
    "v_max3_f32 v222, v222, v35, v36\n\t"
    "v_max3_f32 v223, v223, v51, v52\n\t"
    "v_max3_f32 v220, v220, v5, v6\n\t"
    "v_max3_f32 v221, v221, v21, v22\n\t"
    "v_max3_f32 v222, v222, v37, v38\n\t"
    "v_max3_f32 v223, v223, v53, v54\n\t"
    "v_max3_f32 v220, v220, v7, v8\n\t"
    "v_max3_f32 v221, v221, v23, v24\n\t"
    "v_max3_f32 v222, v222, v39, v40\n\t"
    "v_max3_f32 v223, v223, v55, v56\n\t"
    "v_max3_f32 v220, v220, v9, v10\n\t"
    "v_max3_f32 v221, v221, v25, v26\n\t"
    "v_max3_f32 v222, v222, v41, v42\n\t"
    "s_add_u32 m0, s56, 0\n\t"
    "v_max3_f32 v223, v223, v57, v58\n\t"
    "buffer_load_dwordx4 v201, s[36:39], s52 offen offset:3072 lds\n\t"
    "v_max3_f32 v220, v220, v11, v12\n\t"
    "v_max3_f32 v221, v221, v27, v28\n\t"
    "v_max3_f32 v222, v222, v43, v44\n\t"
    "v_max3_f32 v223, v223, v59, v60\n\t"
    "v_max3_f32 v220, v220, v13, v14\n\t"
    "v_max3_f32 v221, v221, v29, v30\n\t"
    "v_max3_f32 v222, v222, v45, v46\n\t"
    "v_max3_f32 v223, v223, v61, v62\n\t"
    "v_max_f32 v220, v220, v15\n\t"
    "v_max_f32 v221, v221, v31\n\t"
    "v_max_f32 v222, v222, v47\n\t"
    "v_max_f32 v223, v223, v63\n\t"
    "v_mov_b32 v252, v220\n\t"
    "v_mov_b32 v253, v221\n\t"
    "v_mov_b32 v254, v222\n\t"
    "v_mov_b32 v255, v223\n\t"
    "v_permlane32_swap_b32 v220, v252\n\t"
    "v_permlane32_swap_b32 v221, v253\n\t"
    "v_permlane32_swap_b32 v222, v254\n\t"
    "v_permlane32_swap_b32 v223, v255\n\t"
    "s_add_u32 m0, s56, 896\n\t"
    "v_max_f32 v220, v220, v252\n\t"
    "buffer_load_dwordx4 v200, s[36:39], s52 offen offset:128 lds\n\t"
    "v_max_f32 v221, v221, v253\n\t"
    "v_max_f32 v222, v222, v254\n\t"
    "v_max_f32 v223, v223, v255\n\t"
    "v_mov_b32 v252, v220\n\t"
    "v_mov_b32 v253, v221\n\t"
    "v_mov_b32 v254, v222\n\t"
    "v_mov_b32 v255, v223\n\t"
    "v_permlane16_swap_b32 v220, v252\n\t"
    "v_permlane16_swap_b32 v221, v253\n\t"
    "v_permlane16_swap_b32 v222, v254\n\t"
    "v_permlane16_swap_b32 v223, v255\n\t"
    "v_max_f32 v220, v220, v252\n\t"
    "v_max_f32 v221, v221, v253\n\t"
    "v_max_f32 v222, v222, v254\n\t"
    "v_max_f32 v223, v223, v255\n\t"
    "v_add_f32 v216, s63, v220\n\t"
    "v_add_f32 v217, s63, v221\n\t"
    "v_add_f32 v218, s63, v222\n\t"
    "v_add_f32 v219, s63, v223\n\t"
    "v_mul_f32 v212, s62, v216\n\t"
    "v_mul_f32 v213, s62, v217\n\t"
    "v_mul_f32 v214, s62, v218\n\t"
    "s_add_u32 m0, s56, 896\n\t"
    "v_mul_f32 v215, s62, v219\n\t"
    "buffer_load_dwordx4 v201, s[36:39], s52 offen offset:3200 lds\n\t"
    "v_sub_f32 v212, 0x41000000, v212\n\t"
    "v_sub_f32 v213, 0x41000000, v213\n\t"
    "v_sub_f32 v214, 0x41000000, v214\n\t"
    "v_sub_f32 v215, 0x41000000, v215\n\t"
    "v_fma_f32 v0, v0, s62, v212\n\t"
    "v_fma_f32 v1, v1, s62, v212\n\t"
    "v_fma_f32 v2, v2, s62, v212\n\t"
    "v_exp_f32 v0, v0\n\t"
    "v_fma_f32 v3, v3, s62, v212\n\t"
    "v_exp_f32 v1, v1\n\t"
    "s_nop 0\n\t"
    "v_cvt_pk_bf16_f32 v0, v0, v1\n\t"
    "v_exp_f32 v2, v2\n\t"
    "v_fma_f32 v4, v4, s62, v212\n\t"
    "v_exp_f32 v3, v3\n\t"
    "s_add_u32 m0, s56, 1792\n\t"
    "v_cvt_pk_bf16_f32 v1, v2, v3\n\t"
    "buffer_load_dwordx4 v200, s[36:39], s52 offen offset:256 lds\n\t"
    "v_fma_f32 v5, v5, s62, v212\n\t"
    "v_fma_f32 v6, v6, s62, v212\n\t"
    "v_exp_f32 v4, v4\n\t"
    "v_fma_f32 v7, v7, s62, v212\n\t"
    "v_exp_f32 v5, v5\n\t"
    "s_nop 0\n\t"
    "v_cvt_pk_bf16_f32 v2, v4, v5\n\t"
    "v_exp_f32 v6, v6\n\t"
    "v_fma_f32 v16, v16, s62, v213\n\t"
    "v_exp_f32 v7, v7\n\t"
    "s_nop 0\n\t"
    "v_cvt_pk_bf16_f32 v3, v6, v7\n\t"
    "v_exp_f32 v16, v16\n\t"
    "s_add_u32 m0, s56, 1792\n\t"
    "v_fma_f32 v17, v17, s62, v213\n\t"
    "buffer_load_dwordx4 v201, s[36:39], s52 offen offset:3328 lds\n\t"
    "v_fma_f32 v18, v18, s62, v213\n\t"
    "v_fma_f32 v19, v19, s62, v213\n\t"
    "v_exp_f32 v17, v17\n\t"
    "s_nop 0\n\t"
    "v_cvt_pk_bf16_f32 v16, v16, v17\n\t"
    "v_exp_f32 v18, v18\n\t"
    "v_fma_f32 v20, v20, s62, v213\n\t"
    "v_exp_f32 v19, v19\n\t"
    "s_nop 0\n\t"
    "v_cvt_pk_bf16_f32 v17, v18, v19\n\t"
    "v_exp_f32 v20, v20\n\t"
    "v_fma_f32 v21, v21, s62, v213\n\t"
    "v_fma_f32 v22, v22, s62, v213\n\t"
    "v_exp_f32 v21, v21\n\t"
    "s_add_u32 s52, s52, 24576\n\t"
    "v_exp_f32 v22, v22\n\t"
    "v_cvt_pk_bf16_f32 v18, v20, v21\n\t"
    "v_fma_f32 v23, v23, s62, v213\n\t"
    "v_exp_f32 v23, v23\n\t"
    "s_nop 0\n\t"
    "v_cvt_pk_bf16_f32 v19, v22, v23\n\t"
    "v_fma_f32 v32, v32, s62, v214\n\t"
    "s_waitcnt vmcnt(12)\n\t"
    "v_exp_f32 v32, v32\n\t"
    "v_fma_f32 v33, v33, s62, v214\n\t"
    "v_fma_f32 v34, v34, s62, v214\n\t"
    "s_barrier\n\t"
    "ds_read_b128 a[224:227], v192 offset:24576\n\t"
    "v_fma_f32 v35, v35, s62, v214\n\t"
    "ds_read_b128 a[228:231], v192 offset:30720\n\t"
    "v_fma_f32 v36, v36, s62, v214\n\t"
    "ds_read_b128 a[232:235], v192 offset:36864\n\t"
    "v_fma_f32 v37, v37, s62, v214\n\t"
    "v_exp_f32 v33, v33\n\t"
    "v_exp_f32 v34, v34\n\t"
    "v_exp_f32 v35, v35\n\t"
    "ds_read_b128 a[236:239], v192 offset:43008\n\t"
    "v_cvt_pk_bf16_f32 v32, v32, v33\n\t"
    "v_exp_f32 v36, v36\n\t"
    "v_exp_f32 v37, v37\n\t"
    "v_cvt_pk_bf16_f32 v33, v34, v35\n\t"
    "v_cvt_pk_bf16_f32 v34, v36, v37\n\t"
    "ds_read_b128 a[240:243], v193 offset:24576\n\t"
    "v_fma_f32 v38, v38, s62, v214\n\t"
    "v_exp_f32 v38, v38\n\t"
    "v_fma_f32 v39, v39, s62, v214\n\t"
    "v_fma_f32 v48, v48, s62, v215\n\t"
    "v_exp_f32 v39, v39\n\t"
    "ds_read_b128 a[244:247], v193 offset:30720\n\t"
    "v_cvt_pk_bf16_f32 v35, v38, v39\n\t"
    "s_waitcnt lgkmcnt(4)\n\t"
    "v_exp_f32 v48, v48\n\t"
    "LOOP:\n\t"
    "v_mfma_f32_16x16x32_bf16 v[64:67], a[224:227], a[128:131], v[224:227]\n\t"
    "v_fma_f32 v49, v49, s62, v215\n\t"
    "v_fma_f32 v50, v50, s62, v215\n\t"
    "v_mfma_f32_16x16x32_bf16 v[80:83], a[224:227], a[132:135], v[224:227]\n\t"
    "v_exp_f32 v49, v49\n\t"
    "v_mfma_f32_16x16x32_bf16 v[96:99], a[224:227], a[136:139], v[224:227]\n\t"
    "s_add_u32 m0, s57, 24576\n\t"
    "ds_read_b128 a[248:251], v193 offset:36864\n\t"
    "v_cvt_pk_bf16_f32 v48, v48, v49\n\t"
    "v_mfma_f32_16x16x32_bf16 v[112:115], a[224:227], a[140:143], v[224:227]\n\t"
    "buffer_load_dwordx4 v202, s[40:43], s53 offen offset:0 lds\n\t"
    "v_fma_f32 v51, v51, s62, v215\n\t"
    "v_mfma_f32_16x16x32_bf16 v[116:119], a[228:231], a[140:143], v[224:227]\n\t"
    "ds_read_b128 v[248:251], v203 offset:0\n\t"
    "v_fma_f32 v52, v52, s62, v215\n\t"
    "v_mfma_f32_16x16x32_bf16 v[100:103], a[228:231], a[136:139], v[224:227]\n\t"
    "v_exp_f32 v50, v50\n\t"
    "v_mfma_f32_16x16x32_bf16 v[84:87], a[228:231], a[132:135], v[224:227]\n\t"
    "ds_read_b128 a[252:255], v193 offset:43008\n\t"
    "v_fma_f32 v53, v53, s62, v215\n\t"
    "v_mfma_f32_16x16x32_bf16 v[68:71], a[228:231], a[128:131], v[224:227]\n\t"
    "s_waitcnt lgkmcnt(5)\n\t"
    "v_exp_f32 v51, v51\n\t"
    "v_mfma_f32_16x16x32_bf16 v[72:75], a[232:235], a[128:131], v[224:227]\n\t"
    "s_waitcnt lgkmcnt(1)\n\t"
    "buffer_store_dwordx4 v[248:251], v200, s[48:51], s70 offen offset:128 nt\n\t"
    "v_mfma_f32_16x16x32_bf16 v[88:91], a[232:235], a[132:135], v[224:227]\n\t"
    "v_exp_f32 v52, v52\n\t"
    "v_mfma_f32_16x16x32_bf16 v[104:107], a[232:235], a[136:139], v[224:227]\n\t"
    "ds_read_b128 a[224:227], v192 offset:25600\n\t"
    "v_cvt_pk_bf16_f32 v49, v50, v51\n\t"
    "v_mfma_f32_16x16x32_bf16 v[120:123], a[232:235], a[140:143], v[224:227]\n\t"
    "v_exp_f32 v53, v53\n\t"
    "v_mfma_f32_16x16x32_bf16 v[124:127], a[236:239], a[140:143], v[224:227]\n\t"
    "v_cvt_pk_bf16_f32 v50, v52, v53\n\t"
    "v_fma_f32 v54, v54, s62, v215\n\t"
    "v_mfma_f32_16x16x32_bf16 v[108:111], a[236:239], a[136:139], v[224:227]\n\t"
    "v_exp_f32 v54, v54\n\t"
    "v_mfma_f32_16x16x32_bf16 v[92:95], a[236:239], a[132:135], v[224:227]\n\t"
    "s_add_u32 m0, s57, 24576\n\t"
    "ds_read_b128 a[228:231], v192 offset:31744\n\t"
    "v_fma_f32 v55, v55, s62, v215\n\t"
    "v_mfma_f32_16x16x32_bf16 v[76:79], a[236:239], a[128:131], v[224:227]\n\t"
    "buffer_load_dwordx4 v202, s[40:43], s53 offen offset:3072 lds\n\t"
    "v_fma_f32 v8, v8, s62, v212\n\t"
    "v_mfma_f32_16x16x32_bf16 v[64:67], a[240:243], a[144:147], v[64:67]\n\t"
    "v_exp_f32 v55, v55\n\t"
    "v_mfma_f32_16x16x32_bf16 v[80:83], a[240:243], a[148:151], v[80:83]\n\t"
    "v_exp_f32 v8, v8\n\t"
    "v_mfma_f32_16x16x32_bf16 v[96:99], a[240:243], a[152:155], v[96:99]\n\t"
    "ds_read_b128 a[232:235], v192 offset:37888\n\t"
    "v_cvt_pk_bf16_f32 v51, v54, v55\n\t"
    "v_mfma_f32_16x16x32_bf16 v[112:115], a[240:243], a[156:159], v[112:115]\n\t"
    "v_fma_f32 v9, v9, s62, v212\n\t"
    "v_fma_f32 v10, v10, s62, v212\n\t"
    "v_mfma_f32_16x16x32_bf16 v[116:119], a[244:247], a[156:159], v[116:119]\n\t"
    "v_exp_f32 v9, v9\n\t"
    "v_mfma_f32_16x16x32_bf16 v[100:103], a[244:247], a[152:155], v[100:103]\n\t"
    "v_exp_f32 v10, v10\n\t"
    "v_mfma_f32_16x16x32_bf16 v[84:87], a[244:247], a[148:151], v[84:87]\n\t"
    "ds_read_b128 a[236:239], v192 offset:44032\n\t"
    "v_cvt_pk_bf16_f32 v8, v8, v9\n\t"
    "v_mfma_f32_16x16x32_bf16 v[68:71], a[244:247], a[144:147], v[68:71]\n\t"
    "s_waitcnt lgkmcnt(4)\n\t"
    "v_fma_f32 v11, v11, s62, v212\n\t"
    "v_fma_f32 v12, v12, s62, v212\n\t"
    "v_mfma_f32_16x16x32_bf16 v[72:75], a[248:251], a[144:147], v[72:75]\n\t"
    "v_exp_f32 v11, v11\n\t"
    "v_mfma_f32_16x16x32_bf16 v[88:91], a[248:251], a[148:151], v[88:91]\n\t"
    "v_exp_f32 v12, v12\n\t"
    "v_mfma_f32_16x16x32_bf16 v[104:107], a[248:251], a[152:155], v[104:107]\n\t"
    "s_add_u32 m0, s57, 25472\n\t"
    "ds_read_b128 a[240:243], v193 offset:25600\n\t"
    "v_cvt_pk_bf16_f32 v9, v10, v11\n\t"
    "v_mfma_f32_16x16x32_bf16 v[120:123], a[248:251], a[156:159], v[120:123]\n\t"
    "buffer_load_dwordx4 v202, s[40:43], s53 offen offset:128 lds\n\t"
    "v_fma_f32 v13, v13, s62, v212\n\t"
    "v_mfma_f32_16x16x32_bf16 v[124:127], a[252:255], a[156:159], v[124:127]\n\t"
    "v_exp_f32 v13, v13\n\t"
    "v_mfma_f32_16x16x32_bf16 v[108:111], a[252:255], a[152:155], v[108:111]\n\t"
    "v_cvt_pk_bf16_f32 v10, v12, v13\n\t"
    "v_fma_f32 v14, v14, s62, v212\n\t"
    "v_mfma_f32_16x16x32_bf16 v[92:95], a[252:255], a[148:151], v[92:95]\n\t"
    "ds_read_b128 a[244:247], v193 offset:31744\n\t"
    "v_fma_f32 v15, v15, s62, v212\n\t"
    "v_mfma_f32_16x16x32_bf16 v[76:79], a[252:255], a[144:147], v[76:79]\n\t"
    "s_waitcnt lgkmcnt(4)\n\t"
    "v_exp_f32 v14, v14\n\t"
    "v_mfma_f32_16x16x32_bf16 v[64:67], a[224:227], a[160:163], v[64:67]\n\t"
    "v_exp_f32 v15, v15\n\t"
    "v_mfma_f32_16x16x32_bf16 v[80:83], a[224:227], a[164:167], v[80:83]\n\t"
    "v_cvt_pk_bf16_f32 v11, v14, v15\n\t"
    "v_fma_f32 v24, v24, s62, v213\n\t"
    "v_mfma_f32_16x16x32_bf16 v[96:99], a[224:227], a[168:171], v[96:99]\n\t"
    "ds_read_b128 a[248:251], v193 offset:37888\n\t"
    "v_fma_f32 v25, v25, s62, v213\n\t"
    "v_mfma_f32_16x16x32_bf16 v[112:115], a[224:227], a[172:175], v[112:115]\n\t"
    "v_exp_f32 v24, v24\n\t"
    "v_mfma_f32_16x16x32_bf16 v[116:119], a[228:231], a[172:175], v[116:119]\n\t"
    "v_exp_f32 v25, v25\n\t"
    "v_mfma_f32_16x16x32_bf16 v[100:103], a[228:231], a[168:171], v[100:103]\n\t"
    "v_cvt_pk_bf16_f32 v24, v24, v25\n\t"
    "v_fma_f32 v26, v26, s62, v213\n\t"
    "v_mfma_f32_16x16x32_bf16 v[84:87], a[228:231], a[164:167], v[84:87]\n\t"
    "s_add_u32 m0, s57, 25472\n\t"
    "ds_read_b128 a[252:255], v193 offset:44032\n\t"
    "v_fma_f32 v27, v27, s62, v213\n\t"
    "v_mfma_f32_16x16x32_bf16 v[68:71], a[228:231], a[160:163], v[68:71]\n\t"
    "s_waitcnt lgkmcnt(4)\n\t"
    "buffer_load_dwordx4 v202, s[40:43], s53 offen offset:3200 lds\n\t"
    "v_fma_f32 v28, v28, s62, v213\n\t"
    "v_mfma_f32_16x16x32_bf16 v[72:75], a[232:235], a[160:163], v[72:75]\n\t"
    "ds_read_b128 v[248:251], v203 offset:1024\n\t"
    "v_fma_f32 v29, v29, s62, v213\n\t"
    "v_mfma_f32_16x16x32_bf16 v[88:91], a[232:235], a[164:167], v[88:91]\n\t"
    "v_exp_f32 v26, v26\n\t"
    "v_mfma_f32_16x16x32_bf16 v[104:107], a[232:235], a[168:171], v[104:107]\n\t"
    "ds_read_b128 a[224:227], v192 offset:26624\n\t"
    "v_fma_f32 v30, v30, s62, v213\n\t"
    "v_mfma_f32_16x16x32_bf16 v[120:123], a[232:235], a[172:175], v[120:123]\n\t"
    "v_exp_f32 v27, v27\n\t"
    "v_mfma_f32_16x16x32_bf16 v[124:127], a[236:239], a[172:175], v[124:127]\n\t"
    "s_waitcnt lgkmcnt(1)\n\t"
    "buffer_store_dwordx4 v[248:251], v201, s[48:51], s70 offen offset:3200 nt\n\t"
    "v_mfma_f32_16x16x32_bf16 v[108:111], a[236:239], a[168:171], v[108:111]\n\t"
    "v_exp_f32 v28, v28\n\t"
    "v_mfma_f32_16x16x32_bf16 v[92:95], a[236:239], a[164:167], v[92:95]\n\t"
    "ds_read_b128 a[228:231], v192 offset:32768\n\t"
    "v_cvt_pk_bf16_f32 v25, v26, v27\n\t"
    "v_mfma_f32_16x16x32_bf16 v[76:79], a[236:239], a[160:163], v[76:79]\n\t"
    "v_exp_f32 v29, v29\n\t"
    "v_mfma_f32_16x16x32_bf16 v[64:67], a[240:243], a[176:179], v[64:67]\n\t"
    "v_exp_f32 v30, v30\n\t"
    "v_mfma_f32_16x16x32_bf16 v[80:83], a[240:243], a[180:183], v[80:83]\n\t"
    "v_cvt_pk_bf16_f32 v26, v28, v29\n\t"
    "v_fma_f32 v31, v31, s62, v213\n\t"
    "v_mfma_f32_16x16x32_bf16 v[96:99], a[240:243], a[184:187], v[96:99]\n\t"
    "s_add_u32 m0, s57, 26368\n\t"
    "ds_read_b128 a[232:235], v192 offset:38912\n\t"
    "v_fma_f32 v40, v40, s62, v214\n\t"
    "v_mfma_f32_16x16x32_bf16 v[112:115], a[240:243], a[188:191], v[112:115]\n\t"
    "buffer_load_dwordx4 v202, s[40:43], s53 offen offset:256 lds\n\t"
    "v_fma_f32 v41, v41, s62, v214\n\t"
    "v_mfma_f32_16x16x32_bf16 v[116:119], a[244:247], a[188:191], v[116:119]\n\t"
    "v_exp_f32 v31, v31\n\t"
    "v_mfma_f32_16x16x32_bf16 v[100:103], a[244:247], a[184:187], v[100:103]\n\t"
    "v_exp_f32 v40, v40\n\t"
    "v_mfma_f32_16x16x32_bf16 v[84:87], a[244:247], a[180:183], v[84:87]\n\t"
    "ds_read_b128 a[236:239], v192 offset:45056\n\t"
    "v_cvt_pk_bf16_f32 v27, v30, v31\n\t"
    "v_mfma_f32_16x16x32_bf16 v[68:71], a[244:247], a[176:179], v[68:71]\n\t"
    "v_exp_f32 v41, v41\n\t"
    "v_mfma_f32_16x16x32_bf16 v[72:75], a[248:251], a[176:179], v[72:75]\n\t"
    "v_cvt_pk_bf16_f32 v40, v40, v41\n\t"
    "v_fma_f32 v42, v42, s62, v214\n\t"
    "v_mfma_f32_16x16x32_bf16 v[88:91], a[248:251], a[180:183], v[88:91]\n\t"
    "v_exp_f32 v42, v42\n\t"
    "v_mfma_f32_16x16x32_bf16 v[104:107], a[248:251], a[184:187], v[104:107]\n\t"
    "ds_read_b128 a[240:243], v193 offset:26624\n\t"
    "v_fma_f32 v43, v43, s62, v214\n\t"
    "v_mfma_f32_16x16x32_bf16 v[120:123], a[248:251], a[188:191], v[120:123]\n\t"
    "v_exp_f32 v43, v43\n\t"
    "v_mfma_f32_16x16x32_bf16 v[124:127], a[252:255], a[188:191], v[124:127]\n\t"
    "v_cvt_pk_bf16_f32 v41, v42, v43\n\t"
    "v_fma_f32 v44, v44, s62, v214\n\t"
    "v_mfma_f32_16x16x32_bf16 v[108:111], a[252:255], a[184:187], v[108:111]\n\t"
    "v_exp_f32 v44, v44\n\t"
    "v_mfma_f32_16x16x32_bf16 v[92:95], a[252:255], a[180:183], v[92:95]\n\t"
    "s_add_u32 m0, s57, 26368\n\t"
    "ds_read_b128 a[244:247], v193 offset:32768\n\t"
    "v_fma_f32 v45, v45, s62, v214\n\t"
    "v_mfma_f32_16x16x32_bf16 v[76:79], a[252:255], a[176:179], v[76:79]\n\t"
    "s_waitcnt lgkmcnt(4)\n\t"
    "buffer_load_dwordx4 v202, s[40:43], s53 offen offset:3328 lds\n\t"
    "v_fma_f32 v46, v46, s62, v214\n\t"
    "v_mfma_f32_16x16x32_bf16 v[64:67], a[224:227], a[192:195], v[64:67]\n\t"
    "v_exp_f32 v45, v45\n\t"
    "v_mfma_f32_16x16x32_bf16 v[80:83], a[224:227], a[196:199], v[80:83]\n\t"
    "v_exp_f32 v46, v46\n\t"
    "v_mfma_f32_16x16x32_bf16 v[96:99], a[224:227], a[200:203], v[96:99]\n\t"
    "ds_read_b128 a[248:251], v193 offset:38912\n\t"
    "v_cvt_pk_bf16_f32 v42, v44, v45\n\t"
    "v_mfma_f32_16x16x32_bf16 v[112:115], a[224:227], a[204:207], v[112:115]\n\t"
    "v_fma_f32 v47, v47, s62, v214\n\t"
    "v_fma_f32 v56, v56, s62, v215\n\t"
    "v_mfma_f32_16x16x32_bf16 v[116:119], a[228:231], a[204:207], v[116:119]\n\t"
    "v_exp_f32 v47, v47\n\t"
    "v_mfma_f32_16x16x32_bf16 v[100:103], a[228:231], a[200:203], v[100:103]\n\t"
    "v_exp_f32 v56, v56\n\t"
    "v_mfma_f32_16x16x32_bf16 v[84:87], a[228:231], a[196:199], v[84:87]\n\t"
    "ds_read_b128 a[252:255], v193 offset:45056\n\t"
    "v_cvt_pk_bf16_f32 v43, v46, v47\n\t"
    "v_mfma_f32_16x16x32_bf16 v[68:71], a[228:231], a[192:195], v[68:71]\n\t"
    "s_waitcnt lgkmcnt(4)\n\t"
    "v_fma_f32 v57, v57, s62, v215\n\t"
    "v_fma_f32 v58, v58, s62, v215\n\t"
    "v_mfma_f32_16x16x32_bf16 v[72:75], a[232:235], a[192:195], v[72:75]\n\t"
    "v_exp_f32 v57, v57\n\t"
    "v_mfma_f32_16x16x32_bf16 v[88:91], a[232:235], a[196:199], v[88:91]\n\t"
    "v_exp_f32 v58, v58\n\t"
    "v_mfma_f32_16x16x32_bf16 v[104:107], a[232:235], a[200:203], v[104:107]\n\t"
    "s_add_u32 s53, s53, 24576\n\t"
    "v_cvt_pk_bf16_f32 v56, v56, v57\n\t"
    "v_fma_f32 v59, v59, s62, v215\n\t"
    "v_mfma_f32_16x16x32_bf16 v[120:123], a[232:235], a[204:207], v[120:123]\n\t"
    "v_exp_f32 v59, v59\n\t"
    "v_mfma_f32_16x16x32_bf16 v[124:127], a[236:239], a[204:207], v[124:127]\n\t"
    "ds_read_b128 v[248:251], v203 offset:2048\n\t"
    "v_cvt_pk_bf16_f32 v57, v58, v59\n\t"
    "v_mfma_f32_16x16x32_bf16 v[108:111], a[236:239], a[200:203], v[108:111]\n\t"
    "v_fma_f32 v60, v60, s62, v215\n\t"
    "v_fma_f32 v61, v61, s62, v215\n\t"
    "v_mfma_f32_16x16x32_bf16 v[92:95], a[236:239], a[196:199], v[92:95]\n\t"
    "s_waitcnt lgkmcnt(1) vmcnt(14)\n\t"
    "v_exp_f32 v60, v60\n\t"
    "v_mfma_f32_16x16x32_bf16 v[76:79], a[236:239], a[192:195], v[76:79]\n\t"
    "v_exp_f32 v61, v61\n\t"
    "v_mfma_f32_16x16x32_bf16 v[64:67], a[240:243], a[208:211], v[64:67]\n\t"
    "s_barrier\n\t"
    "ds_read_b64_tr_b16 a[224:225], v196 offset:0\n\t"
    "v_cvt_pk_bf16_f32 v58, v60, v61\n\t"
    "v_mfma_f32_16x16x32_bf16 v[80:83], a[240:243], a[212:215], v[80:83]\n\t"
    "s_waitcnt lgkmcnt(1)\n\t"
    "ds_read_b64_tr_b16 a[226:227], v196 offset:6144\n\t"
    "v_fma_f32 v62, v62, s62, v215\n\t"
    "v_mfma_f32_16x16x32_bf16 v[96:99], a[240:243], a[216:219], v[96:99]\n\t"
    "ds_read_b64_tr_b16 a[228:229], v197 offset:0\n\t"
    "v_fma_f32 v63, v63, s62, v215\n\t"
    "v_mfma_f32_16x16x32_bf16 v[112:115], a[240:243], a[220:223], v[112:115]\n\t"
    "ds_read_b64_tr_b16 a[230:231], v197 offset:6144\n\t"
    "v_mfma_f32_16x16x32_bf16 v[116:119], a[244:247], a[220:223], v[116:119]\n\t"
    "ds_read_b64_tr_b16 a[232:233], v198 offset:0\n\t"
    "v_mfma_f32_16x16x32_bf16 v[100:103], a[244:247], a[216:219], v[100:103]\n\t"
    "ds_read_b64_tr_b16 a[234:235], v198 offset:6144\n\t"
    "v_mfma_f32_16x16x32_bf16 v[84:87], a[244:247], a[212:215], v[84:87]\n\t"
    "ds_read_b64_tr_b16 a[236:237], v199 offset:0\n\t"
    "v_mfma_f32_16x16x32_bf16 v[68:71], a[244:247], a[208:211], v[68:71]\n\t"
    "ds_read_b64_tr_b16 a[238:239], v199 offset:6144\n\t"
    "v_mfma_f32_16x16x32_bf16 v[72:75], a[248:251], a[208:211], v[72:75]\n\t"
    "ds_read_b64_tr_b16 a[240:241], v196 offset:12288\n\t"
    "v_mfma_f32_16x16x32_bf16 v[88:91], a[248:251], a[212:215], v[88:91]\n\t"
    "buffer_store_dwordx4 v[248:251], v200, s[48:51], s78 offen offset:128 nt\n\t"
    "v_mfma_f32_16x16x32_bf16 v[104:107], a[248:251], a[216:219], v[104:107]\n\t"
    "ds_read_b64_tr_b16 a[242:243], v196 offset:18432\n\t"
    "v_mfma_f32_16x16x32_bf16 v[120:123], a[248:251], a[220:223], v[120:123]\n\t"
    "v_exp_f32 v62, v62\n\t"
    "v_mfma_f32_16x16x32_bf16 v[124:127], a[252:255], a[220:223], v[124:127]\n\t"
    "ds_read_b64_tr_b16 a[244:245], v197 offset:12288\n\t"
    "v_mfma_f32_16x16x32_bf16 v[108:111], a[252:255], a[216:219], v[108:111]\n\t"
    "v_exp_f32 v63, v63\n\t"
    "v_mfma_f32_16x16x32_bf16 v[92:95], a[252:255], a[212:215], v[92:95]\n\t"
    "ds_read_b64_tr_b16 a[246:247], v197 offset:18432\n\t"
    "v_cvt_pk_bf16_f32 v59, v62, v63\n\t"
    "v_mfma_f32_16x16x32_bf16 v[76:79], a[252:255], a[208:211], v[76:79]\n\t"
    "s_waitcnt lgkmcnt(8)\n\t"
    "v_mfma_f32_16x16x32_bf16 a[0:3], a[224:227], v[0:3], 0\n\t"
    "ds_read_b64_tr_b16 a[248:249], v198 offset:12288\n\t"
    "v_mfma_f32_16x16x32_bf16 a[4:7], a[224:227], v[16:19], 0\n\t"
    "v_mfma_f32_16x16x32_bf16 a[8:11], a[224:227], v[32:35], 0\n\t"
    "s_add_u32 m0, s56, 24576\n\t"
    "ds_read_b64_tr_b16 a[250:251], v198 offset:18432\n\t"
    "v_mfma_f32_16x16x32_bf16 a[12:15], a[224:227], v[48:51], 0\n\t"
    "buffer_load_dwordx4 v200, s[36:39], s52 offen offset:0 lds\n\t"
    "v_mfma_f32_16x16x32_bf16 a[28:31], a[228:231], v[48:51], 0\n\t"
    "ds_read_b64_tr_b16 a[252:253], v199 offset:12288\n\t"
    "v_max3_f32 v220, v64, v65, v66\n\t"
    "v_mfma_f32_16x16x32_bf16 a[24:27], a[228:231], v[32:35], 0\n\t"
    "v_max3_f32 v221, v80, v81, v82\n\t"
    "v_max3_f32 v222, v96, v97, v98\n\t"
    "v_mfma_f32_16x16x32_bf16 a[20:23], a[228:231], v[16:19], 0\n\t"
    "ds_read_b64_tr_b16 a[254:255], v199 offset:18432\n\t"
    "v_max3_f32 v223, v112, v113, v114\n\t"
    "v_mfma_f32_16x16x32_bf16 a[16:19], a[228:231], v[0:3], 0\n\t"
    "s_waitcnt lgkmcnt(8)\n\t"
    "v_max3_f32 v220, v220, v67, v68\n\t"
    "v_max3_f32 v221, v221, v83, v84\n\t"
    "v_mfma_f32_16x16x32_bf16 a[32:35], a[232:235], v[0:3], 0\n\t"
    "ds_read_b64_tr_b16 a[224:225], v196 offset:1024\n\t"
    "v_max3_f32 v222, v222, v99, v100\n\t"
    "v_mfma_f32_16x16x32_bf16 a[36:39], a[232:235], v[16:19], 0\n\t"
    "v_max3_f32 v223, v223, v115, v116\n\t"
    "v_max3_f32 v220, v220, v69, v70\n\t"
    "v_mfma_f32_16x16x32_bf16 v[228:231], v[244:247], v[0:3], 0\n\t"
    "v_max3_f32 v221, v221, v85, v86\n\t"
    "v_max3_f32 v222, v222, v101, v102\n\t"
    "v_mfma_f32_16x16x32_bf16 a[40:43], a[232:235], v[32:35], 0\n\t"
    "ds_read_b64_tr_b16 a[226:227], v196 offset:7168\n\t"
    "v_max3_f32 v223, v223, v117, v118\n\t"
    "v_mfma_f32_16x16x32_bf16 a[44:47], a[232:235], v[48:51], 0\n\t"
    "v_max3_f32 v220, v220, v71, v72\n\t"
    "v_max3_f32 v221, v221, v87, v88\n\t"
    "v_mfma_f32_16x16x32_bf16 a[60:63], a[236:239], v[48:51], 0\n\t"
    "ds_read_b64_tr_b16 a[228:229], v197 offset:1024\n\t"
    "v_max3_f32 v222, v222, v103, v104\n\t"
    "v_mfma_f32_16x16x32_bf16 a[56:59], a[236:239], v[32:35], 0\n\t"
    "v_max3_f32 v223, v223, v119, v120\n\t"
    "v_max3_f32 v220, v220, v73, v74\n\t"
    "v_mfma_f32_16x16x32_bf16 v[232:235], v[244:247], v[16:19], 0\n\t"
    "v_max3_f32 v221, v221, v89, v90\n\t"
    "v_max3_f32 v222, v222, v105, v106\n\t"
    "v_mfma_f32_16x16x32_bf16 a[52:55], a[236:239], v[16:19], 0\n\t"
    "s_add_u32 m0, s56, 24576\n\t"
    "ds_read_b64_tr_b16 a[230:231], v197 offset:7168\n\t"
    "v_max3_f32 v223, v223, v121, v122\n\t"
    "v_mfma_f32_16x16x32_bf16 a[48:51], a[236:239], v[0:3], 0\n\t"
    "s_waitcnt lgkmcnt(8)\n\t"
    "buffer_load_dwordx4 v201, s[36:39], s52 offen offset:3072 lds\n\t"
    "v_max3_f32 v220, v220, v75, v76\n\t"
    "v_mfma_f32_16x16x32_bf16 a[0:3], a[240:243], v[8:11], a[0:3]\n\t"
    "ds_read_b64_tr_b16 a[232:233], v198 offset:1024\n\t"
    "v_max3_f32 v221, v221, v91, v92\n\t"
    "v_mfma_f32_16x16x32_bf16 a[4:7], a[240:243], v[24:27], a[4:7]\n\t"
    "ds_read_b128 v[248:251], v203 offset:3072\n\t"
    "v_max3_f32 v222, v222, v107, v108\n\t"
    "v_mfma_f32_16x16x32_bf16 v[236:239], v[244:247], v[32:35], 0\n\t"
    "v_max3_f32 v223, v223, v123, v124\n\t"
    "v_max3_f32 v220, v220, v77, v78\n\t"
    "v_mfma_f32_16x16x32_bf16 a[8:11], a[240:243], v[40:43], a[8:11]\n\t"
    "ds_read_b64_tr_b16 a[234:235], v198 offset:7168\n\t"
    "v_max3_f32 v221, v221, v93, v94\n\t"
    "v_mfma_f32_16x16x32_bf16 a[12:15], a[240:243], v[56:59], a[12:15]\n\t"
    "v_max3_f32 v222, v222, v109, v110\n\t"
    "v_max3_f32 v223, v223, v125, v126\n\t"
    "v_mfma_f32_16x16x32_bf16 a[28:31], a[244:247], v[56:59], a[28:31]\n\t"
    "s_waitcnt lgkmcnt(1)\n\t"
    "ds_read_b64_tr_b16 a[236:237], v199 offset:1024\n\t"
    "v_max_f32 v220, v220, v79\n\t"
    "v_mfma_f32_16x16x32_bf16 a[24:27], a[244:247], v[40:43], a[24:27]\n\t"
    "buffer_store_dwordx4 v[248:251], v201, s[48:51], s78 offen offset:3200 nt\n\t"
    "v_mfma_f32_16x16x32_bf16 v[240:243], v[244:247], v[48:51], 0\n\t"
    "v_max_f32 v221, v221, v95\n\t"
    "v_max_f32 v222, v222, v111\n\t"
    "v_mfma_f32_16x16x32_bf16 a[20:23], a[244:247], v[24:27], a[20:23]\n\t"
    "ds_read_b64_tr_b16 a[238:239], v199 offset:7168\n\t"
    "v_max_f32 v223, v223, v127\n\t"
    "v_mfma_f32_16x16x32_bf16 a[16:19], a[244:247], v[8:11], a[16:19]\n\t"
    "v_cmp_gt_f32 vcc, v220, v216\n\t"
    "v_cmp_gt_f32 s[88:89], v221, v217\n\t"
    "v_mfma_f32_16x16x32_bf16 a[32:35], a[248:251], v[8:11], a[32:35]\n\t"
    "s_or_b64 vcc, vcc, s[88:89]\n\t"
    "ds_read_b64_tr_b16 a[240:241], v196 offset:13312\n\t"
    "v_cmp_gt_f32 s[90:91], v222, v218\n\t"
    "v_mfma_f32_16x16x32_bf16 a[36:39], a[248:251], v[24:27], a[36:39]\n\t"
    "v_cmp_gt_f32 s[92:93], v223, v219\n\t"
    "s_or_b64 s[88:89], s[90:91], s[92:93]\n\t"
    "v_mfma_f32_16x16x32_bf16 a[40:43], a[248:251], v[40:43], a[40:43]\n\t"
    "s_add_u32 m0, s56, 25472\n\t"
    "ds_read_b64_tr_b16 a[242:243], v196 offset:19456\n\t"
    "s_or_b64 vcc, vcc, s[88:89]\n\t"
    "s_cbranch_scc1 SA1\n\t"
    "NSA1:\n\t"
    "v_mfma_f32_16x16x32_bf16 a[44:47], a[248:251], v[56:59], a[44:47]\n\t"
    "buffer_load_dwordx4 v200, s[36:39], s52 offen offset:128 lds\n\t"
    "v_fma_f32 v64, v64, s62, v212\n\t"
    "v_mfma_f32_16x16x32_bf16 a[60:63], a[252:255], v[56:59], a[60:63]\n\t"
    "ds_read_b64_tr_b16 a[244:245], v197 offset:13312\n\t"
    "v_fma_f32 v65, v65, s62, v212\n\t"
    "v_mfma_f32_16x16x32_bf16 a[56:59], a[252:255], v[40:43], a[56:59]\n\t"
    "v_exp_f32 v64, v64\n\t"
    "v_mfma_f32_16x16x32_bf16 v[228:231], v[244:247], v[8:11], v[228:231]\n\t"
    "v_exp_f32 v65, v65\n\t"
    "v_mfma_f32_16x16x32_bf16 a[52:55], a[252:255], v[24:27], a[52:55]\n\t"
    "ds_read_b64_tr_b16 a[246:247], v197 offset:19456\n\t"
    "v_cvt_pk_bf16_f32 v64, v64, v65\n\t"
    "v_mfma_f32_16x16x32_bf16 a[48:51], a[252:255], v[8:11], a[48:51]\n\t"
    "v_fma_f32 v66, v66, s62, v212\n\t"
    "v_fma_f32 v67, v67, s62, v212\n\t"
    "v_mfma_f32_16x16x32_bf16 a[64:67], a[224:227], v[0:3], 0\n\t"
    "ds_read_b64_tr_b16 a[248:249], v198 offset:13312\n\t"
    "v_fma_f32 v68, v68, s62, v212\n\t"
    "v_mfma_f32_16x16x32_bf16 a[68:71], a[224:227], v[16:19], 0\n\t"
    "v_exp_f32 v66, v66\n\t"
    "v_mfma_f32_16x16x32_bf16 v[232:235], v[244:247], v[24:27], v[232:235]\n\t"
    "v_exp_f32 v67, v67\n\t"
    "v_mfma_f32_16x16x32_bf16 a[72:75], a[224:227], v[32:35], 0\n\t"
    "ds_read_b64_tr_b16 a[250:251], v198 offset:19456\n\t"
    "v_cvt_pk_bf16_f32 v65, v66, v67\n\t"
    "v_mfma_f32_16x16x32_bf16 a[76:79], a[224:227], v[48:51], 0\n\t"
    "v_exp_f32 v68, v68\n\t"
    "v_mfma_f32_16x16x32_bf16 a[92:95], a[228:231], v[48:51], 0\n\t"
    "ds_read_b64_tr_b16 a[252:253], v199 offset:13312\n\t"
    "v_fma_f32 v69, v69, s62, v212\n\t"
    "v_mfma_f32_16x16x32_bf16 a[88:91], a[228:231], v[32:35], 0\n\t"
    "v_exp_f32 v69, v69\n\t"
    "v_mfma_f32_16x16x32_bf16 v[236:239], v[244:247], v[40:43], v[236:239]\n\t"
    "v_cvt_pk_bf16_f32 v66, v68, v69\n\t"
    "v_fma_f32 v70, v70, s62, v212\n\t"
    "v_mfma_f32_16x16x32_bf16 a[84:87], a[228:231], v[16:19], 0\n\t"
    "s_add_u32 m0, s56, 25472\n\t"
    "ds_read_b64_tr_b16 a[254:255], v199 offset:19456\n\t"
    "v_fma_f32 v71, v71, s62, v212\n\t"
    "v_mfma_f32_16x16x32_bf16 a[80:83], a[228:231], v[0:3], 0\n\t"
    "s_waitcnt lgkmcnt(8)\n\t"
    "buffer_load_dwordx4 v201, s[36:39], s52 offen offset:3200 lds\n\t"
    "v_fma_f32 v80, v80, s62, v213\n\t"
    "v_mfma_f32_16x16x32_bf16 a[96:99], a[232:235], v[0:3], 0\n\t"
    "ds_read_b64_tr_b16 a[224:225], v196 offset:2048\n\t"
    "v_fma_f32 v81, v81, s62, v213\n\t"
    "v_mfma_f32_16x16x32_bf16 a[100:103], a[232:235], v[16:19], 0\n\t"
    "v_exp_f32 v70, v70\n\t"
    "v_mfma_f32_16x16x32_bf16 v[240:243], v[244:247], v[56:59], v[240:243]\n\t"
    "v_exp_f32 v71, v71\n\t"
    "v_mfma_f32_16x16x32_bf16 a[104:107], a[232:235], v[32:35], 0\n\t"
    "ds_read_b64_tr_b16 a[226:227], v196 offset:8192\n\t"
    "v_cvt_pk_bf16_f32 v67, v70, v71\n\t"
    "v_mfma_f32_16x16x32_bf16 a[108:111], a[232:235], v[48:51], 0\n\t"
    "v_exp_f32 v80, v80\n\t"
    "v_mfma_f32_16x16x32_bf16 a[124:127], a[236:239], v[48:51], 0\n\t"
    "ds_read_b64_tr_b16 a[228:229], v197 offset:2048\n\t"
    "v_fma_f32 v82, v82, s62, v213\n\t"
    "v_mfma_f32_16x16x32_bf16 a[120:123], a[236:239], v[32:35], 0\n\t"
    "v_exp_f32 v81, v81\n\t"
    "v_mfma_f32_16x16x32_bf16 a[116:119], a[236:239], v[16:19], 0\n\t"
    "ds_read_b64_tr_b16 a[230:231], v197 offset:8192\n\t"
    "v_cvt_pk_bf16_f32 v80, v80, v81\n\t"
    "v_mfma_f32_16x16x32_bf16 a[112:115], a[236:239], v[0:3], 0\n\t"
    "s_waitcnt lgkmcnt(8)\n\t"
    "v_exp_f32 v82, v82\n\t"
    "v_mfma_f32_16x16x32_bf16 a[64:67], a[240:243], v[8:11], a[64:67]\n\t"
    "ds_read_b64_tr_b16 a[232:233], v198 offset:2048\n\t"
    "v_fma_f32 v83, v83, s62, v213\n\t"
    "v_mfma_f32_16x16x32_bf16 a[68:71], a[240:243], v[24:27], a[68:71]\n\t"
    "v_exp_f32 v83, v83\n\t"
    "v_mfma_f32_16x16x32_bf16 a[72:75], a[240:243], v[40:43], a[72:75]\n\t"
    "s_add_u32 m0, s56, 26368\n\t"
    "ds_read_b64_tr_b16 a[234:235], v198 offset:8192\n\t"
    "v_cvt_pk_bf16_f32 v81, v82, v83\n\t"
    "v_mfma_f32_16x16x32_bf16 a[76:79], a[240:243], v[56:59], a[76:79]\n\t"
    "buffer_load_dwordx4 v200, s[36:39], s52 offen offset:256 lds\n\t"
    "v_fma_f32 v84, v84, s62, v213\n\t"
    "v_mfma_f32_16x16x32_bf16 a[92:95], a[244:247], v[56:59], a[92:95]\n\t"
    "ds_read_b64_tr_b16 a[236:237], v199 offset:2048\n\t"
    "v_fma_f32 v85, v85, s62, v213\n\t"
    "v_mfma_f32_16x16x32_bf16 a[88:91], a[244:247], v[40:43], a[88:91]\n\t"
    "ds_read_b128 v[248:251], v203 offset:4096\n\t"
    "v_fma_f32 v86, v86, s62, v213\n\t"
    "v_mfma_f32_16x16x32_bf16 a[84:87], a[244:247], v[24:27], a[84:87]\n\t"
    "ds_read_b64_tr_b16 a[238:239], v199 offset:8192\n\t"
    "v_fma_f32 v87, v87, s62, v213\n\t"
    "v_mfma_f32_16x16x32_bf16 a[80:83], a[244:247], v[8:11], a[80:83]\n\t"
    "s_waitcnt lgkmcnt(9)\n\t"
    "v_exp_f32 v84, v84\n\t"
    "v_mfma_f32_16x16x32_bf16 a[96:99], a[248:251], v[8:11], a[96:99]\n\t"
    "s_waitcnt lgkmcnt(1)\n\t"
    "ds_read_b64_tr_b16 a[240:241], v196 offset:14336\n\t"
    "v_fma_f32 v96, v96, s62, v214\n\t"
    "v_mfma_f32_16x16x32_bf16 a[100:103], a[248:251], v[24:27], a[100:103]\n\t"
    "buffer_store_dwordx4 v[248:251], v200, s[48:51], s79 offen offset:128 nt\n\t"
    "v_mfma_f32_16x16x32_bf16 a[104:107], a[248:251], v[40:43], a[104:107]\n\t"
    "ds_read_b64_tr_b16 a[242:243], v196 offset:20480\n\t"
    "v_fma_f32 v97, v97, s62, v214\n\t"
    "v_mfma_f32_16x16x32_bf16 a[108:111], a[248:251], v[56:59], a[108:111]\n\t"
    "v_exp_f32 v85, v85\n\t"
    "v_mfma_f32_16x16x32_bf16 a[124:127], a[252:255], v[56:59], a[124:127]\n\t"
    "ds_read_b64_tr_b16 a[244:245], v197 offset:14336\n\t"
    "v_cvt_pk_bf16_f32 v82, v84, v85\n\t"
    "v_mfma_f32_16x16x32_bf16 a[120:123], a[252:255], v[40:43], a[120:123]\n\t"
    "v_exp_f32 v86, v86\n\t"
    "v_mfma_f32_16x16x32_bf16 a[116:119], a[252:255], v[24:27], a[116:119]\n\t"
    "s_add_u32 m0, s56, 26368\n\t"
    "ds_read_b64_tr_b16 a[246:247], v197 offset:20480\n\t"
    "v_fma_f32 v98, v98, s62, v214\n\t"
    "v_mfma_f32_16x16x32_bf16 a[112:115], a[252:255], v[8:11], a[112:115]\n\t"
    "buffer_load_dwordx4 v201, s[36:39], s52 offen offset:3328 lds\n\t"
    "v_fma_f32 v99, v99, s62, v214\n\t"
    "v_mfma_f32_16x16x32_bf16 v[128:131], a[224:227], v[0:3], 0\n\t"
    "ds_read_b64_tr_b16 a[248:249], v198 offset:14336\n\t"
    "v_fma_f32 v100, v100, s62, v214\n\t"
    "v_mfma_f32_16x16x32_bf16 v[132:135], a[224:227], v[16:19], 0\n\t"
    "v_exp_f32 v87, v87\n\t"
    "v_mfma_f32_16x16x32_bf16 v[136:139], a[224:227], v[32:35], 0\n\t"
    "ds_read_b64_tr_b16 a[250:251], v198 offset:20480\n\t"
    "v_cvt_pk_bf16_f32 v83, v86, v87\n\t"
    "v_mfma_f32_16x16x32_bf16 v[140:143], a[224:227], v[48:51], 0\n\t"
    "v_exp_f32 v96, v96\n\t"
    "v_mfma_f32_16x16x32_bf16 v[156:159], a[228:231], v[48:51], 0\n\t"
    "ds_read_b64_tr_b16 a[252:253], v199 offset:14336\n\t"
    "v_fma_f32 v101, v101, s62, v214\n\t"
    "v_mfma_f32_16x16x32_bf16 v[152:155], a[228:231], v[32:35], 0\n\t"
    "v_exp_f32 v97, v97\n\t"
    "v_mfma_f32_16x16x32_bf16 v[148:151], a[228:231], v[16:19], 0\n\t"
    "ds_read_b64_tr_b16 a[254:255], v199 offset:20480\n\t"
    "v_cvt_pk_bf16_f32 v96, v96, v97\n\t"
    "v_mfma_f32_16x16x32_bf16 v[144:147], a[228:231], v[0:3], 0\n\t"
    "s_waitcnt lgkmcnt(8)\n\t"
    "v_exp_f32 v98, v98\n\t"
    "v_mfma_f32_16x16x32_bf16 v[160:163], a[232:235], v[0:3], 0\n\t"
    "v_exp_f32 v99, v99\n\t"
    "v_mfma_f32_16x16x32_bf16 v[164:167], a[232:235], v[16:19], 0\n\t"
    "v_exp_f32 v100, v100\n\t"
    "v_mfma_f32_16x16x32_bf16 v[168:171], a[232:235], v[32:35], 0\n\t"
    "s_add_u32 s52, s52, 24576\n\t"
    "v_exp_f32 v101, v101\n\t"
    "v_mfma_f32_16x16x32_bf16 v[172:175], a[232:235], v[48:51], 0\n\t"
    "v_cvt_pk_bf16_f32 v97, v98, v99\n\t"
    "v_cvt_pk_bf16_f32 v98, v100, v101\n\t"
    "v_mfma_f32_16x16x32_bf16 v[188:191], a[236:239], v[48:51], 0\n\t"
    "v_fma_f32 v102, v102, s62, v214\n\t"
    "v_fma_f32 v103, v103, s62, v214\n\t"
    "v_mfma_f32_16x16x32_bf16 v[184:187], a[236:239], v[32:35], 0\n\t"
    "v_exp_f32 v102, v102\n\t"
    "v_mfma_f32_16x16x32_bf16 v[180:183], a[236:239], v[16:19], 0\n\t"
    "s_waitcnt lgkmcnt(0) vmcnt(17)\n\t"
    "v_exp_f32 v103, v103\n\t"
    "v_mfma_f32_16x16x32_bf16 v[176:179], a[236:239], v[0:3], 0\n\t"
    "v_cvt_pk_bf16_f32 v99, v102, v103\n\t"
    "v_fma_f32 v112, v112, s62, v215\n\t"
    "v_mfma_f32_16x16x32_bf16 v[128:131], a[240:243], v[8:11], v[128:131]\n\t"
    "s_barrier\n\t"
    "ds_read_b128 a[224:227], v192 offset:0\n\t"
    "v_fma_f32 v113, v113, s62, v215\n\t"
    "v_mfma_f32_16x16x32_bf16 v[132:135], a[240:243], v[24:27], v[132:135]\n\t"
    "ds_read_b128 a[228:231], v192 offset:6144\n\t"
    "v_fma_f32 v114, v114, s62, v215\n\t"
    "v_mfma_f32_16x16x32_bf16 v[136:139], a[240:243], v[40:43], v[136:139]\n\t"
    "ds_read_b128 a[232:235], v192 offset:12288\n\t"
    "v_fma_f32 v115, v115, s62, v215\n\t"
    "v_mfma_f32_16x16x32_bf16 v[140:143], a[240:243], v[56:59], v[140:143]\n\t"
    "v_exp_f32 v112, v112\n\t"
    "v_mfma_f32_16x16x32_bf16 v[156:159], a[244:247], v[56:59], v[156:159]\n\t"
    "s_cmp_eq_u32 s69, 3\n\t"
    "s_cselect_b32 s46, 0, s46\n\t"
    "v_fma_f32 v116, v116, s62, v215\n\t"
    "v_mfma_f32_16x16x32_bf16 v[152:155], a[244:247], v[40:43], v[152:155]\n\t"
    "v_exp_f32 v113, v113\n\t"
    "v_mfma_f32_16x16x32_bf16 v[148:151], a[244:247], v[24:27], v[148:151]\n\t"
    "ds_read_b128 a[236:239], v192 offset:18432\n\t"
    "v_cvt_pk_bf16_f32 v112, v112, v113\n\t"
    "v_mfma_f32_16x16x32_bf16 v[144:147], a[244:247], v[8:11], v[144:147]\n\t"
    "v_exp_f32 v114, v114\n\t"
    "v_mfma_f32_16x16x32_bf16 v[160:163], a[248:251], v[8:11], v[160:163]\n\t"
    "ds_read_b128 v[248:251], v203 offset:5120\n\t"
    "v_fma_f32 v117, v117, s62, v215\n\t"
    "v_mfma_f32_16x16x32_bf16 v[164:167], a[248:251], v[24:27], v[164:167]\n\t"
    "v_exp_f32 v115, v115\n\t"
    "v_mfma_f32_16x16x32_bf16 v[168:171], a[248:251], v[40:43], v[168:171]\n\t"
    "ds_read_b128 a[240:243], v193 offset:0\n\t"
    "v_cvt_pk_bf16_f32 v113, v114, v115\n\t"
    "v_mfma_f32_16x16x32_bf16 v[172:175], a[248:251], v[56:59], v[172:175]\n\t"
    "v_exp_f32 v116, v116\n\t"
    "v_mfma_f32_16x16x32_bf16 v[188:191], a[252:255], v[56:59], v[188:191]\n\t"
    "s_waitcnt lgkmcnt(1)\n\t"
    "buffer_store_dwordx4 v[248:251], v201, s[48:51], s79 offen offset:3200 nt\n\t"
    "v_mfma_f32_16x16x32_bf16 v[184:187], a[252:255], v[40:43], v[184:187]\n\t"
    "v_exp_f32 v117, v117\n\t"
    "v_mfma_f32_16x16x32_bf16 v[180:183], a[252:255], v[24:27], v[180:183]\n\t"
    "ds_read_b128 a[244:247], v193 offset:6144\n\t"
    "v_cvt_pk_bf16_f32 v114, v116, v117\n\t"
    "v_mfma_f32_16x16x32_bf16 v[176:179], a[252:255], v[8:11], v[176:179]\n\t"
    "v_fma_f32 v118, v118, s62, v215\n\t"
    "v_fma_f32 v119, v119, s62, v215\n\t"
    "v_mfma_f32_16x16x32_bf16 v[0:3], a[224:227], a[128:131], v[224:227]\n\t"
    "s_cmp_lg_u32 s72, 0\n\t"
    "s_cbranch_scc1 SB1\n\t"
    "NSB1:\n\t"
    "v_fma_f32 v72, v72, s62, v212\n\t"
    "v_mfma_f32_16x16x32_bf16 v[16:19], a[224:227], a[132:135], v[224:227]\n\t"
    "v_exp_f32 v118, v118\n\t"
    "v_mfma_f32_16x16x32_bf16 v[32:35], a[224:227], a[136:139], v[224:227]\n\t"
    "s_add_u32 m0, s57, 0\n\t"
    "ds_read_b128 a[248:251], v193 offset:12288\n\t"
    "v_fma_f32 v73, v73, s62, v212\n\t"
    "v_mfma_f32_16x16x32_bf16 v[48:51], a[224:227], a[140:143], v[224:227]\n\t"
    "buffer_load_dwordx4 v202, s[40:43], s53 offen offset:0 lds\n\t"
    "v_fma_f32 v74, v74, s62, v212\n\t"
    "v_mfma_f32_16x16x32_bf16 v[52:55], a[228:231], a[140:143], v[224:227]\n\t"
    "v_exp_f32 v119, v119\n\t"
    "v_mfma_f32_16x16x32_bf16 v[36:39], a[228:231], a[136:139], v[224:227]\n\t"
    "v_exp_f32 v72, v72\n\t"
    "v_mfma_f32_16x16x32_bf16 v[20:23], a[228:231], a[132:135], v[224:227]\n\t"
    "ds_read_b128 a[252:255], v193 offset:18432\n\t"
    "v_cvt_pk_bf16_f32 v115, v118, v119\n\t"
    "v_mfma_f32_16x16x32_bf16 v[4:7], a[228:231], a[128:131], v[224:227]\n\t"
    "v_exp_f32 v73, v73\n\t"
    "v_mfma_f32_16x16x32_bf16 v[8:11], a[232:235], a[128:131], v[224:227]\n\t"
    "v_exp_f32 v74, v74\n\t"
    "v_mfma_f32_16x16x32_bf16 v[24:27], a[232:235], a[132:135], v[224:227]\n\t"
    "v_cvt_pk_bf16_f32 v72, v72, v73\n\t"
    "v_fma_f32 v75, v75, s62, v212\n\t"
    "v_mfma_f32_16x16x32_bf16 v[40:43], a[232:235], a[136:139], v[224:227]\n\t"
    "ds_read_b128 a[224:227], v192 offset:1024\n\t"
    "v_fma_f32 v76, v76, s62, v212\n\t"
    "v_mfma_f32_16x16x32_bf16 v[56:59], a[232:235], a[140:143], v[224:227]\n\t"
    "v_exp_f32 v75, v75\n\t"
    "v_mfma_f32_16x16x32_bf16 v[60:63], a[236:239], a[140:143], v[224:227]\n\t"
    "v_exp_f32 v76, v76\n\t"
    "v_mfma_f32_16x16x32_bf16 v[44:47], a[236:239], a[136:139], v[224:227]\n\t"
    "v_cvt_pk_bf16_f32 v73, v74, v75\n\t"
    "v_fma_f32 v77, v77, s62, v212\n\t"
    "v_mfma_f32_16x16x32_bf16 v[28:31], a[236:239], a[132:135], v[224:227]\n\t"
    "s_add_u32 m0, s57, 0\n\t"
    "ds_read_b128 a[228:231], v192 offset:7168\n\t"
    "v_fma_f32 v78, v78, s62, v212\n\t"
    "v_mfma_f32_16x16x32_bf16 v[12:15], a[236:239], a[128:131], v[224:227]\n\t"
    "s_waitcnt lgkmcnt(4)\n\t"
    "buffer_load_dwordx4 v202, s[40:43], s53 offen offset:3072 lds\n\t"
    "v_fma_f32 v79, v79, s62, v212\n\t"
    "v_mfma_f32_16x16x32_bf16 v[0:3], a[240:243], a[144:147], v[0:3]\n\t"
    "v_exp_f32 v77, v77\n\t"
    "v_mfma_f32_16x16x32_bf16 v[16:19], a[240:243], a[148:151], v[16:19]\n\t"
    "v_exp_f32 v78, v78\n\t"
    "v_mfma_f32_16x16x32_bf16 v[32:35], a[240:243], a[152:155], v[32:35]\n\t"
    "ds_read_b128 a[232:235], v192 offset:13312\n\t"
    "v_cvt_pk_bf16_f32 v74, v76, v77\n\t"
    "v_mfma_f32_16x16x32_bf16 v[48:51], a[240:243], a[156:159], v[48:51]\n\t"
    "v_exp_f32 v79, v79\n\t"
    "v_mfma_f32_16x16x32_bf16 v[52:55], a[244:247], a[156:159], v[52:55]\n\t"
    "v_cvt_pk_bf16_f32 v75, v78, v79\n\t"
    "v_fma_f32 v88, v88, s62, v213\n\t"
    "v_mfma_f32_16x16x32_bf16 v[36:39], a[244:247], a[152:155], v[36:39]\n\t"
    "v_exp_f32 v88, v88\n\t"
    "v_mfma_f32_16x16x32_bf16 v[20:23], a[244:247], a[148:151], v[20:23]\n\t"
    "ds_read_b128 a[236:239], v192 offset:19456\n\t"
    "v_fma_f32 v89, v89, s62, v213\n\t"
    "v_mfma_f32_16x16x32_bf16 v[4:7], a[244:247], a[144:147], v[4:7]\n\t"
    "s_waitcnt lgkmcnt(4)\n\t"
    "v_exp_f32 v89, v89\n\t"
    "v_mfma_f32_16x16x32_bf16 v[8:11], a[248:251], a[144:147], v[8:11]\n\t"
    "v_cvt_pk_bf16_f32 v88, v88, v89\n\t"
    "v_fma_f32 v90, v90, s62, v213\n\t"
    "v_mfma_f32_16x16x32_bf16 v[24:27], a[248:251], a[148:151], v[24:27]\n\t"
    "v_exp_f32 v90, v90\n\t"
    "v_mfma_f32_16x16x32_bf16 v[40:43], a[248:251], a[152:155], v[40:43]\n\t"
    "s_add_u32 m0, s57, 896\n\t"
    "ds_read_b128 a[240:243], v193 offset:1024\n\t"
    "v_fma_f32 v91, v91, s62, v213\n\t"
    "v_mfma_f32_16x16x32_bf16 v[56:59], a[248:251], a[156:159], v[56:59]\n\t"
    "buffer_load_dwordx4 v202, s[40:43], s53 offen offset:128 lds\n\t"
    "v_fma_f32 v92, v92, s62, v213\n\t"
    "v_mfma_f32_16x16x32_bf16 v[60:63], a[252:255], a[156:159], v[60:63]\n\t"
    "ds_read_b128 v[248:251], v203 offset:6144\n\t"
    "v_fma_f32 v93, v93, s62, v213\n\t"
    "v_mfma_f32_16x16x32_bf16 v[44:47], a[252:255], a[152:155], v[44:47]\n\t"
    "v_exp_f32 v91, v91\n\t"
    "v_mfma_f32_16x16x32_bf16 v[28:31], a[252:255], a[148:151], v[28:31]\n\t"
    "ds_read_b128 a[244:247], v193 offset:7168\n\t"
    "v_cvt_pk_bf16_f32 v89, v90, v91\n\t"
    "v_mfma_f32_16x16x32_bf16 v[12:15], a[252:255], a[144:147], v[12:15]\n\t"
    "s_waitcnt lgkmcnt(5)\n\t"
    "v_exp_f32 v92, v92\n\t"
    "v_mfma_f32_16x16x32_bf16 v[0:3], a[224:227], a[160:163], v[0:3]\n\t"
    "s_waitcnt lgkmcnt(1)\n\t"
    "buffer_store_dwordx4 v[248:251], v200, s[48:51], s80 offen offset:128 nt\n\t"
    "v_mfma_f32_16x16x32_bf16 v[16:19], a[224:227], a[164:167], v[16:19]\n\t"
    "v_exp_f32 v93, v93\n\t"
    "v_mfma_f32_16x16x32_bf16 v[32:35], a[224:227], a[168:171], v[32:35]\n\t"
    "ds_read_b128 a[248:251], v193 offset:13312\n\t"
    "v_cvt_pk_bf16_f32 v90, v92, v93\n\t"
    "v_mfma_f32_16x16x32_bf16 v[48:51], a[224:227], a[172:175], v[48:51]\n\t"
    "v_fma_f32 v94, v94, s62, v213\n\t"
    "v_fma_f32 v95, v95, s62, v213\n\t"
    "v_mfma_f32_16x16x32_bf16 v[52:55], a[228:231], a[172:175], v[52:55]\n\t"
    "v_exp_f32 v94, v94\n\t"
    "v_mfma_f32_16x16x32_bf16 v[36:39], a[228:231], a[168:171], v[36:39]\n\t"
    "v_exp_f32 v95, v95\n\t"
    "v_mfma_f32_16x16x32_bf16 v[20:23], a[228:231], a[164:167], v[20:23]\n\t"
    "s_add_u32 m0, s57, 896\n\t"
    "ds_read_b128 a[252:255], v193 offset:19456\n\t"
    "v_cvt_pk_bf16_f32 v91, v94, v95\n\t"
    "v_mfma_f32_16x16x32_bf16 v[4:7], a[228:231], a[160:163], v[4:7]\n\t"
    "buffer_load_dwordx4 v202, s[40:43], s53 offen offset:3200 lds\n\t"
    "v_fma_f32 v104, v104, s62, v214\n\t"
    "v_mfma_f32_16x16x32_bf16 v[8:11], a[232:235], a[160:163], v[8:11]\n\t"
    "v_exp_f32 v104, v104\n\t"
    "v_mfma_f32_16x16x32_bf16 v[24:27], a[232:235], a[164:167], v[24:27]\n\t"
    "v_fma_f32 v105, v105, s62, v214\n\t"
    "v_fma_f32 v106, v106, s62, v214\n\t"
    "v_mfma_f32_16x16x32_bf16 v[40:43], a[232:235], a[168:171], v[40:43]\n\t"
    "ds_read_b128 a[224:227], v192 offset:2048\n\t"
    "v_fma_f32 v107, v107, s62, v214\n\t"
    "v_mfma_f32_16x16x32_bf16 v[56:59], a[232:235], a[172:175], v[56:59]\n\t"
    "v_exp_f32 v105, v105\n\t"
    "v_mfma_f32_16x16x32_bf16 v[60:63], a[236:239], a[172:175], v[60:63]\n\t"
    "v_exp_f32 v106, v106\n\t"
    "v_mfma_f32_16x16x32_bf16 v[44:47], a[236:239], a[168:171], v[44:47]\n\t"
    "v_exp_f32 v107, v107\n\t"
    "v_mfma_f32_16x16x32_bf16 v[28:31], a[236:239], a[164:167], v[28:31]\n\t"
    "ds_read_b128 a[228:231], v192 offset:8192\n\t"
    "v_cvt_pk_bf16_f32 v104, v104, v105\n\t"
    "v_mfma_f32_16x16x32_bf16 v[12:15], a[236:239], a[160:163], v[12:15]\n\t"
    "s_waitcnt lgkmcnt(4)\n\t"
    "v_cvt_pk_bf16_f32 v105, v106, v107\n\t"
    "v_fma_f32 v108, v108, s62, v214\n\t"
    "v_mfma_f32_16x16x32_bf16 v[0:3], a[240:243], a[176:179], v[0:3]\n\t"
    "v_exp_f32 v108, v108\n\t"
    "v_mfma_f32_16x16x32_bf16 v[16:19], a[240:243], a[180:183], v[16:19]\n\t"
    "v_fma_f32 v109, v109, s62, v214\n\t"
    "v_fma_f32 v110, v110, s62, v214\n\t"
    "v_mfma_f32_16x16x32_bf16 v[32:35], a[240:243], a[184:187], v[32:35]\n\t"
    "s_add_u32 m0, s57, 1792\n\t"
    "ds_read_b128 a[232:235], v192 offset:14336\n\t"
    "v_fma_f32 v111, v111, s62, v214\n\t"
    "v_mfma_f32_16x16x32_bf16 v[48:51], a[240:243], a[188:191], v[48:51]\n\t"
    "buffer_load_dwordx4 v202, s[40:43], s53 offen offset:256 lds\n\t"
    "v_fma_f32 v120, v120, s62, v215\n\t"
    "v_mfma_f32_16x16x32_bf16 v[52:55], a[244:247], a[188:191], v[52:55]\n\t"
    "v_exp_f32 v109, v109\n\t"
    "v_mfma_f32_16x16x32_bf16 v[36:39], a[244:247], a[184:187], v[36:39]\n\t"
    "v_exp_f32 v110, v110\n\t"
    "v_mfma_f32_16x16x32_bf16 v[20:23], a[244:247], a[180:183], v[20:23]\n\t"
    "ds_read_b128 a[236:239], v192 offset:20480\n\t"
    "v_cvt_pk_bf16_f32 v106, v108, v109\n\t"
    "v_mfma_f32_16x16x32_bf16 v[4:7], a[244:247], a[176:179], v[4:7]\n\t"
    "s_waitcnt lgkmcnt(4)\n\t"
    "v_exp_f32 v111, v111\n\t"
    "v_mfma_f32_16x16x32_bf16 v[8:11], a[248:251], a[176:179], v[8:11]\n\t"
    "v_exp_f32 v120, v120\n\t"
    "v_mfma_f32_16x16x32_bf16 v[24:27], a[248:251], a[180:183], v[24:27]\n\t"
    "v_cvt_pk_bf16_f32 v107, v110, v111\n\t"
    "v_fma_f32 v121, v121, s62, v215\n\t"
    "v_mfma_f32_16x16x32_bf16 v[40:43], a[248:251], a[184:187], v[40:43]\n\t"
    "ds_read_b128 a[240:243], v193 offset:2048\n\t"
    "v_fma_f32 v122, v122, s62, v215\n\t"
    "v_mfma_f32_16x16x32_bf16 v[56:59], a[248:251], a[188:191], v[56:59]\n\t"
    "v_exp_f32 v121, v121\n\t"
    "v_mfma_f32_16x16x32_bf16 v[60:63], a[252:255], a[188:191], v[60:63]\n\t"
    "v_exp_f32 v122, v122\n\t"
    "v_mfma_f32_16x16x32_bf16 v[44:47], a[252:255], a[184:187], v[44:47]\n\t"
    "v_cvt_pk_bf16_f32 v120, v120, v121\n\t"
    "v_fma_f32 v123, v123, s62, v215\n\t"
    "v_mfma_f32_16x16x32_bf16 v[28:31], a[252:255], a[180:183], v[28:31]\n\t"
    "s_add_u32 m0, s57, 1792\n\t"
    "ds_read_b128 a[244:247], v193 offset:8192\n\t"
    "v_fma_f32 v124, v124, s62, v215\n\t"
    "v_mfma_f32_16x16x32_bf16 v[12:15], a[252:255], a[176:179], v[12:15]\n\t"
    "s_waitcnt lgkmcnt(4)\n\t"
    "buffer_load_dwordx4 v202, s[40:43], s53 offen offset:3328 lds\n\t"
    "v_fma_f32 v125, v125, s62, v215\n\t"
    "v_mfma_f32_16x16x32_bf16 v[0:3], a[224:227], a[192:195], v[0:3]\n\t"
    "ds_read_b128 v[248:251], v203 offset:7168\n\t"
    "v_fma_f32 v126, v126, s62, v215\n\t"
    "v_mfma_f32_16x16x32_bf16 v[16:19], a[224:227], a[196:199], v[16:19]\n\t"
    "v_exp_f32 v123, v123\n\t"
    "v_mfma_f32_16x16x32_bf16 v[32:35], a[224:227], a[200:203], v[32:35]\n\t"
    "ds_read_b128 a[248:251], v193 offset:14336\n\t"
    "v_cvt_pk_bf16_f32 v121, v122, v123\n\t"
    "v_mfma_f32_16x16x32_bf16 v[48:51], a[224:227], a[204:207], v[48:51]\n\t"
    "v_exp_f32 v124, v124\n\t"
    "v_mfma_f32_16x16x32_bf16 v[52:55], a[228:231], a[204:207], v[52:55]\n\t"
    "s_waitcnt lgkmcnt(1)\n\t"
    "buffer_store_dwordx4 v[248:251], v201, s[48:51], s80 offen offset:3200 nt\n\t"
    "v_mfma_f32_16x16x32_bf16 v[36:39], a[228:231], a[200:203], v[36:39]\n\t"
    "v_exp_f32 v125, v125\n\t"
    "v_mfma_f32_16x16x32_bf16 v[20:23], a[228:231], a[196:199], v[20:23]\n\t"
    "ds_read_b128 a[252:255], v193 offset:20480\n\t"
    "v_cvt_pk_bf16_f32 v122, v124, v125\n\t"
    "v_mfma_f32_16x16x32_bf16 v[4:7], a[228:231], a[192:195], v[4:7]\n\t"
    "v_exp_f32 v126, v126\n\t"
    "v_mfma_f32_16x16x32_bf16 v[8:11], a[232:235], a[192:195], v[8:11]\n\t"
    "v_fma_f32 v127, v127, s62, v215\n\t"
    "v_mfma_f32_16x16x32_bf16 v[24:27], a[232:235], a[196:199], v[24:27]\n\t"
    "v_exp_f32 v127, v127\n\t"
    "v_mfma_f32_16x16x32_bf16 v[40:43], a[232:235], a[200:203], v[40:43]\n\t"
    "s_add_u32 s53, s53, 24576\n\t"
    "v_cvt_pk_bf16_f32 v123, v126, v127\n\t"
    "v_mfma_f32_16x16x32_bf16 v[56:59], a[232:235], a[204:207], v[56:59]\n\t"
    "v_mfma_f32_16x16x32_bf16 v[60:63], a[236:239], a[204:207], v[60:63]\n\t"
    "v_mfma_f32_16x16x32_bf16 v[44:47], a[236:239], a[200:203], v[44:47]\n\t"
    "v_mfma_f32_16x16x32_bf16 v[28:31], a[236:239], a[196:199], v[28:31]\n\t"
    "s_waitcnt lgkmcnt(0) vmcnt(18)\n\t"
    "v_mfma_f32_16x16x32_bf16 v[12:15], a[236:239], a[192:195], v[12:15]\n\t"
    "v_mfma_f32_16x16x32_bf16 v[0:3], a[240:243], a[208:211], v[0:3]\n\t"
    "s_barrier\n\t"
    "ds_read_b64_tr_b16 a[224:225], v196 offset:24576\n\t"
    "v_mfma_f32_16x16x32_bf16 v[16:19], a[240:243], a[212:215], v[16:19]\n\t"
    "ds_read_b64_tr_b16 a[226:227], v196 offset:30720\n\t"
    "v_mfma_f32_16x16x32_bf16 v[32:35], a[240:243], a[216:219], v[32:35]\n\t"
    "ds_read_b64_tr_b16 a[228:229], v197 offset:24576\n\t"
    "v_mfma_f32_16x16x32_bf16 v[48:51], a[240:243], a[220:223], v[48:51]\n\t"
    "ds_read_b64_tr_b16 a[230:231], v197 offset:30720\n\t"
    "v_mfma_f32_16x16x32_bf16 v[52:55], a[244:247], a[220:223], v[52:55]\n\t"
    "ds_read_b64_tr_b16 a[232:233], v198 offset:24576\n\t"
    "v_mfma_f32_16x16x32_bf16 v[36:39], a[244:247], a[216:219], v[36:39]\n\t"
    "ds_read_b64_tr_b16 a[234:235], v198 offset:30720\n\t"
    "v_mfma_f32_16x16x32_bf16 v[20:23], a[244:247], a[212:215], v[20:23]\n\t"
    "ds_read_b64_tr_b16 a[236:237], v199 offset:24576\n\t"
    "v_mfma_f32_16x16x32_bf16 v[4:7], a[244:247], a[208:211], v[4:7]\n\t"
    "ds_read_b64_tr_b16 a[238:239], v199 offset:30720\n\t"
    "v_mfma_f32_16x16x32_bf16 v[8:11], a[248:251], a[208:211], v[8:11]\n\t"
    "ds_read_b64_tr_b16 a[240:241], v196 offset:36864\n\t"
    "v_mfma_f32_16x16x32_bf16 v[24:27], a[248:251], a[212:215], v[24:27]\n\t"
    "v_mfma_f32_16x16x32_bf16 v[40:43], a[248:251], a[216:219], v[40:43]\n\t"
    "ds_read_b64_tr_b16 a[242:243], v196 offset:43008\n\t"
    "v_mfma_f32_16x16x32_bf16 v[56:59], a[248:251], a[220:223], v[56:59]\n\t"
    "v_mfma_f32_16x16x32_bf16 v[60:63], a[252:255], a[220:223], v[60:63]\n\t"
    "ds_read_b64_tr_b16 a[244:245], v197 offset:36864\n\t"
    "v_mfma_f32_16x16x32_bf16 v[44:47], a[252:255], a[216:219], v[44:47]\n\t"
    "v_mfma_f32_16x16x32_bf16 v[28:31], a[252:255], a[212:215], v[28:31]\n\t"
    "ds_read_b64_tr_b16 a[246:247], v197 offset:43008\n\t"
    "v_mfma_f32_16x16x32_bf16 v[12:15], a[252:255], a[208:211], v[12:15]\n\t"
    "s_waitcnt lgkmcnt(8)\n\t"
    "v_mfma_f32_16x16x32_bf16 a[0:3], a[224:227], v[64:67], a[0:3]\n\t"
    "ds_read_b64_tr_b16 a[248:249], v198 offset:36864\n\t"
    "v_mfma_f32_16x16x32_bf16 a[4:7], a[224:227], v[80:83], a[4:7]\n\t"
    "v_mfma_f32_16x16x32_bf16 a[8:11], a[224:227], v[96:99], a[8:11]\n\t"
    "s_add_u32 m0, s56, 0\n\t"
    "ds_read_b64_tr_b16 a[250:251], v198 offset:43008\n\t"
    "v_mfma_f32_16x16x32_bf16 a[12:15], a[224:227], v[112:115], a[12:15]\n\t"
    "buffer_load_dwordx4 v200, s[36:39], s52 offen offset:0 lds\n\t"
    "v_mfma_f32_16x16x32_bf16 a[28:31], a[228:231], v[112:115], a[28:31]\n\t"
    "ds_read_b64_tr_b16 a[252:253], v199 offset:36864\n\t"
    "v_max3_f32 v220, v0, v1, v2\n\t"
    "v_mfma_f32_16x16x32_bf16 a[24:27], a[228:231], v[96:99], a[24:27]\n\t"
    "ds_read_b128 v[248:251], v203 offset:8192\n\t"
    "v_max3_f32 v221, v16, v17, v18\n\t"
    "v_mfma_f32_16x16x32_bf16 a[20:23], a[228:231], v[80:83], a[20:23]\n\t"
    "ds_read_b64_tr_b16 a[254:255], v199 offset:43008\n\t"
    "v_max3_f32 v222, v32, v33, v34\n\t"
    "v_mfma_f32_16x16x32_bf16 a[16:19], a[228:231], v[64:67], a[16:19]\n\t"
    "s_waitcnt lgkmcnt(9)\n\t"
    "v_max3_f32 v223, v48, v49, v50\n\t"
    "v_max3_f32 v220, v220, v3, v4\n\t"
    "v_mfma_f32_16x16x32_bf16 a[32:35], a[232:235], v[64:67], a[32:35]\n\t"
    "s_waitcnt lgkmcnt(1)\n\t"
    "ds_read_b64_tr_b16 a[224:225], v196 offset:25600\n\t"
    "v_max3_f32 v221, v221, v19, v20\n\t"
    "v_mfma_f32_16x16x32_bf16 a[36:39], a[232:235], v[80:83], a[36:39]\n\t"
    "buffer_store_dwordx4 v[248:251], v200, s[48:51], s70 offen offset:256 nt\n\t"
    "v_mfma_f32_16x16x32_bf16 v[228:231], v[244:247], v[64:67], v[228:231]\n\t"
    "v_max3_f32 v222, v222, v35, v36\n\t"
    "v_max3_f32 v223, v223, v51, v52\n\t"
    "v_mfma_f32_16x16x32_bf16 a[40:43], a[232:235], v[96:99], a[40:43]\n\t"
    "ds_read_b64_tr_b16 a[226:227], v196 offset:31744\n\t"
    "v_max3_f32 v220, v220, v5, v6\n\t"
    "v_mfma_f32_16x16x32_bf16 a[44:47], a[232:235], v[112:115], a[44:47]\n\t"
    "v_max3_f32 v221, v221, v21, v22\n\t"
    "v_max3_f32 v222, v222, v37, v38\n\t"
    "v_mfma_f32_16x16x32_bf16 a[60:63], a[236:239], v[112:115], a[60:63]\n\t"
    "ds_read_b64_tr_b16 a[228:229], v197 offset:25600\n\t"
    "v_max3_f32 v223, v223, v53, v54\n\t"
    "v_mfma_f32_16x16x32_bf16 a[56:59], a[236:239], v[96:99], a[56:59]\n\t"
    "v_max3_f32 v220, v220, v7, v8\n\t"
    "v_max3_f32 v221, v221, v23, v24\n\t"
    "v_mfma_f32_16x16x32_bf16 v[232:235], v[244:247], v[80:83], v[232:235]\n\t"
    "v_max3_f32 v222, v222, v39, v40\n\t"
    "v_max3_f32 v223, v223, v55, v56\n\t"
    "v_mfma_f32_16x16x32_bf16 a[52:55], a[236:239], v[80:83], a[52:55]\n\t"
    "s_add_u32 m0, s56, 0\n\t"
    "ds_read_b64_tr_b16 a[230:231], v197 offset:31744\n\t"
    "v_max3_f32 v220, v220, v9, v10\n\t"
    "v_mfma_f32_16x16x32_bf16 a[48:51], a[236:239], v[64:67], a[48:51]\n\t"
    "buffer_load_dwordx4 v201, s[36:39], s52 offen offset:3072 lds\n\t"
    "v_max3_f32 v221, v221, v25, v26\n\t"
    "v_mfma_f32_16x16x32_bf16 a[0:3], a[240:243], v[72:75], a[0:3]\n\t"
    "ds_read_b64_tr_b16 a[232:233], v198 offset:25600\n\t"
    "v_max3_f32 v222, v222, v41, v42\n\t"
    "v_mfma_f32_16x16x32_bf16 a[4:7], a[240:243], v[88:91], a[4:7]\n\t"
    "v_max3_f32 v223, v223, v57, v58\n\t"
    "v_max3_f32 v220, v220, v11, v12\n\t"
    "v_mfma_f32_16x16x32_bf16 v[236:239], v[244:247], v[96:99], v[236:239]\n\t"
    "v_max3_f32 v221, v221, v27, v28\n\t"
    "v_max3_f32 v222, v222, v43, v44\n\t"
    "v_mfma_f32_16x16x32_bf16 a[8:11], a[240:243], v[104:107], a[8:11]\n\t"
    "ds_read_b64_tr_b16 a[234:235], v198 offset:31744\n\t"
    "v_max3_f32 v223, v223, v59, v60\n\t"
    "v_mfma_f32_16x16x32_bf16 a[12:15], a[240:243], v[120:123], a[12:15]\n\t"
    "v_max3_f32 v220, v220, v13, v14\n\t"
    "v_max3_f32 v221, v221, v29, v30\n\t"
    "v_mfma_f32_16x16x32_bf16 a[28:31], a[244:247], v[120:123], a[28:31]\n\t"
    "ds_read_b64_tr_b16 a[236:237], v199 offset:25600\n\t"
    "v_max3_f32 v222, v222, v45, v46\n\t"
    "v_mfma_f32_16x16x32_bf16 a[24:27], a[244:247], v[104:107], a[24:27]\n\t"
    "v_max3_f32 v223, v223, v61, v62\n\t"
    "v_max_f32 v220, v220, v15\n\t"
    "v_mfma_f32_16x16x32_bf16 v[240:243], v[244:247], v[112:115], v[240:243]\n\t"
    "v_max_f32 v221, v221, v31\n\t"
    "v_max_f32 v222, v222, v47\n\t"
    "v_mfma_f32_16x16x32_bf16 a[20:23], a[244:247], v[88:91], a[20:23]\n\t"
    "ds_read_b64_tr_b16 a[238:239], v199 offset:31744\n\t"
    "v_max_f32 v223, v223, v63\n\t"
    "v_mfma_f32_16x16x32_bf16 a[16:19], a[244:247], v[72:75], a[16:19]\n\t"
    "s_waitcnt lgkmcnt(8)\n\t"
    "v_cmp_gt_f32 vcc, v220, v216\n\t"
    "v_cmp_gt_f32 s[88:89], v221, v217\n\t"
    "v_mfma_f32_16x16x32_bf16 a[32:35], a[248:251], v[72:75], a[32:35]\n\t"
    "s_or_b64 vcc, vcc, s[88:89]\n\t"
    "ds_read_b64_tr_b16 a[240:241], v196 offset:37888\n\t"
    "v_cmp_gt_f32 s[90:91], v222, v218\n\t"
    "v_mfma_f32_16x16x32_bf16 a[36:39], a[248:251], v[88:91], a[36:39]\n\t"
    "v_cmp_gt_f32 s[92:93], v223, v219\n\t"
    "s_or_b64 s[88:89], s[90:91], s[92:93]\n\t"
    "v_mfma_f32_16x16x32_bf16 a[40:43], a[248:251], v[104:107], a[40:43]\n\t"
    "s_add_u32 m0, s56, 896\n\t"
    "ds_read_b64_tr_b16 a[242:243], v196 offset:44032\n\t"
    "s_or_b64 vcc, vcc, s[88:89]\n\t"
    "s_cbranch_scc1 SA2\n\t"
    "NSA2:\n\t"
    "v_mfma_f32_16x16x32_bf16 a[44:47], a[248:251], v[120:123], a[44:47]\n\t"
    "buffer_load_dwordx4 v200, s[36:39], s52 offen offset:128 lds\n\t"
    "v_fma_f32 v0, v0, s62, v212\n\t"
    "v_mfma_f32_16x16x32_bf16 a[60:63], a[252:255], v[120:123], a[60:63]\n\t"
    "ds_read_b64_tr_b16 a[244:245], v197 offset:37888\n\t"
    "v_fma_f32 v1, v1, s62, v212\n\t"
    "v_mfma_f32_16x16x32_bf16 a[56:59], a[252:255], v[104:107], a[56:59]\n\t"
    "v_exp_f32 v0, v0\n\t"
    "v_mfma_f32_16x16x32_bf16 v[228:231], v[244:247], v[72:75], v[228:231]\n\t"
    "v_exp_f32 v1, v1\n\t"
    "v_mfma_f32_16x16x32_bf16 a[52:55], a[252:255], v[88:91], a[52:55]\n\t"
    "ds_read_b64_tr_b16 a[246:247], v197 offset:44032\n\t"
    "v_cvt_pk_bf16_f32 v0, v0, v1\n\t"
    "v_mfma_f32_16x16x32_bf16 a[48:51], a[252:255], v[72:75], a[48:51]\n\t"
    "s_waitcnt lgkmcnt(8)\n\t"
    "v_fma_f32 v2, v2, s62, v212\n\t"
    "v_fma_f32 v3, v3, s62, v212\n\t"
    "v_mfma_f32_16x16x32_bf16 a[64:67], a[224:227], v[64:67], a[64:67]\n\t"
    "ds_read_b64_tr_b16 a[248:249], v198 offset:37888\n\t"
    "v_fma_f32 v4, v4, s62, v212\n\t"
    "v_mfma_f32_16x16x32_bf16 a[68:71], a[224:227], v[80:83], a[68:71]\n\t"
    "v_exp_f32 v2, v2\n\t"
    "v_mfma_f32_16x16x32_bf16 v[232:235], v[244:247], v[88:91], v[232:235]\n\t"
    "v_exp_f32 v3, v3\n\t"
    "v_mfma_f32_16x16x32_bf16 a[72:75], a[224:227], v[96:99], a[72:75]\n\t"
    "ds_read_b64_tr_b16 a[250:251], v198 offset:44032\n\t"
    "v_cvt_pk_bf16_f32 v1, v2, v3\n\t"
    "v_mfma_f32_16x16x32_bf16 a[76:79], a[224:227], v[112:115], a[76:79]\n\t"
    "v_exp_f32 v4, v4\n\t"
    "v_mfma_f32_16x16x32_bf16 a[92:95], a[228:231], v[112:115], a[92:95]\n\t"
    "ds_read_b64_tr_b16 a[252:253], v199 offset:37888\n\t"
    "v_fma_f32 v5, v5, s62, v212\n\t"
    "v_mfma_f32_16x16x32_bf16 a[88:91], a[228:231], v[96:99], a[88:91]\n\t"
    "v_exp_f32 v5, v5\n\t"
    "v_mfma_f32_16x16x32_bf16 v[236:239], v[244:247], v[104:107], v[236:239]\n\t"
    "v_cvt_pk_bf16_f32 v2, v4, v5\n\t"
    "v_fma_f32 v6, v6, s62, v212\n\t"
    "v_mfma_f32_16x16x32_bf16 a[84:87], a[228:231], v[80:83], a[84:87]\n\t"
    "s_add_u32 m0, s56, 896\n\t"
    "ds_read_b64_tr_b16 a[254:255], v199 offset:44032\n\t"
    "v_fma_f32 v7, v7, s62, v212\n\t"
    "v_mfma_f32_16x16x32_bf16 a[80:83], a[228:231], v[64:67], a[80:83]\n\t"
    "s_waitcnt lgkmcnt(8)\n\t"
    "buffer_load_dwordx4 v201, s[36:39], s52 offen offset:3200 lds\n\t"
    "v_fma_f32 v16, v16, s62, v213\n\t"
    "v_mfma_f32_16x16x32_bf16 a[96:99], a[232:235], v[64:67], a[96:99]\n\t"
    "ds_read_b64_tr_b16 a[224:225], v196 offset:26624\n\t"
    "v_fma_f32 v17, v17, s62, v213\n\t"
    "v_mfma_f32_16x16x32_bf16 a[100:103], a[232:235], v[80:83], a[100:103]\n\t"
    "ds_read_b128 v[248:251], v203 offset:9216\n\t"
    "v_fma_f32 v18, v18, s62, v213\n\t"
    "v_mfma_f32_16x16x32_bf16 v[240:243], v[244:247], v[120:123], v[240:243]\n\t"
    "v_exp_f32 v6, v6\n\t"
    "v_mfma_f32_16x16x32_bf16 a[104:107], a[232:235], v[96:99], a[104:107]\n\t"
    "ds_read_b64_tr_b16 a[226:227], v196 offset:32768\n\t"
    "v_fma_f32 v19, v19, s62, v213\n\t"
    "v_mfma_f32_16x16x32_bf16 a[108:111], a[232:235], v[112:115], a[108:111]\n\t"
    "v_exp_f32 v7, v7\n\t"
    "v_mfma_f32_16x16x32_bf16 a[124:127], a[236:239], v[112:115], a[124:127]\n\t"
    "s_waitcnt lgkmcnt(1)\n\t"
    "ds_read_b64_tr_b16 a[228:229], v197 offset:26624\n\t"
    "v_cvt_pk_bf16_f32 v3, v6, v7\n\t"
    "v_mfma_f32_16x16x32_bf16 a[120:123], a[236:239], v[96:99], a[120:123]\n\t"
    "buffer_store_dwordx4 v[248:251], v201, s[48:51], s70 offen offset:3328 nt\n\t"
    "v_mfma_f32_16x16x32_bf16 a[116:119], a[236:239], v[80:83], a[116:119]\n\t"
    "ds_read_b64_tr_b16 a[230:231], v197 offset:32768\n\t"
    "v_fma_f32 v20, v20, s62, v213\n\t"
    "v_mfma_f32_16x16x32_bf16 a[112:115], a[236:239], v[64:67], a[112:115]\n\t"
    "v_exp_f32 v16, v16\n\t"
    "v_mfma_f32_16x16x32_bf16 a[64:67], a[240:243], v[72:75], a[64:67]\n\t"
    "ds_read_b64_tr_b16 a[232:233], v198 offset:26624\n\t"
    "v_fma_f32 v21, v21, s62, v213\n\t"
    "v_mfma_f32_16x16x32_bf16 a[68:71], a[240:243], v[88:91], a[68:71]\n\t"
    "v_exp_f32 v17, v17\n\t"
    "v_mfma_f32_16x16x32_bf16 a[72:75], a[240:243], v[104:107], a[72:75]\n\t"
    "s_add_u32 m0, s56, 1792\n\t"
    "ds_read_b64_tr_b16 a[234:235], v198 offset:32768\n\t"
    "v_cvt_pk_bf16_f32 v16, v16, v17\n\t"
    "v_mfma_f32_16x16x32_bf16 a[76:79], a[240:243], v[120:123], a[76:79]\n\t"
    "buffer_load_dwordx4 v200, s[36:39], s52 offen offset:256 lds\n\t"
    "v_fma_f32 v22, v22, s62, v213\n\t"
    "v_mfma_f32_16x16x32_bf16 a[92:95], a[244:247], v[120:123], a[92:95]\n\t"
    "ds_read_b64_tr_b16 a[236:237], v199 offset:26624\n\t"
    "v_fma_f32 v23, v23, s62, v213\n\t"
    "v_mfma_f32_16x16x32_bf16 a[88:91], a[244:247], v[104:107], a[88:91]\n\t"
    "v_exp_f32 v18, v18\n\t"
    "v_mfma_f32_16x16x32_bf16 a[84:87], a[244:247], v[88:91], a[84:87]\n\t"
    "ds_read_b64_tr_b16 a[238:239], v199 offset:32768\n\t"
    "v_fma_f32 v32, v32, s62, v214\n\t"
    "v_mfma_f32_16x16x32_bf16 a[80:83], a[244:247], v[72:75], a[80:83]\n\t"
    "v_exp_f32 v19, v19\n\t"
    "v_mfma_f32_16x16x32_bf16 a[96:99], a[248:251], v[72:75], a[96:99]\n\t"
    "ds_read_b64_tr_b16 a[240:241], v196 offset:38912\n\t"
    "v_cvt_pk_bf16_f32 v17, v18, v19\n\t"
    "v_mfma_f32_16x16x32_bf16 a[100:103], a[248:251], v[88:91], a[100:103]\n\t"
    "v_exp_f32 v20, v20\n\t"
    "v_mfma_f32_16x16x32_bf16 a[104:107], a[248:251], v[104:107], a[104:107]\n\t"
    "ds_read_b64_tr_b16 a[242:243], v196 offset:45056\n\t"
    "v_fma_f32 v33, v33, s62, v214\n\t"
    "v_mfma_f32_16x16x32_bf16 a[108:111], a[248:251], v[120:123], a[108:111]\n\t"
    "v_exp_f32 v21, v21\n\t"
    "v_mfma_f32_16x16x32_bf16 a[124:127], a[252:255], v[120:123], a[124:127]\n\t"
    "ds_read_b64_tr_b16 a[244:245], v197 offset:38912\n\t"
    "v_cvt_pk_bf16_f32 v18, v20, v21\n\t"
    "v_mfma_f32_16x16x32_bf16 a[120:123], a[252:255], v[104:107], a[120:123]\n\t"
    "v_exp_f32 v22, v22\n\t"
    "v_mfma_f32_16x16x32_bf16 a[116:119], a[252:255], v[88:91], a[116:119]\n\t"
    "s_add_u32 m0, s56, 1792\n\t"
    "ds_read_b64_tr_b16 a[246:247], v197 offset:45056\n\t"
    "v_fma_f32 v34, v34, s62, v214\n\t"
    "v_mfma_f32_16x16x32_bf16 a[112:115], a[252:255], v[72:75], a[112:115]\n\t"
    "s_waitcnt lgkmcnt(8)\n\t"
    "buffer_load_dwordx4 v201, s[36:39], s52 offen offset:3328 lds\n\t"
    "v_fma_f32 v35, v35, s62, v214\n\t"
    "v_mfma_f32_16x16x32_bf16 v[128:131], a[224:227], v[64:67], v[128:131]\n\t"
    "ds_read_b64_tr_b16 a[248:249], v198 offset:38912\n\t"
    "v_fma_f32 v36, v36, s62, v214\n\t"
    "v_mfma_f32_16x16x32_bf16 v[132:135], a[224:227], v[80:83], v[132:135]\n\t"
    "v_exp_f32 v23, v23\n\t"
    "v_mfma_f32_16x16x32_bf16 v[136:139], a[224:227], v[96:99], v[136:139]\n\t"
    "ds_read_b64_tr_b16 a[250:251], v198 offset:45056\n\t"
    "v_cvt_pk_bf16_f32 v19, v22, v23\n\t"
    "v_mfma_f32_16x16x32_bf16 v[140:143], a[224:227], v[112:115], v[140:143]\n\t"
    "v_exp_f32 v32, v32\n\t"
    "v_mfma_f32_16x16x32_bf16 v[156:159], a[228:231], v[112:115], v[156:159]\n\t"
    "ds_read_b64_tr_b16 a[252:253], v199 offset:38912\n\t"
    "v_fma_f32 v37, v37, s62, v214\n\t"
    "v_mfma_f32_16x16x32_bf16 v[152:155], a[228:231], v[96:99], v[152:155]\n\t"
    "v_exp_f32 v33, v33\n\t"
    "v_mfma_f32_16x16x32_bf16 v[148:151], a[228:231], v[80:83], v[148:151]\n\t"
    "ds_read_b64_tr_b16 a[254:255], v199 offset:45056\n\t"
    "v_cvt_pk_bf16_f32 v32, v32, v33\n\t"
    "v_mfma_f32_16x16x32_bf16 v[144:147], a[228:231], v[64:67], v[144:147]\n\t"
    "s_waitcnt lgkmcnt(8)\n\t"
    "v_exp_f32 v34, v34\n\t"
    "v_mfma_f32_16x16x32_bf16 v[160:163], a[232:235], v[64:67], v[160:163]\n\t"
    "v_exp_f32 v35, v35\n\t"
    "v_mfma_f32_16x16x32_bf16 v[164:167], a[232:235], v[80:83], v[164:167]\n\t"
    "v_exp_f32 v36, v36\n\t"
    "v_mfma_f32_16x16x32_bf16 v[168:171], a[232:235], v[96:99], v[168:171]\n\t"
    "s_add_u32 s52, s52, 24576\n\t"
    "v_exp_f32 v37, v37\n\t"
    "v_mfma_f32_16x16x32_bf16 v[172:175], a[232:235], v[112:115], v[172:175]\n\t"
    "v_cvt_pk_bf16_f32 v33, v34, v35\n\t"
    "v_cvt_pk_bf16_f32 v34, v36, v37\n\t"
    "v_mfma_f32_16x16x32_bf16 v[188:191], a[236:239], v[112:115], v[188:191]\n\t"
    "ds_read_b128 v[248:251], v203 offset:10240\n\t"
    "v_fma_f32 v38, v38, s62, v214\n\t"
    "v_mfma_f32_16x16x32_bf16 v[184:187], a[236:239], v[96:99], v[184:187]\n\t"
    "v_exp_f32 v38, v38\n\t"
    "v_mfma_f32_16x16x32_bf16 v[180:183], a[236:239], v[80:83], v[180:183]\n\t"
    "s_waitcnt lgkmcnt(1) vmcnt(17)\n\t"
    "v_fma_f32 v39, v39, s62, v214\n\t"
    "v_fma_f32 v48, v48, s62, v215\n\t"
    "v_mfma_f32_16x16x32_bf16 v[176:179], a[236:239], v[64:67], v[176:179]\n\t"
    "v_exp_f32 v39, v39\n\t"
    "v_mfma_f32_16x16x32_bf16 v[128:131], a[240:243], v[72:75], v[128:131]\n\t"
    "s_barrier\n\t"
    "ds_read_b128 a[224:227], v192 offset:24576\n\t"
    "v_cvt_pk_bf16_f32 v35, v38, v39\n\t"
    "v_mfma_f32_16x16x32_bf16 v[132:135], a[240:243], v[88:91], v[132:135]\n\t"
    "s_waitcnt lgkmcnt(1)\n\t"
    "ds_read_b128 a[228:231], v192 offset:30720\n\t"
    "v_fma_f32 v49, v49, s62, v215\n\t"
    "v_mfma_f32_16x16x32_bf16 v[136:139], a[240:243], v[104:107], v[136:139]\n\t"
    "ds_read_b128 a[232:235], v192 offset:36864\n\t"
    "v_fma_f32 v50, v50, s62, v215\n\t"
    "v_mfma_f32_16x16x32_bf16 v[140:143], a[240:243], v[120:123], v[140:143]\n\t"
    "buffer_store_dwordx4 v[248:251], v200, s[48:51], s78 offen offset:256 nt\n\t"
    "v_mfma_f32_16x16x32_bf16 v[156:159], a[244:247], v[120:123], v[156:159]\n\t"
    "v_exp_f32 v48, v48\n\t"
    "v_mfma_f32_16x16x32_bf16 v[152:155], a[244:247], v[104:107], v[152:155]\n\t"
    "v_exp_f32 v49, v49\n\t"
    "v_mfma_f32_16x16x32_bf16 v[148:151], a[244:247], v[88:91], v[148:151]\n\t"
    "ds_read_b128 a[236:239], v192 offset:43008\n\t"
    "v_cvt_pk_bf16_f32 v48, v48, v49\n\t"
    "v_mfma_f32_16x16x32_bf16 v[144:147], a[244:247], v[72:75], v[144:147]\n\t"
    "v_exp_f32 v50, v50\n\t"
    "v_mfma_f32_16x16x32_bf16 v[160:163], a[248:251], v[72:75], v[160:163]\n\t"
    "v_fma_f32 v51, v51, s62, v215\n\t"
    "v_fma_f32 v52, v52, s62, v215\n\t"
    "v_mfma_f32_16x16x32_bf16 v[164:167], a[248:251], v[88:91], v[164:167]\n\t"
    "v_exp_f32 v51, v51\n\t"
    "v_mfma_f32_16x16x32_bf16 v[168:171], a[248:251], v[104:107], v[168:171]\n\t"
    "ds_read_b128 a[240:243], v193 offset:24576\n\t"
    "v_cvt_pk_bf16_f32 v49, v50, v51\n\t"
    "v_mfma_f32_16x16x32_bf16 v[172:175], a[248:251], v[120:123], v[172:175]\n\t"
    "v_exp_f32 v52, v52\n\t"
    "v_mfma_f32_16x16x32_bf16 v[188:191], a[252:255], v[120:123], v[188:191]\n\t"
    "v_fma_f32 v53, v53, s62, v215\n\t"
    "v_fma_f32 v54, v54, s62, v215\n\t"
    "v_mfma_f32_16x16x32_bf16 v[184:187], a[252:255], v[104:107], v[184:187]\n\t"
    "v_exp_f32 v53, v53\n\t"
    "v_mfma_f32_16x16x32_bf16 v[180:183], a[252:255], v[88:91], v[180:183]\n\t"
    "ds_read_b128 a[244:247], v193 offset:30720\n\t"
    "v_cvt_pk_bf16_f32 v50, v52, v53\n\t"
    "v_mfma_f32_16x16x32_bf16 v[176:179], a[252:255], v[72:75], v[176:179]\n\t"
    "s_waitcnt lgkmcnt(4)\n\t"
    "v_exp_f32 v54, v54\n\t"
    "v_mfma_f32_16x16x32_bf16 v[64:67], a[224:227], a[128:131], v[224:227]\n\t"
    "s_cmp_lg_u32 s72, 0\n\t"
    "s_cbranch_scc1 SB2\n\t"
    "NSB2:\n\t"
    "v_fma_f32 v55, v55, s62, v215\n\t"
    "v_mfma_f32_16x16x32_bf16 v[80:83], a[224:227], a[132:135], v[224:227]\n\t"
    "v_exp_f32 v55, v55\n\t"
    "v_mfma_f32_16x16x32_bf16 v[96:99], a[224:227], a[136:139], v[224:227]\n\t"
    "s_add_u32 m0, s57, 24576\n\t"
    "ds_read_b128 a[248:251], v193 offset:36864\n\t"
    "v_cvt_pk_bf16_f32 v51, v54, v55\n\t"
    "v_mfma_f32_16x16x32_bf16 v[112:115], a[224:227], a[140:143], v[224:227]\n\t"
    "buffer_load_dwordx4 v202, s[40:43], s53 offen offset:0 lds\n\t"
    "v_fma_f32 v8, v8, s62, v212\n\t"
    "v_mfma_f32_16x16x32_bf16 v[116:119], a[228:231], a[140:143], v[224:227]\n\t"
    "v_exp_f32 v8, v8\n\t"
    "v_mfma_f32_16x16x32_bf16 v[100:103], a[228:231], a[136:139], v[224:227]\n\t"
    "v_fma_f32 v9, v9, s62, v212\n\t"
    "v_fma_f32 v10, v10, s62, v212\n\t"
    "v_mfma_f32_16x16x32_bf16 v[84:87], a[228:231], a[132:135], v[224:227]\n\t"
    "ds_read_b128 a[252:255], v193 offset:43008\n\t"
    "v_fma_f32 v11, v11, s62, v212\n\t"
    "v_mfma_f32_16x16x32_bf16 v[68:71], a[228:231], a[128:131], v[224:227]\n\t"
    "s_waitcnt lgkmcnt(4)\n\t"
    "v_exp_f32 v9, v9\n\t"
    "v_mfma_f32_16x16x32_bf16 v[72:75], a[232:235], a[128:131], v[224:227]\n\t"
    "v_exp_f32 v10, v10\n\t"
    "v_mfma_f32_16x16x32_bf16 v[88:91], a[232:235], a[132:135], v[224:227]\n\t"
    "v_exp_f32 v11, v11\n\t"
    "v_mfma_f32_16x16x32_bf16 v[104:107], a[232:235], a[136:139], v[224:227]\n\t"
    "ds_read_b128 a[224:227], v192 offset:25600\n\t"
    "v_cvt_pk_bf16_f32 v8, v8, v9\n\t"
    "v_mfma_f32_16x16x32_bf16 v[120:123], a[232:235], a[140:143], v[224:227]\n\t"
    "v_cvt_pk_bf16_f32 v9, v10, v11\n\t"
    "v_fma_f32 v12, v12, s62, v212\n\t"
    "v_mfma_f32_16x16x32_bf16 v[124:127], a[236:239], a[140:143], v[224:227]\n\t"
    "v_exp_f32 v12, v12\n\t"
    "v_mfma_f32_16x16x32_bf16 v[108:111], a[236:239], a[136:139], v[224:227]\n\t"
    "v_fma_f32 v13, v13, s62, v212\n\t"
    "v_fma_f32 v14, v14, s62, v212\n\t"
    "v_mfma_f32_16x16x32_bf16 v[92:95], a[236:239], a[132:135], v[224:227]\n\t"
    "s_add_u32 m0, s57, 24576\n\t"
    "ds_read_b128 a[228:231], v192 offset:31744\n\t"
    "v_fma_f32 v15, v15, s62, v212\n\t"
    "v_mfma_f32_16x16x32_bf16 v[76:79], a[236:239], a[128:131], v[224:227]\n\t"
    "s_waitcnt lgkmcnt(4)\n\t"
    "buffer_load_dwordx4 v202, s[40:43], s53 offen offset:3072 lds\n\t"
    "v_fma_f32 v24, v24, s62, v213\n\t"
    "v_mfma_f32_16x16x32_bf16 v[64:67], a[240:243], a[144:147], v[64:67]\n\t"
    "ds_read_b128 v[248:251], v203 offset:11264\n\t"
    "v_fma_f32 v25, v25, s62, v213\n\t"
    "v_mfma_f32_16x16x32_bf16 v[80:83], a[240:243], a[148:151], v[80:83]\n\t"
    "v_exp_f32 v13, v13\n\t"
    "v_mfma_f32_16x16x32_bf16 v[96:99], a[240:243], a[152:155], v[96:99]\n\t"
    "ds_read_b128 a[232:235], v192 offset:37888\n\t"
    "v_cvt_pk_bf16_f32 v10, v12, v13\n\t"
    "v_mfma_f32_16x16x32_bf16 v[112:115], a[240:243], a[156:159], v[112:115]\n\t"
    "v_exp_f32 v14, v14\n\t"
    "v_mfma_f32_16x16x32_bf16 v[116:119], a[244:247], a[156:159], v[116:119]\n\t"
    "s_waitcnt lgkmcnt(1)\n\t"
    "buffer_store_dwordx4 v[248:251], v201, s[48:51], s78 offen offset:3328 nt\n\t"
    "v_mfma_f32_16x16x32_bf16 v[100:103], a[244:247], a[152:155], v[100:103]\n\t"
    "v_exp_f32 v15, v15\n\t"
    "v_mfma_f32_16x16x32_bf16 v[84:87], a[244:247], a[148:151], v[84:87]\n\t"
    "ds_read_b128 a[236:239], v192 offset:44032\n\t"
    "v_cvt_pk_bf16_f32 v11, v14, v15\n\t"
    "v_mfma_f32_16x16x32_bf16 v[68:71], a[244:247], a[144:147], v[68:71]\n\t"
    "v_exp_f32 v24, v24\n\t"
    "v_mfma_f32_16x16x32_bf16 v[72:75], a[248:251], a[144:147], v[72:75]\n\t"
    "v_exp_f32 v25, v25\n\t"
    "v_mfma_f32_16x16x32_bf16 v[88:91], a[248:251], a[148:151], v[88:91]\n\t"
    "v_cvt_pk_bf16_f32 v24, v24, v25\n\t"
    "v_fma_f32 v26, v26, s62, v213\n\t"
    "v_mfma_f32_16x16x32_bf16 v[104:107], a[248:251], a[152:155], v[104:107]\n\t"
    "s_add_u32 m0, s57, 25472\n\t"
    "ds_read_b128 a[240:243], v193 offset:25600\n\t"
    "v_fma_f32 v27, v27, s62, v213\n\t"
    "v_mfma_f32_16x16x32_bf16 v[120:123], a[248:251], a[156:159], v[120:123]\n\t"
    "buffer_load_dwordx4 v202, s[40:43], s53 offen offset:128 lds\n\t"
    "v_fma_f32 v28, v28, s62, v213\n\t"
    "v_mfma_f32_16x16x32_bf16 v[124:127], a[252:255], a[156:159], v[124:127]\n\t"
    "v_exp_f32 v26, v26\n\t"
    "v_mfma_f32_16x16x32_bf16 v[108:111], a[252:255], a[152:155], v[108:111]\n\t"
    "v_exp_f32 v27, v27\n\t"
    "v_mfma_f32_16x16x32_bf16 v[92:95], a[252:255], a[148:151], v[92:95]\n\t"
    "ds_read_b128 a[244:247], v193 offset:31744\n\t"
    "v_cvt_pk_bf16_f32 v25, v26, v27\n\t"
    "v_mfma_f32_16x16x32_bf16 v[76:79], a[252:255], a[144:147], v[76:79]\n\t"
    "v_exp_f32 v28, v28\n\t"
    "v_mfma_f32_16x16x32_bf16 v[64:67], a[224:227], a[160:163], v[64:67]\n\t"
    "v_fma_f32 v29, v29, s62, v213\n\t"
    "v_fma_f32 v30, v30, s62, v213\n\t"
    "v_mfma_f32_16x16x32_bf16 v[80:83], a[224:227], a[164:167], v[80:83]\n\t"
    "v_exp_f32 v29, v29\n\t"
    "v_mfma_f32_16x16x32_bf16 v[96:99], a[224:227], a[168:171], v[96:99]\n\t"
    "ds_read_b128 a[248:251], v193 offset:37888\n\t"
    "v_cvt_pk_bf16_f32 v26, v28, v29\n\t"
    "v_mfma_f32_16x16x32_bf16 v[112:115], a[224:227], a[172:175], v[112:115]\n\t"
    "v_exp_f32 v30, v30\n\t"
    "v_mfma_f32_16x16x32_bf16 v[116:119], a[228:231], a[172:175], v[116:119]\n\t"
    "v_fma_f32 v31, v31, s62, v213\n\t"
    "v_fma_f32 v40, v40, s62, v214\n\t"
    "v_mfma_f32_16x16x32_bf16 v[100:103], a[228:231], a[168:171], v[100:103]\n\t"
    "v_exp_f32 v31, v31\n\t"
    "v_mfma_f32_16x16x32_bf16 v[84:87], a[228:231], a[164:167], v[84:87]\n\t"
    "s_add_u32 m0, s57, 25472\n\t"
    "ds_read_b128 a[252:255], v193 offset:44032\n\t"
    "v_cvt_pk_bf16_f32 v27, v30, v31\n\t"
    "v_mfma_f32_16x16x32_bf16 v[68:71], a[228:231], a[160:163], v[68:71]\n\t"
    "s_waitcnt lgkmcnt(4)\n\t"
    "buffer_load_dwordx4 v202, s[40:43], s53 offen offset:3200 lds\n\t"
    "v_fma_f32 v41, v41, s62, v214\n\t"
    "v_mfma_f32_16x16x32_bf16 v[72:75], a[232:235], a[160:163], v[72:75]\n\t"
    "v_exp_f32 v40, v40\n\t"
    "v_mfma_f32_16x16x32_bf16 v[88:91], a[232:235], a[164:167], v[88:91]\n\t"
    "v_exp_f32 v41, v41\n\t"
    "v_mfma_f32_16x16x32_bf16 v[104:107], a[232:235], a[168:171], v[104:107]\n\t"
    "ds_read_b128 a[224:227], v192 offset:26624\n\t"
    "v_cvt_pk_bf16_f32 v40, v40, v41\n\t"
    "v_mfma_f32_16x16x32_bf16 v[120:123], a[232:235], a[172:175], v[120:123]\n\t"
    "v_fma_f32 v42, v42, s62, v214\n\t"
    "v_fma_f32 v43, v43, s62, v214\n\t"
    "v_mfma_f32_16x16x32_bf16 v[124:127], a[236:239], a[172:175], v[124:127]\n\t"
    "s_add_u32 m0, s58, 0\n\t"
    "v_exp_f32 v42, v42\n\t"
    "v_mfma_f32_16x16x32_bf16 v[108:111], a[236:239], a[168:171], v[108:111]\n\t"
    "s_add_u32 s75, s54, s96\n\t"
    "buffer_load_dwordx4 v200, s[44:47], s75 offen offset:0 nt lds\n\t"
    "v_fma_f32 v44, v44, s62, v214\n\t"
    "v_mfma_f32_16x16x32_bf16 v[92:95], a[236:239], a[164:167], v[92:95]\n\t"
    "ds_read_b128 a[228:231], v192 offset:32768\n\t"
    "v_fma_f32 v45, v45, s62, v214\n\t"
    "v_mfma_f32_16x16x32_bf16 v[76:79], a[236:239], a[160:163], v[76:79]\n\t"
    "s_waitcnt lgkmcnt(4)\n\t"
    "v_exp_f32 v43, v43\n\t"
    "v_mfma_f32_16x16x32_bf16 v[64:67], a[240:243], a[176:179], v[64:67]\n\t"
    "v_exp_f32 v44, v44\n\t"
    "v_mfma_f32_16x16x32_bf16 v[80:83], a[240:243], a[180:183], v[80:83]\n\t"
    "v_exp_f32 v45, v45\n\t"
    "v_mfma_f32_16x16x32_bf16 v[96:99], a[240:243], a[184:187], v[96:99]\n\t"
    "s_add_u32 m0, s57, 26368\n\t"
    "ds_read_b128 a[232:235], v192 offset:38912\n\t"
    "v_cvt_pk_bf16_f32 v41, v42, v43\n\t"
    "v_mfma_f32_16x16x32_bf16 v[112:115], a[240:243], a[188:191], v[112:115]\n\t"
    "buffer_load_dwordx4 v202, s[40:43], s53 offen offset:256 lds\n\t"
    "v_cvt_pk_bf16_f32 v42, v44, v45\n\t"
    "v_mfma_f32_16x16x32_bf16 v[116:119], a[244:247], a[188:191], v[116:119]\n\t"
    "ds_read_b128 v[248:251], v203 offset:12288\n\t"
    "v_fma_f32 v46, v46, s62, v214\n\t"
    "v_mfma_f32_16x16x32_bf16 v[100:103], a[244:247], a[184:187], v[100:103]\n\t"
    "v_exp_f32 v46, v46\n\t"
    "v_mfma_f32_16x16x32_bf16 v[84:87], a[244:247], a[180:183], v[84:87]\n\t"
    "ds_read_b128 a[236:239], v192 offset:45056\n\t"
    "v_fma_f32 v47, v47, s62, v214\n\t"
    "v_mfma_f32_16x16x32_bf16 v[68:71], a[244:247], a[176:179], v[68:71]\n\t"
    "s_waitcnt lgkmcnt(5)\n\t"
    "v_exp_f32 v47, v47\n\t"
    "v_mfma_f32_16x16x32_bf16 v[72:75], a[248:251], a[176:179], v[72:75]\n\t"
    "s_waitcnt lgkmcnt(1)\n\t"
    "buffer_store_dwordx4 v[248:251], v200, s[48:51], s79 offen offset:256 nt\n\t"
    "v_mfma_f32_16x16x32_bf16 v[88:91], a[248:251], a[180:183], v[88:91]\n\t"
    "v_cvt_pk_bf16_f32 v43, v46, v47\n\t"
    "v_fma_f32 v56, v56, s62, v215\n\t"
    "v_mfma_f32_16x16x32_bf16 v[104:107], a[248:251], a[184:187], v[104:107]\n\t"
    "ds_read_b128 a[240:243], v193 offset:26624\n\t"
    "v_fma_f32 v57, v57, s62, v215\n\t"
    "v_mfma_f32_16x16x32_bf16 v[120:123], a[248:251], a[188:191], v[120:123]\n\t"
    "v_exp_f32 v56, v56\n\t"
    "v_mfma_f32_16x16x32_bf16 v[124:127], a[252:255], a[188:191], v[124:127]\n\t"
    "v_exp_f32 v57, v57\n\t"
    "v_mfma_f32_16x16x32_bf16 v[108:111], a[252:255], a[184:187], v[108:111]\n\t"
    "v_cvt_pk_bf16_f32 v56, v56, v57\n\t"
    "v_fma_f32 v58, v58, s62, v215\n\t"
    "v_mfma_f32_16x16x32_bf16 v[92:95], a[252:255], a[180:183], v[92:95]\n\t"
    "s_add_u32 m0, s57, 26368\n\t"
    "ds_read_b128 a[244:247], v193 offset:32768\n\t"
    "v_fma_f32 v59, v59, s62, v215\n\t"
    "v_mfma_f32_16x16x32_bf16 v[76:79], a[252:255], a[176:179], v[76:79]\n\t"
    "buffer_load_dwordx4 v202, s[40:43], s53 offen offset:3328 lds\n\t"
    "v_fma_f32 v60, v60, s62, v215\n\t"
    "v_mfma_f32_16x16x32_bf16 v[64:67], a[224:227], a[192:195], v[64:67]\n\t"
    "v_exp_f32 v58, v58\n\t"
    "v_mfma_f32_16x16x32_bf16 v[80:83], a[224:227], a[196:199], v[80:83]\n\t"
    "v_exp_f32 v59, v59\n\t"
    "v_mfma_f32_16x16x32_bf16 v[96:99], a[224:227], a[200:203], v[96:99]\n\t"
    "ds_read_b128 a[248:251], v193 offset:38912\n\t"
    "v_cvt_pk_bf16_f32 v57, v58, v59\n\t"
    "v_mfma_f32_16x16x32_bf16 v[112:115], a[224:227], a[204:207], v[112:115]\n\t"
    "v_exp_f32 v60, v60\n\t"
    "v_mfma_f32_16x16x32_bf16 v[116:119], a[228:231], a[204:207], v[116:119]\n\t"
    "v_fma_f32 v61, v61, s62, v215\n\t"
    "v_fma_f32 v62, v62, s62, v215\n\t"
    "v_mfma_f32_16x16x32_bf16 v[100:103], a[228:231], a[200:203], v[100:103]\n\t"
    "v_exp_f32 v61, v61\n\t"
    "v_mfma_f32_16x16x32_bf16 v[84:87], a[228:231], a[196:199], v[84:87]\n\t"
    "ds_read_b128 a[252:255], v193 offset:45056\n\t"
    "v_cvt_pk_bf16_f32 v58, v60, v61\n\t"
    "v_mfma_f32_16x16x32_bf16 v[68:71], a[228:231], a[192:195], v[68:71]\n\t"
    "s_waitcnt lgkmcnt(4)\n\t"
    "v_exp_f32 v62, v62\n\t"
    "v_mfma_f32_16x16x32_bf16 v[72:75], a[232:235], a[192:195], v[72:75]\n\t"
    "v_fma_f32 v63, v63, s62, v215\n\t"
    "v_mfma_f32_16x16x32_bf16 v[88:91], a[232:235], a[196:199], v[88:91]\n\t"
    "v_exp_f32 v63, v63\n\t"
    "v_mfma_f32_16x16x32_bf16 v[104:107], a[232:235], a[200:203], v[104:107]\n\t"
    "s_add_u32 s53, s53, 24576\n\t"
    "v_cvt_pk_bf16_f32 v59, v62, v63\n\t"
    "v_mfma_f32_16x16x32_bf16 v[120:123], a[232:235], a[204:207], v[120:123]\n\t"
    "v_mfma_f32_16x16x32_bf16 v[124:127], a[236:239], a[204:207], v[124:127]\n\t"
    "v_mfma_f32_16x16x32_bf16 v[108:111], a[236:239], a[200:203], v[108:111]\n\t"
    "v_mfma_f32_16x16x32_bf16 v[92:95], a[236:239], a[196:199], v[92:95]\n\t"
    "s_waitcnt lgkmcnt(0) vmcnt(19)\n\t"
    "v_mfma_f32_16x16x32_bf16 v[76:79], a[236:239], a[192:195], v[76:79]\n\t"
    "v_mfma_f32_16x16x32_bf16 v[64:67], a[240:243], a[208:211], v[64:67]\n\t"
    "s_barrier\n\t"
    "ds_read_b64_tr_b16 a[224:225], v196 offset:0\n\t"
    "v_mfma_f32_16x16x32_bf16 v[80:83], a[240:243], a[212:215], v[80:83]\n\t"
    "s_add_u32 m0, s58, 4294965248\n\t"
    "ds_read_b64_tr_b16 a[226:227], v196 offset:6144\n\t"
    "v_mfma_f32_16x16x32_bf16 v[96:99], a[240:243], a[216:219], v[96:99]\n\t"
    "s_add_u32 s75, s54, s96\n\t"
    "ds_read_b64_tr_b16 a[228:229], v197 offset:0\n\t"
    "v_mfma_f32_16x16x32_bf16 v[112:115], a[240:243], a[220:223], v[112:115]\n\t"
    "ds_read_b64_tr_b16 a[230:231], v197 offset:6144\n\t"
    "v_mfma_f32_16x16x32_bf16 v[116:119], a[244:247], a[220:223], v[116:119]\n\t"
    "ds_read_b64_tr_b16 a[232:233], v198 offset:0\n\t"
    "v_mfma_f32_16x16x32_bf16 v[100:103], a[244:247], a[216:219], v[100:103]\n\t"
    "ds_read_b64_tr_b16 a[234:235], v198 offset:6144\n\t"
    "v_mfma_f32_16x16x32_bf16 v[84:87], a[244:247], a[212:215], v[84:87]\n\t"
    "ds_read_b64_tr_b16 a[236:237], v199 offset:0\n\t"
    "v_mfma_f32_16x16x32_bf16 v[68:71], a[244:247], a[208:211], v[68:71]\n\t"
    "ds_read_b64_tr_b16 a[238:239], v199 offset:6144\n\t"
    "v_mfma_f32_16x16x32_bf16 v[72:75], a[248:251], a[208:211], v[72:75]\n\t"
    "ds_read_b64_tr_b16 a[240:241], v196 offset:12288\n\t"
    "v_mfma_f32_16x16x32_bf16 v[88:91], a[248:251], a[212:215], v[88:91]\n\t"
    "buffer_load_dwordx4 v201, s[44:47], s75 offen offset:3072 nt lds\n\t"
    "v_mfma_f32_16x16x32_bf16 v[104:107], a[248:251], a[216:219], v[104:107]\n\t"
    "ds_read_b64_tr_b16 a[242:243], v196 offset:18432\n\t"
    "v_mfma_f32_16x16x32_bf16 v[120:123], a[248:251], a[220:223], v[120:123]\n\t"
    "ds_read_b128 v[248:251], v203 offset:13312\n\t"
    "v_mfma_f32_16x16x32_bf16 v[124:127], a[252:255], a[220:223], v[124:127]\n\t"
    "s_waitcnt lgkmcnt(0)\n\t"
    "ds_read_b64_tr_b16 a[244:245], v197 offset:12288\n\t"
    "v_mfma_f32_16x16x32_bf16 v[108:111], a[252:255], a[216:219], v[108:111]\n\t"
    "buffer_store_dwordx4 v[248:251], v201, s[48:51], s79 offen offset:3328 nt\n\t"
    "v_mfma_f32_16x16x32_bf16 v[92:95], a[252:255], a[212:215], v[92:95]\n\t"
    "ds_read_b64_tr_b16 a[246:247], v197 offset:18432\n\t"
    "v_mfma_f32_16x16x32_bf16 v[76:79], a[252:255], a[208:211], v[76:79]\n\t"
    "v_mfma_f32_16x16x32_bf16 a[0:3], a[224:227], v[0:3], a[0:3]\n\t"
    "ds_read_b64_tr_b16 a[248:249], v198 offset:12288\n\t"
    "v_mfma_f32_16x16x32_bf16 a[4:7], a[224:227], v[16:19], a[4:7]\n\t"
    "v_mfma_f32_16x16x32_bf16 a[8:11], a[224:227], v[32:35], a[8:11]\n\t"
    "s_add_u32 m0, s56, 24576\n\t"
    "ds_read_b64_tr_b16 a[250:251], v198 offset:18432\n\t"
    "v_mfma_f32_16x16x32_bf16 a[12:15], a[224:227], v[48:51], a[12:15]\n\t"
    "buffer_load_dwordx4 v200, s[36:39], s52 offen offset:0 lds\n\t"
    "v_mfma_f32_16x16x32_bf16 a[28:31], a[228:231], v[48:51], a[28:31]\n\t"
    "ds_read_b64_tr_b16 a[252:253], v199 offset:12288\n\t"
    "v_max3_f32 v220, v64, v65, v66\n\t"
    "v_mfma_f32_16x16x32_bf16 a[24:27], a[228:231], v[32:35], a[24:27]\n\t"
    "v_max3_f32 v221, v80, v81, v82\n\t"
    "v_max3_f32 v222, v96, v97, v98\n\t"
    "v_mfma_f32_16x16x32_bf16 a[20:23], a[228:231], v[16:19], a[20:23]\n\t"
    "ds_read_b64_tr_b16 a[254:255], v199 offset:18432\n\t"
    "v_max3_f32 v223, v112, v113, v114\n\t"
    "v_mfma_f32_16x16x32_bf16 a[16:19], a[228:231], v[0:3], a[16:19]\n\t"
    "v_max3_f32 v220, v220, v67, v68\n\t"
    "v_max3_f32 v221, v221, v83, v84\n\t"
    "v_mfma_f32_16x16x32_bf16 a[32:35], a[232:235], v[0:3], a[32:35]\n\t"
    "ds_read_b64_tr_b16 a[224:225], v196 offset:1024\n\t"
    "v_max3_f32 v222, v222, v99, v100\n\t"
    "v_mfma_f32_16x16x32_bf16 a[36:39], a[232:235], v[16:19], a[36:39]\n\t"
    "v_max3_f32 v223, v223, v115, v116\n\t"
    "v_max3_f32 v220, v220, v69, v70\n\t"
    "v_mfma_f32_16x16x32_bf16 v[228:231], v[244:247], v[0:3], v[228:231]\n\t"
    "v_max3_f32 v221, v221, v85, v86\n\t"
    "v_max3_f32 v222, v222, v101, v102\n\t"
    "v_mfma_f32_16x16x32_bf16 a[40:43], a[232:235], v[32:35], a[40:43]\n\t"
    "ds_read_b64_tr_b16 a[226:227], v196 offset:7168\n\t"
    "v_max3_f32 v223, v223, v117, v118\n\t"
    "v_mfma_f32_16x16x32_bf16 a[44:47], a[232:235], v[48:51], a[44:47]\n\t"
    "v_max3_f32 v220, v220, v71, v72\n\t"
    "v_max3_f32 v221, v221, v87, v88\n\t"
    "v_mfma_f32_16x16x32_bf16 a[60:63], a[236:239], v[48:51], a[60:63]\n\t"
    "ds_read_b64_tr_b16 a[228:229], v197 offset:1024\n\t"
    "v_max3_f32 v222, v222, v103, v104\n\t"
    "v_mfma_f32_16x16x32_bf16 a[56:59], a[236:239], v[32:35], a[56:59]\n\t"
    "v_max3_f32 v223, v223, v119, v120\n\t"
    "v_max3_f32 v220, v220, v73, v74\n\t"
    "v_mfma_f32_16x16x32_bf16 v[232:235], v[244:247], v[16:19], v[232:235]\n\t"
    "v_max3_f32 v221, v221, v89, v90\n\t"
    "v_max3_f32 v222, v222, v105, v106\n\t"
    "v_mfma_f32_16x16x32_bf16 a[52:55], a[236:239], v[16:19], a[52:55]\n\t"
    "s_add_u32 m0, s56, 24576\n\t"
    "ds_read_b64_tr_b16 a[230:231], v197 offset:7168\n\t"
    "v_max3_f32 v223, v223, v121, v122\n\t"
    "v_mfma_f32_16x16x32_bf16 a[48:51], a[236:239], v[0:3], a[48:51]\n\t"
    "s_waitcnt lgkmcnt(8)\n\t"
    "buffer_load_dwordx4 v201, s[36:39], s52 offen offset:3072 lds\n\t"
    "v_max3_f32 v220, v220, v75, v76\n\t"
    "v_mfma_f32_16x16x32_bf16 a[0:3], a[240:243], v[8:11], a[0:3]\n\t"
    "ds_read_b64_tr_b16 a[232:233], v198 offset:1024\n\t"
    "v_max3_f32 v221, v221, v91, v92\n\t"
    "v_mfma_f32_16x16x32_bf16 a[4:7], a[240:243], v[24:27], a[4:7]\n\t"
    "v_max3_f32 v222, v222, v107, v108\n\t"
    "v_max3_f32 v223, v223, v123, v124\n\t"
    "v_mfma_f32_16x16x32_bf16 v[236:239], v[244:247], v[32:35], v[236:239]\n\t"
    "v_max3_f32 v220, v220, v77, v78\n\t"
    "v_max3_f32 v221, v221, v93, v94\n\t"
    "v_mfma_f32_16x16x32_bf16 a[8:11], a[240:243], v[40:43], a[8:11]\n\t"
    "ds_read_b64_tr_b16 a[234:235], v198 offset:7168\n\t"
    "v_max3_f32 v222, v222, v109, v110\n\t"
    "v_mfma_f32_16x16x32_bf16 a[12:15], a[240:243], v[56:59], a[12:15]\n\t"
    "v_max3_f32 v223, v223, v125, v126\n\t"
    "v_max_f32 v220, v220, v79\n\t"
    "v_mfma_f32_16x16x32_bf16 a[28:31], a[244:247], v[56:59], a[28:31]\n\t"
    "ds_read_b64_tr_b16 a[236:237], v199 offset:1024\n\t"
    "v_max_f32 v221, v221, v95\n\t"
    "v_mfma_f32_16x16x32_bf16 a[24:27], a[244:247], v[40:43], a[24:27]\n\t"
    "v_max_f32 v222, v222, v111\n\t"
    "v_max_f32 v223, v223, v127\n\t"
    "v_mfma_f32_16x16x32_bf16 v[240:243], v[244:247], v[48:51], v[240:243]\n\t"
    "v_cmp_gt_f32 vcc, v220, v216\n\t"
    "v_cmp_gt_f32 s[88:89], v221, v217\n\t"
    "v_mfma_f32_16x16x32_bf16 a[20:23], a[244:247], v[24:27], a[20:23]\n\t"
    "s_or_b64 vcc, vcc, s[88:89]\n\t"
    "ds_read_b64_tr_b16 a[238:239], v199 offset:7168\n\t"
    "v_cmp_gt_f32 s[90:91], v222, v218\n\t"
    "v_mfma_f32_16x16x32_bf16 a[16:19], a[244:247], v[8:11], a[16:19]\n\t"
    "s_waitcnt lgkmcnt(8)\n\t"
    "v_cmp_gt_f32 s[92:93], v223, v219\n\t"
    "s_or_b64 s[88:89], s[90:91], s[92:93]\n\t"
    "v_mfma_f32_16x16x32_bf16 a[32:35], a[248:251], v[8:11], a[32:35]\n\t"
    "s_or_b64 vcc, vcc, s[88:89]\n\t"
    "s_cbranch_scc1 SA3\n\t"
    "NSA3:\n\t"
    "ds_read_b64_tr_b16 a[240:241], v196 offset:13312\n\t"
    "v_mfma_f32_16x16x32_bf16 a[36:39], a[248:251], v[24:27], a[36:39]\n\t"
    "v_fma_f32 v64, v64, s62, v212\n\t"
    "v_fma_f32 v65, v65, s62, v212\n\t"
    "v_mfma_f32_16x16x32_bf16 a[40:43], a[248:251], v[40:43], a[40:43]\n\t"
    "s_add_u32 m0, s56, 25472\n\t"
    "ds_read_b64_tr_b16 a[242:243], v196 offset:19456\n\t"
    "v_fma_f32 v66, v66, s62, v212\n\t"
    "v_mfma_f32_16x16x32_bf16 a[44:47], a[248:251], v[56:59], a[44:47]\n\t"
    "buffer_load_dwordx4 v200, s[36:39], s52 offen offset:128 lds\n\t"
    "v_fma_f32 v67, v67, s62, v212\n\t"
    "v_mfma_f32_16x16x32_bf16 a[60:63], a[252:255], v[56:59], a[60:63]\n\t"
    "ds_read_b64_tr_b16 a[244:245], v197 offset:13312\n\t"
    "v_fma_f32 v68, v68, s62, v212\n\t"
    "v_mfma_f32_16x16x32_bf16 a[56:59], a[252:255], v[40:43], a[56:59]\n\t"
    "ds_read_b128 v[248:251], v203 offset:14336\n\t"
    "v_fma_f32 v69, v69, s62, v212\n\t"
    "v_mfma_f32_16x16x32_bf16 v[228:231], v[244:247], v[8:11], v[228:231]\n\t"
    "v_exp_f32 v64, v64\n\t"
    "v_mfma_f32_16x16x32_bf16 a[52:55], a[252:255], v[24:27], a[52:55]\n\t"
    "ds_read_b64_tr_b16 a[246:247], v197 offset:19456\n\t"
    "v_fma_f32 v70, v70, s62, v212\n\t"
    "v_mfma_f32_16x16x32_bf16 a[48:51], a[252:255], v[8:11], a[48:51]\n\t"
    "s_waitcnt lgkmcnt(9)\n\t"
    "v_exp_f32 v65, v65\n\t"
    "v_mfma_f32_16x16x32_bf16 a[64:67], a[224:227], v[0:3], a[64:67]\n\t"
    "s_waitcnt lgkmcnt(1)\n\t"
    "ds_read_b64_tr_b16 a[248:249], v198 offset:13312\n\t"
    "v_cvt_pk_bf16_f32 v64, v64, v65\n\t"
    "v_mfma_f32_16x16x32_bf16 a[68:71], a[224:227], v[16:19], a[68:71]\n\t"
    "buffer_store_dwordx4 v[248:251], v200, s[48:51], s80 offen offset:256 nt\n\t"
    "v_mfma_f32_16x16x32_bf16 v[232:235], v[244:247], v[24:27], v[232:235]\n\t"
    "v_exp_f32 v66, v66\n\t"
    "v_mfma_f32_16x16x32_bf16 a[72:75], a[224:227], v[32:35], a[72:75]\n\t"
    "ds_read_b64_tr_b16 a[250:251], v198 offset:19456\n\t"
    "v_fma_f32 v71, v71, s62, v212\n\t"
    "v_mfma_f32_16x16x32_bf16 a[76:79], a[224:227], v[48:51], a[76:79]\n\t"
    "v_exp_f32 v67, v67\n\t"
    "v_mfma_f32_16x16x32_bf16 a[92:95], a[228:231], v[48:51], a[92:95]\n\t"
    "ds_read_b64_tr_b16 a[252:253], v199 offset:13312\n\t"
    "v_cvt_pk_bf16_f32 v65, v66, v67\n\t"
    "v_mfma_f32_16x16x32_bf16 a[88:91], a[228:231], v[32:35], a[88:91]\n\t"
    "v_exp_f32 v68, v68\n\t"
    "v_mfma_f32_16x16x32_bf16 v[236:239], v[244:247], v[40:43], v[236:239]\n\t"
    "v_exp_f32 v69, v69\n\t"
    "v_mfma_f32_16x16x32_bf16 a[84:87], a[228:231], v[16:19], a[84:87]\n\t"
    "s_add_u32 m0, s56, 25472\n\t"
    "ds_read_b64_tr_b16 a[254:255], v199 offset:19456\n\t"
    "v_cvt_pk_bf16_f32 v66, v68, v69\n\t"
    "v_mfma_f32_16x16x32_bf16 a[80:83], a[228:231], v[0:3], a[80:83]\n\t"
    "buffer_load_dwordx4 v201, s[36:39], s52 offen offset:3200 lds\n\t"
    "v_fma_f32 v80, v80, s62, v213\n\t"
    "v_mfma_f32_16x16x32_bf16 a[96:99], a[232:235], v[0:3], a[96:99]\n\t"
    "ds_read_b64_tr_b16 a[224:225], v196 offset:2048\n\t"
    "v_fma_f32 v81, v81, s62, v213\n\t"
    "v_mfma_f32_16x16x32_bf16 a[100:103], a[232:235], v[16:19], a[100:103]\n\t"
    "v_exp_f32 v70, v70\n\t"
    "v_mfma_f32_16x16x32_bf16 v[240:243], v[244:247], v[56:59], v[240:243]\n\t"
    "v_exp_f32 v71, v71\n\t"
    "v_mfma_f32_16x16x32_bf16 a[104:107], a[232:235], v[32:35], a[104:107]\n\t"
    "ds_read_b64_tr_b16 a[226:227], v196 offset:8192\n\t"
    "v_cvt_pk_bf16_f32 v67, v70, v71\n\t"
    "v_mfma_f32_16x16x32_bf16 a[108:111], a[232:235], v[48:51], a[108:111]\n\t"
    "v_exp_f32 v80, v80\n\t"
    "v_mfma_f32_16x16x32_bf16 a[124:127], a[236:239], v[48:51], a[124:127]\n\t"
    "s_add_u32 m0, s58, 2048\n\t"
    "ds_read_b64_tr_b16 a[228:229], v197 offset:2048\n\t"
    "v_fma_f32 v82, v82, s62, v213\n\t"
    "v_mfma_f32_16x16x32_bf16 a[120:123], a[236:239], v[32:35], a[120:123]\n\t"
    "s_add_u32 s75, s54, s97\n\t"
    "buffer_load_dwordx4 v200, s[44:47], s75 offen offset:0 nt lds\n\t"
    "v_fma_f32 v83, v83, s62, v213\n\t"
    "v_mfma_f32_16x16x32_bf16 a[116:119], a[236:239], v[16:19], a[116:119]\n\t"
    "ds_read_b64_tr_b16 a[230:231], v197 offset:8192\n\t"
    "v_fma_f32 v84, v84, s62, v213\n\t"
    "v_mfma_f32_16x16x32_bf16 a[112:115], a[236:239], v[0:3], a[112:115]\n\t"
    "s_waitcnt lgkmcnt(8)\n\t"
    "v_exp_f32 v81, v81\n\t"
    "v_mfma_f32_16x16x32_bf16 a[64:67], a[240:243], v[8:11], a[64:67]\n\t"
    "ds_read_b64_tr_b16 a[232:233], v198 offset:2048\n\t"
    "v_cvt_pk_bf16_f32 v80, v80, v81\n\t"
    "v_mfma_f32_16x16x32_bf16 a[68:71], a[240:243], v[24:27], a[68:71]\n\t"
    "v_exp_f32 v82, v82\n\t"
    "v_mfma_f32_16x16x32_bf16 a[72:75], a[240:243], v[40:43], a[72:75]\n\t"
    "s_add_u32 m0, s56, 26368\n\t"
    "ds_read_b64_tr_b16 a[234:235], v198 offset:8192\n\t"
    "v_fma_f32 v85, v85, s62, v213\n\t"
    "v_mfma_f32_16x16x32_bf16 a[76:79], a[240:243], v[56:59], a[76:79]\n\t"
    "buffer_load_dwordx4 v200, s[36:39], s52 offen offset:256 lds\n\t"
    "v_fma_f32 v86, v86, s62, v213\n\t"
    "v_mfma_f32_16x16x32_bf16 a[92:95], a[244:247], v[56:59], a[92:95]\n\t"
    "ds_read_b64_tr_b16 a[236:237], v199 offset:2048\n\t"
    "v_fma_f32 v87, v87, s62, v213\n\t"
    "v_mfma_f32_16x16x32_bf16 a[88:91], a[244:247], v[40:43], a[88:91]\n\t"
    "v_exp_f32 v83, v83\n\t"
    "v_mfma_f32_16x16x32_bf16 a[84:87], a[244:247], v[24:27], a[84:87]\n\t"
    "ds_read_b64_tr_b16 a[238:239], v199 offset:8192\n\t"
    "v_cvt_pk_bf16_f32 v81, v82, v83\n\t"
    "v_mfma_f32_16x16x32_bf16 a[80:83], a[244:247], v[8:11], a[80:83]\n\t"
    "s_waitcnt lgkmcnt(8)\n\t"
    "v_exp_f32 v84, v84\n\t"
    "v_mfma_f32_16x16x32_bf16 a[96:99], a[248:251], v[8:11], a[96:99]\n\t"
    "ds_read_b64_tr_b16 a[240:241], v196 offset:14336\n\t"
    "v_fma_f32 v96, v96, s62, v214\n\t"
    "v_mfma_f32_16x16x32_bf16 a[100:103], a[248:251], v[24:27], a[100:103]\n\t"
    "v_exp_f32 v85, v85\n\t"
    "v_mfma_f32_16x16x32_bf16 a[104:107], a[248:251], v[40:43], a[104:107]\n\t"
    "ds_read_b64_tr_b16 a[242:243], v196 offset:20480\n\t"
    "v_cvt_pk_bf16_f32 v82, v84, v85\n\t"
    "v_mfma_f32_16x16x32_bf16 a[108:111], a[248:251], v[56:59], a[108:111]\n\t"
    "v_exp_f32 v86, v86\n\t"
    "v_mfma_f32_16x16x32_bf16 a[124:127], a[252:255], v[56:59], a[124:127]\n\t"
    "ds_read_b64_tr_b16 a[244:245], v197 offset:14336\n\t"
    "v_fma_f32 v97, v97, s62, v214\n\t"
    "v_mfma_f32_16x16x32_bf16 a[120:123], a[252:255], v[40:43], a[120:123]\n\t"
    "v_exp_f32 v87, v87\n\t"
    "v_mfma_f32_16x16x32_bf16 a[116:119], a[252:255], v[24:27], a[116:119]\n\t"
    "s_add_u32 m0, s56, 26368\n\t"
    "ds_read_b64_tr_b16 a[246:247], v197 offset:20480\n\t"
    "v_cvt_pk_bf16_f32 v83, v86, v87\n\t"
    "v_mfma_f32_16x16x32_bf16 a[112:115], a[252:255], v[8:11], a[112:115]\n\t"
    "s_waitcnt lgkmcnt(8)\n\t"
    "buffer_load_dwordx4 v201, s[36:39], s52 offen offset:3328 lds\n\t"
    "v_fma_f32 v98, v98, s62, v214\n\t"
    "v_mfma_f32_16x16x32_bf16 v[128:131], a[224:227], v[0:3], v[128:131]\n\t"
    "ds_read_b64_tr_b16 a[248:249], v198 offset:14336\n\t"
    "v_fma_f32 v99, v99, s62, v214\n\t"
    "v_mfma_f32_16x16x32_bf16 v[132:135], a[224:227], v[16:19], v[132:135]\n\t"
    "ds_read_b128 v[248:251], v203 offset:15360\n\t"
    "v_fma_f32 v100, v100, s62, v214\n\t"
    "v_mfma_f32_16x16x32_bf16 v[136:139], a[224:227], v[32:35], v[136:139]\n\t"
    "ds_read_b64_tr_b16 a[250:251], v198 offset:20480\n\t"
    "v_fma_f32 v101, v101, s62, v214\n\t"
    "v_mfma_f32_16x16x32_bf16 v[140:143], a[224:227], v[48:51], v[140:143]\n\t"
    "v_exp_f32 v96, v96\n\t"
    "v_mfma_f32_16x16x32_bf16 v[156:159], a[228:231], v[48:51], v[156:159]\n\t"
    "s_waitcnt lgkmcnt(1)\n\t"
    "ds_read_b64_tr_b16 a[252:253], v199 offset:14336\n\t"
    "v_fma_f32 v102, v102, s62, v214\n\t"
    "v_mfma_f32_16x16x32_bf16 v[152:155], a[228:231], v[32:35], v[152:155]\n\t"
    "buffer_store_dwordx4 v[248:251], v201, s[48:51], s80 offen offset:3328 nt\n\t"
    "v_mfma_f32_16x16x32_bf16 v[148:151], a[228:231], v[16:19], v[148:151]\n\t"
    "ds_read_b64_tr_b16 a[254:255], v199 offset:20480\n\t"
    "v_mfma_f32_16x16x32_bf16 v[144:147], a[228:231], v[0:3], v[144:147]\n\t"
    "v_exp_f32 v97, v97\n\t"
    "v_mfma_f32_16x16x32_bf16 v[160:163], a[232:235], v[0:3], v[160:163]\n\t"
    "v_exp_f32 v98, v98\n\t"
    "v_mfma_f32_16x16x32_bf16 v[164:167], a[232:235], v[16:19], v[164:167]\n\t"
    "v_exp_f32 v99, v99\n\t"
    "v_mfma_f32_16x16x32_bf16 v[168:171], a[232:235], v[32:35], v[168:171]\n\t"
    "s_add_u32 s52, s52, 24576\n\t"
    "v_cvt_pk_bf16_f32 v96, v96, v97\n\t"
    "v_cvt_pk_bf16_f32 v97, v98, v99\n\t"
    "v_mfma_f32_16x16x32_bf16 v[172:175], a[232:235], v[48:51], v[172:175]\n\t"
    "v_exp_f32 v100, v100\n\t"
    "v_mfma_f32_16x16x32_bf16 v[188:191], a[236:239], v[48:51], v[188:191]\n\t"
    "v_exp_f32 v101, v101\n\t"
    "v_mfma_f32_16x16x32_bf16 v[184:187], a[236:239], v[32:35], v[184:187]\n\t"
    "v_exp_f32 v102, v102\n\t"
    "v_mfma_f32_16x16x32_bf16 v[180:183], a[236:239], v[16:19], v[180:183]\n\t"
    "s_waitcnt lgkmcnt(0) vmcnt(21)\n\t"
    "v_cvt_pk_bf16_f32 v98, v100, v101\n\t"
    "v_fma_f32 v103, v103, s62, v214\n\t"
    "v_mfma_f32_16x16x32_bf16 v[176:179], a[236:239], v[0:3], v[176:179]\n\t"
    "v_exp_f32 v103, v103\n\t"
    "v_mfma_f32_16x16x32_bf16 v[128:131], a[240:243], v[8:11], v[128:131]\n\t"
    "s_barrier\n\t"
    "ds_read_b128 a[224:227], v192 offset:0\n\t"
    "v_cvt_pk_bf16_f32 v99, v102, v103\n\t"
    "v_mfma_f32_16x16x32_bf16 v[132:135], a[240:243], v[24:27], v[132:135]\n\t"
    "s_add_u32 m0, s58, 0\n\t"
    "ds_read_b128 a[228:231], v192 offset:6144\n\t"
    "v_fma_f32 v112, v112, s62, v215\n\t"
    "v_mfma_f32_16x16x32_bf16 v[136:139], a[240:243], v[40:43], v[136:139]\n\t"
    "s_add_u32 s75, s54, s97\n\t"
    "ds_read_b128 a[232:235], v192 offset:12288\n\t"
    "v_fma_f32 v113, v113, s62, v215\n\t"
    "v_mfma_f32_16x16x32_bf16 v[140:143], a[240:243], v[56:59], v[140:143]\n\t"
    "buffer_load_dwordx4 v201, s[44:47], s75 offen offset:3072 nt lds\n\t"
    "v_fma_f32 v114, v114, s62, v215\n\t"
    "v_mfma_f32_16x16x32_bf16 v[156:159], a[244:247], v[56:59], v[156:159]\n\t"
    "v_exp_f32 v112, v112\n\t"
    "v_mfma_f32_16x16x32_bf16 v[152:155], a[244:247], v[40:43], v[152:155]\n\t"
    "v_exp_f32 v113, v113\n\t"
    "v_mfma_f32_16x16x32_bf16 v[148:151], a[244:247], v[24:27], v[148:151]\n\t"
    "ds_read_b128 a[236:239], v192 offset:18432\n\t"
    "v_cvt_pk_bf16_f32 v112, v112, v113\n\t"
    "v_mfma_f32_16x16x32_bf16 v[144:147], a[244:247], v[8:11], v[144:147]\n\t"
    "v_exp_f32 v114, v114\n\t"
    "v_mfma_f32_16x16x32_bf16 v[160:163], a[248:251], v[8:11], v[160:163]\n\t"
    "v_fma_f32 v115, v115, s62, v215\n\t"
    "v_fma_f32 v116, v116, s62, v215\n\t"
    "v_mfma_f32_16x16x32_bf16 v[164:167], a[248:251], v[24:27], v[164:167]\n\t"
    "v_exp_f32 v115, v115\n\t"
    "v_mfma_f32_16x16x32_bf16 v[168:171], a[248:251], v[40:43], v[168:171]\n\t"
    "ds_read_b128 a[240:243], v193 offset:0\n\t"
    "v_cvt_pk_bf16_f32 v113, v114, v115\n\t"
    "v_mfma_f32_16x16x32_bf16 v[172:175], a[248:251], v[56:59], v[172:175]\n\t"
    "v_exp_f32 v116, v116\n\t"
    "v_mfma_f32_16x16x32_bf16 v[188:191], a[252:255], v[56:59], v[188:191]\n\t"
    "v_fma_f32 v117, v117, s62, v215\n\t"
    "v_fma_f32 v118, v118, s62, v215\n\t"
    "v_mfma_f32_16x16x32_bf16 v[184:187], a[252:255], v[40:43], v[184:187]\n\t"
    "v_exp_f32 v117, v117\n\t"
    "v_mfma_f32_16x16x32_bf16 v[180:183], a[252:255], v[24:27], v[180:183]\n\t"
    "ds_read_b128 a[244:247], v193 offset:6144\n\t"
    "v_cvt_pk_bf16_f32 v114, v116, v117\n\t"
    "v_mfma_f32_16x16x32_bf16 v[176:179], a[252:255], v[8:11], v[176:179]\n\t"
    "s_waitcnt lgkmcnt(4)\n\t"
    "v_exp_f32 v118, v118\n\t"
    "v_mfma_f32_16x16x32_bf16 v[0:3], a[224:227], a[128:131], v[224:227]\n\t"
    "s_cmp_lg_u32 s72, 0\n\t"
    "s_cbranch_scc1 SB3\n\t"
    "NSB3:\n\t"
    "v_fma_f32 v119, v119, s62, v215\n\t"
    "v_mfma_f32_16x16x32_bf16 v[16:19], a[224:227], a[132:135], v[224:227]\n\t"
    "v_exp_f32 v119, v119\n\t"
    "v_mfma_f32_16x16x32_bf16 v[32:35], a[224:227], a[136:139], v[224:227]\n\t"
    "s_add_u32 m0, s57, 0\n\t"
    "ds_read_b128 a[248:251], v193 offset:12288\n\t"
    "v_cvt_pk_bf16_f32 v115, v118, v119\n\t"
    "v_mfma_f32_16x16x32_bf16 v[48:51], a[224:227], a[140:143], v[224:227]\n\t"
    "buffer_load_dwordx4 v202, s[40:43], s53 offen offset:0 lds\n\t"
    "v_fma_f32 v72, v72, s62, v212\n\t"
    "v_mfma_f32_16x16x32_bf16 v[52:55], a[228:231], a[140:143], v[224:227]\n\t"
    "v_exp_f32 v72, v72\n\t"
    "v_mfma_f32_16x16x32_bf16 v[36:39], a[228:231], a[136:139], v[224:227]\n\t"
    "v_fma_f32 v73, v73, s62, v212\n\t"
    "v_fma_f32 v74, v74, s62, v212\n\t"
    "v_mfma_f32_16x16x32_bf16 v[20:23], a[228:231], a[132:135], v[224:227]\n\t"
    "ds_read_b128 a[252:255], v193 offset:18432\n\t"
    "v_fma_f32 v75, v75, s62, v212\n\t"
    "v_mfma_f32_16x16x32_bf16 v[4:7], a[228:231], a[128:131], v[224:227]\n\t"
    "s_waitcnt lgkmcnt(4)\n\t"
    "v_exp_f32 v73, v73\n\t"
    "v_mfma_f32_16x16x32_bf16 v[8:11], a[232:235], a[128:131], v[224:227]\n\t"
    "v_exp_f32 v74, v74\n\t"
    "v_mfma_f32_16x16x32_bf16 v[24:27], a[232:235], a[132:135], v[224:227]\n\t"
    "v_exp_f32 v75, v75\n\t"
    "v_mfma_f32_16x16x32_bf16 v[40:43], a[232:235], a[136:139], v[224:227]\n\t"
    "ds_read_b128 a[224:227], v192 offset:1024\n\t"
    "v_cvt_pk_bf16_f32 v72, v72, v73\n\t"
    "v_mfma_f32_16x16x32_bf16 v[56:59], a[232:235], a[140:143], v[224:227]\n\t"
    "v_cvt_pk_bf16_f32 v73, v74, v75\n\t"
    "v_fma_f32 v76, v76, s62, v212\n\t"
    "v_mfma_f32_16x16x32_bf16 v[60:63], a[236:239], a[140:143], v[224:227]\n\t"
    "v_exp_f32 v76, v76\n\t"
    "v_mfma_f32_16x16x32_bf16 v[44:47], a[236:239], a[136:139], v[224:227]\n\t"
    "v_fma_f32 v77, v77, s62, v212\n\t"
    "v_fma_f32 v78, v78, s62, v212\n\t"
    "v_mfma_f32_16x16x32_bf16 v[28:31], a[236:239], a[132:135], v[224:227]\n\t"
    "s_add_u32 m0, s57, 0\n\t"
    "ds_read_b128 a[228:231], v192 offset:7168\n\t"
    "v_fma_f32 v79, v79, s62, v212\n\t"
    "v_mfma_f32_16x16x32_bf16 v[12:15], a[236:239], a[128:131], v[224:227]\n\t"
    "s_waitcnt lgkmcnt(4)\n\t"
    "buffer_load_dwordx4 v202, s[40:43], s53 offen offset:3072 lds\n\t"
    "v_fma_f32 v88, v88, s62, v213\n\t"
    "v_mfma_f32_16x16x32_bf16 v[0:3], a[240:243], a[144:147], v[0:3]\n\t"
    "v_exp_f32 v77, v77\n\t"
    "v_mfma_f32_16x16x32_bf16 v[16:19], a[240:243], a[148:151], v[16:19]\n\t"
    "v_exp_f32 v78, v78\n\t"
    "v_mfma_f32_16x16x32_bf16 v[32:35], a[240:243], a[152:155], v[32:35]\n\t"
    "ds_read_b128 a[232:235], v192 offset:13312\n\t"
    "v_cvt_pk_bf16_f32 v74, v76, v77\n\t"
    "v_mfma_f32_16x16x32_bf16 v[48:51], a[240:243], a[156:159], v[48:51]\n\t"
    "v_exp_f32 v79, v79\n\t"
    "v_mfma_f32_16x16x32_bf16 v[52:55], a[244:247], a[156:159], v[52:55]\n\t"
    "v_exp_f32 v88, v88\n\t"
    "v_mfma_f32_16x16x32_bf16 v[36:39], a[244:247], a[152:155], v[36:39]\n\t"
    "v_cvt_pk_bf16_f32 v75, v78, v79\n\t"
    "v_fma_f32 v89, v89, s62, v213\n\t"
    "v_mfma_f32_16x16x32_bf16 v[20:23], a[244:247], a[148:151], v[20:23]\n\t"
    "ds_read_b128 a[236:239], v192 offset:19456\n\t"
    "v_fma_f32 v90, v90, s62, v213\n\t"
    "v_mfma_f32_16x16x32_bf16 v[4:7], a[244:247], a[144:147], v[4:7]\n\t"
    "s_waitcnt lgkmcnt(4)\n\t"
    "v_exp_f32 v89, v89\n\t"
    "v_mfma_f32_16x16x32_bf16 v[8:11], a[248:251], a[144:147], v[8:11]\n\t"
    "v_exp_f32 v90, v90\n\t"
    "v_mfma_f32_16x16x32_bf16 v[24:27], a[248:251], a[148:151], v[24:27]\n\t"
    "v_cvt_pk_bf16_f32 v88, v88, v89\n\t"
    "v_fma_f32 v91, v91, s62, v213\n\t"
    "v_mfma_f32_16x16x32_bf16 v[40:43], a[248:251], a[152:155], v[40:43]\n\t"
    "s_add_u32 m0, s57, 896\n\t"
    "ds_read_b128 a[240:243], v193 offset:1024\n\t"
    "v_fma_f32 v92, v92, s62, v213\n\t"
    "v_mfma_f32_16x16x32_bf16 v[56:59], a[248:251], a[156:159], v[56:59]\n\t"
    "buffer_load_dwordx4 v202, s[40:43], s53 offen offset:128 lds\n\t"
    "v_fma_f32 v93, v93, s62, v213\n\t"
    "v_mfma_f32_16x16x32_bf16 v[60:63], a[252:255], a[156:159], v[60:63]\n\t"
    "v_exp_f32 v91, v91\n\t"
    "v_mfma_f32_16x16x32_bf16 v[44:47], a[252:255], a[152:155], v[44:47]\n\t"
    "v_exp_f32 v92, v92\n\t"
    "v_mfma_f32_16x16x32_bf16 v[28:31], a[252:255], a[148:151], v[28:31]\n\t"
    "ds_read_b128 a[244:247], v193 offset:7168\n\t"
    "v_cvt_pk_bf16_f32 v89, v90, v91\n\t"
    "v_mfma_f32_16x16x32_bf16 v[12:15], a[252:255], a[144:147], v[12:15]\n\t"
    "s_waitcnt lgkmcnt(4)\n\t"
    "v_exp_f32 v93, v93\n\t"
    "v_mfma_f32_16x16x32_bf16 v[0:3], a[224:227], a[160:163], v[0:3]\n\t"
    "v_cvt_pk_bf16_f32 v90, v92, v93\n\t"
    "v_fma_f32 v94, v94, s62, v213\n\t"
    "v_mfma_f32_16x16x32_bf16 v[16:19], a[224:227], a[164:167], v[16:19]\n\t"
    "v_exp_f32 v94, v94\n\t"
    "v_mfma_f32_16x16x32_bf16 v[32:35], a[224:227], a[168:171], v[32:35]\n\t"
    "ds_read_b128 a[248:251], v193 offset:13312\n\t"
    "v_fma_f32 v95, v95, s62, v213\n\t"
    "v_mfma_f32_16x16x32_bf16 v[48:51], a[224:227], a[172:175], v[48:51]\n\t"
    "v_exp_f32 v95, v95\n\t"
    "v_mfma_f32_16x16x32_bf16 v[52:55], a[228:231], a[172:175], v[52:55]\n\t"
    "v_cvt_pk_bf16_f32 v91, v94, v95\n\t"
    "v_fma_f32 v104, v104, s62, v214\n\t"
    "v_mfma_f32_16x16x32_bf16 v[36:39], a[228:231], a[168:171], v[36:39]\n\t"
    "v_exp_f32 v104, v104\n\t"
    "v_mfma_f32_16x16x32_bf16 v[20:23], a[228:231], a[164:167], v[20:23]\n\t"
    "s_add_u32 m0, s57, 896\n\t"
    "ds_read_b128 a[252:255], v193 offset:19456\n\t"
    "v_fma_f32 v105, v105, s62, v214\n\t"
    "v_mfma_f32_16x16x32_bf16 v[4:7], a[228:231], a[160:163], v[4:7]\n\t"
    "s_waitcnt lgkmcnt(4)\n\t"
    "buffer_load_dwordx4 v202, s[40:43], s53 offen offset:3200 lds\n\t"
    "v_fma_f32 v106, v106, s62, v214\n\t"
    "v_mfma_f32_16x16x32_bf16 v[8:11], a[232:235], a[160:163], v[8:11]\n\t"
    "v_exp_f32 v105, v105\n\t"
    "v_mfma_f32_16x16x32_bf16 v[24:27], a[232:235], a[164:167], v[24:27]\n\t"
    "v_exp_f32 v106, v106\n\t"
    "v_mfma_f32_16x16x32_bf16 v[40:43], a[232:235], a[168:171], v[40:43]\n\t"
    "ds_read_b128 a[224:227], v192 offset:2048\n\t"
    "v_cvt_pk_bf16_f32 v104, v104, v105\n\t"
    "v_mfma_f32_16x16x32_bf16 v[56:59], a[232:235], a[172:175], v[56:59]\n\t"
    "v_fma_f32 v107, v107, s62, v214\n\t"
    "v_fma_f32 v108, v108, s62, v214\n\t"
    "v_mfma_f32_16x16x32_bf16 v[60:63], a[236:239], a[172:175], v[60:63]\n\t"
    "s_add_u32 m0, s58, 4096\n\t"
    "v_exp_f32 v107, v107\n\t"
    "v_mfma_f32_16x16x32_bf16 v[44:47], a[236:239], a[168:171], v[44:47]\n\t"
    "s_add_u32 s75, s54, s98\n\t"
    "buffer_load_dwordx4 v200, s[44:47], s75 offen offset:0 nt lds\n\t"
    "v_cvt_pk_bf16_f32 v105, v106, v107\n\t"
    "v_mfma_f32_16x16x32_bf16 v[28:31], a[236:239], a[164:167], v[28:31]\n\t"
    "ds_read_b128 a[228:231], v192 offset:8192\n\t"
    "v_fma_f32 v109, v109, s62, v214\n\t"
    "v_mfma_f32_16x16x32_bf16 v[12:15], a[236:239], a[160:163], v[12:15]\n\t"
    "s_waitcnt lgkmcnt(4)\n\t"
    "v_exp_f32 v108, v108\n\t"
    "v_mfma_f32_16x16x32_bf16 v[0:3], a[240:243], a[176:179], v[0:3]\n\t"
    "v_exp_f32 v109, v109\n\t"
    "v_mfma_f32_16x16x32_bf16 v[16:19], a[240:243], a[180:183], v[16:19]\n\t"
    "v_cvt_pk_bf16_f32 v106, v108, v109\n\t"
    "v_fma_f32 v110, v110, s62, v214\n\t"
    "v_mfma_f32_16x16x32_bf16 v[32:35], a[240:243], a[184:187], v[32:35]\n\t"
    "s_add_u32 m0, s57, 1792\n\t"
    "ds_read_b128 a[232:235], v192 offset:14336\n\t"
    "v_fma_f32 v111, v111, s62, v214\n\t"
    "v_mfma_f32_16x16x32_bf16 v[48:51], a[240:243], a[188:191], v[48:51]\n\t"
    "buffer_load_dwordx4 v202, s[40:43], s53 offen offset:256 lds\n\t"
    "v_fma_f32 v120, v120, s62, v215\n\t"
    "v_mfma_f32_16x16x32_bf16 v[52:55], a[244:247], a[188:191], v[52:55]\n\t"
    "v_exp_f32 v110, v110\n\t"
    "v_mfma_f32_16x16x32_bf16 v[36:39], a[244:247], a[184:187], v[36:39]\n\t"
    "v_exp_f32 v111, v111\n\t"
    "v_mfma_f32_16x16x32_bf16 v[20:23], a[244:247], a[180:183], v[20:23]\n\t"
    "ds_read_b128 a[236:239], v192 offset:20480\n\t"
    "v_cvt_pk_bf16_f32 v107, v110, v111\n\t"
    "v_mfma_f32_16x16x32_bf16 v[4:7], a[244:247], a[176:179], v[4:7]\n\t"
    "s_waitcnt lgkmcnt(4)\n\t"
    "v_exp_f32 v120, v120\n\t"
    "v_mfma_f32_16x16x32_bf16 v[8:11], a[248:251], a[176:179], v[8:11]\n\t"
    "v_fma_f32 v121, v121, s62, v215\n\t"
    "v_fma_f32 v122, v122, s62, v215\n\t"
    "v_mfma_f32_16x16x32_bf16 v[24:27], a[248:251], a[180:183], v[24:27]\n\t"
    "v_exp_f32 v121, v121\n\t"
    "v_mfma_f32_16x16x32_bf16 v[40:43], a[248:251], a[184:187], v[40:43]\n\t"
    "ds_read_b128 a[240:243], v193 offset:2048\n\t"
    "v_cvt_pk_bf16_f32 v120, v120, v121\n\t"
    "v_mfma_f32_16x16x32_bf16 v[56:59], a[248:251], a[188:191], v[56:59]\n\t"
    "v_exp_f32 v122, v122\n\t"
    "v_mfma_f32_16x16x32_bf16 v[60:63], a[252:255], a[188:191], v[60:63]\n\t"
    "v_fma_f32 v123, v123, s62, v215\n\t"
    "v_fma_f32 v124, v124, s62, v215\n\t"
    "v_mfma_f32_16x16x32_bf16 v[44:47], a[252:255], a[184:187], v[44:47]\n\t"
    "v_exp_f32 v123, v123\n\t"
    "v_mfma_f32_16x16x32_bf16 v[28:31], a[252:255], a[180:183], v[28:31]\n\t"
    "s_add_u32 m0, s57, 1792\n\t"
    "ds_read_b128 a[244:247], v193 offset:8192\n\t"
    "v_cvt_pk_bf16_f32 v121, v122, v123\n\t"
    "v_mfma_f32_16x16x32_bf16 v[12:15], a[252:255], a[176:179], v[12:15]\n\t"
    "s_waitcnt lgkmcnt(4)\n\t"
    "buffer_load_dwordx4 v202, s[40:43], s53 offen offset:3328 lds\n\t"
    "v_fma_f32 v125, v125, s62, v215\n\t"
    "v_mfma_f32_16x16x32_bf16 v[0:3], a[224:227], a[192:195], v[0:3]\n\t"
    "v_exp_f32 v124, v124\n\t"
    "v_mfma_f32_16x16x32_bf16 v[16:19], a[224:227], a[196:199], v[16:19]\n\t"
    "v_exp_f32 v125, v125\n\t"
    "v_mfma_f32_16x16x32_bf16 v[32:35], a[224:227], a[200:203], v[32:35]\n\t"
    "ds_read_b128 a[248:251], v193 offset:14336\n\t"
    "v_cvt_pk_bf16_f32 v122, v124, v125\n\t"
    "v_mfma_f32_16x16x32_bf16 v[48:51], a[224:227], a[204:207], v[48:51]\n\t"
    "v_fma_f32 v126, v126, s62, v215\n\t"
    "v_fma_f32 v127, v127, s62, v215\n\t"
    "v_mfma_f32_16x16x32_bf16 v[52:55], a[228:231], a[204:207], v[52:55]\n\t"
    "v_exp_f32 v126, v126\n\t"
    "v_mfma_f32_16x16x32_bf16 v[36:39], a[228:231], a[200:203], v[36:39]\n\t"
    "v_exp_f32 v127, v127\n\t"
    "v_mfma_f32_16x16x32_bf16 v[20:23], a[228:231], a[196:199], v[20:23]\n\t"
    "ds_read_b128 a[252:255], v193 offset:20480\n\t"
    "v_cvt_pk_bf16_f32 v123, v126, v127\n\t"
    "v_mfma_f32_16x16x32_bf16 v[4:7], a[228:231], a[192:195], v[4:7]\n\t"
    "s_waitcnt lgkmcnt(4)\n\t"
    "v_mfma_f32_16x16x32_bf16 v[8:11], a[232:235], a[192:195], v[8:11]\n\t"
    "v_mfma_f32_16x16x32_bf16 v[24:27], a[232:235], a[196:199], v[24:27]\n\t"
    "v_mfma_f32_16x16x32_bf16 v[40:43], a[232:235], a[200:203], v[40:43]\n\t"
    "s_add_u32 s53, s53, 24576\n\t"
    "v_mfma_f32_16x16x32_bf16 v[56:59], a[232:235], a[204:207], v[56:59]\n\t"
    "v_mfma_f32_16x16x32_bf16 v[60:63], a[236:239], a[204:207], v[60:63]\n\t"
    "v_mfma_f32_16x16x32_bf16 v[44:47], a[236:239], a[200:203], v[44:47]\n\t"
    "v_mfma_f32_16x16x32_bf16 v[28:31], a[236:239], a[196:199], v[28:31]\n\t"
    "s_waitcnt lgkmcnt(0) vmcnt(19)\n\t"
    "v_mfma_f32_16x16x32_bf16 v[12:15], a[236:239], a[192:195], v[12:15]\n\t"
    "v_mfma_f32_16x16x32_bf16 v[0:3], a[240:243], a[208:211], v[0:3]\n\t"
    "s_barrier\n\t"
    "ds_read_b64_tr_b16 a[224:225], v196 offset:24576\n\t"
    "v_mfma_f32_16x16x32_bf16 v[16:19], a[240:243], a[212:215], v[16:19]\n\t"
    "s_add_u32 m0, s58, 2048\n\t"
    "ds_read_b64_tr_b16 a[226:227], v196 offset:30720\n\t"
    "v_mfma_f32_16x16x32_bf16 v[32:35], a[240:243], a[216:219], v[32:35]\n\t"
    "s_add_u32 s75, s54, s98\n\t"
    "ds_read_b64_tr_b16 a[228:229], v197 offset:24576\n\t"
    "v_mfma_f32_16x16x32_bf16 v[48:51], a[240:243], a[220:223], v[48:51]\n\t"
    "ds_read_b64_tr_b16 a[230:231], v197 offset:30720\n\t"
    "v_mfma_f32_16x16x32_bf16 v[52:55], a[244:247], a[220:223], v[52:55]\n\t"
    "ds_read_b64_tr_b16 a[232:233], v198 offset:24576\n\t"
    "v_mfma_f32_16x16x32_bf16 v[36:39], a[244:247], a[216:219], v[36:39]\n\t"
    "ds_read_b64_tr_b16 a[234:235], v198 offset:30720\n\t"
    "v_mfma_f32_16x16x32_bf16 v[20:23], a[244:247], a[212:215], v[20:23]\n\t"
    "ds_read_b64_tr_b16 a[236:237], v199 offset:24576\n\t"
    "v_mfma_f32_16x16x32_bf16 v[4:7], a[244:247], a[208:211], v[4:7]\n\t"
    "ds_read_b64_tr_b16 a[238:239], v199 offset:30720\n\t"
    "v_mfma_f32_16x16x32_bf16 v[8:11], a[248:251], a[208:211], v[8:11]\n\t"
    "ds_read_b64_tr_b16 a[240:241], v196 offset:36864\n\t"
    "v_mfma_f32_16x16x32_bf16 v[24:27], a[248:251], a[212:215], v[24:27]\n\t"
    "buffer_load_dwordx4 v201, s[44:47], s75 offen offset:3072 nt lds\n\t"
    "v_mfma_f32_16x16x32_bf16 v[40:43], a[248:251], a[216:219], v[40:43]\n\t"
    "ds_read_b64_tr_b16 a[242:243], v196 offset:43008\n\t"
    "v_mfma_f32_16x16x32_bf16 v[56:59], a[248:251], a[220:223], v[56:59]\n\t"
    "v_mfma_f32_16x16x32_bf16 v[60:63], a[252:255], a[220:223], v[60:63]\n\t"
    "ds_read_b64_tr_b16 a[244:245], v197 offset:36864\n\t"
    "v_mfma_f32_16x16x32_bf16 v[44:47], a[252:255], a[216:219], v[44:47]\n\t"
    "v_mfma_f32_16x16x32_bf16 v[28:31], a[252:255], a[212:215], v[28:31]\n\t"
    "ds_read_b64_tr_b16 a[246:247], v197 offset:43008\n\t"
    "v_mfma_f32_16x16x32_bf16 v[12:15], a[252:255], a[208:211], v[12:15]\n\t"
    "s_waitcnt lgkmcnt(8)\n\t"
    "v_mfma_f32_16x16x32_bf16 a[0:3], a[224:227], v[64:67], a[0:3]\n\t"
    "ds_read_b64_tr_b16 a[248:249], v198 offset:36864\n\t"
    "v_mfma_f32_16x16x32_bf16 a[4:7], a[224:227], v[80:83], a[4:7]\n\t"
    "v_mfma_f32_16x16x32_bf16 a[8:11], a[224:227], v[96:99], a[8:11]\n\t"
    "s_add_u32 m0, s56, 0\n\t"
    "ds_read_b64_tr_b16 a[250:251], v198 offset:43008\n\t"
    "v_mfma_f32_16x16x32_bf16 a[12:15], a[224:227], v[112:115], a[12:15]\n\t"
    "buffer_load_dwordx4 v200, s[36:39], s52 offen offset:0 lds\n\t"
    "v_mfma_f32_16x16x32_bf16 a[28:31], a[228:231], v[112:115], a[28:31]\n\t"
    "ds_read_b64_tr_b16 a[252:253], v199 offset:36864\n\t"
    "v_max3_f32 v220, v0, v1, v2\n\t"
    "v_mfma_f32_16x16x32_bf16 a[24:27], a[228:231], v[96:99], a[24:27]\n\t"
    "v_max3_f32 v221, v16, v17, v18\n\t"
    "v_max3_f32 v222, v32, v33, v34\n\t"
    "v_mfma_f32_16x16x32_bf16 a[20:23], a[228:231], v[80:83], a[20:23]\n\t"
    "ds_read_b64_tr_b16 a[254:255], v199 offset:43008\n\t"
    "v_max3_f32 v223, v48, v49, v50\n\t"
    "v_mfma_f32_16x16x32_bf16 a[16:19], a[228:231], v[64:67], a[16:19]\n\t"
    "s_waitcnt lgkmcnt(8)\n\t"
    "v_max3_f32 v220, v220, v3, v4\n\t"
    "v_max3_f32 v221, v221, v19, v20\n\t"
    "v_mfma_f32_16x16x32_bf16 a[32:35], a[232:235], v[64:67], a[32:35]\n\t"
    "ds_read_b64_tr_b16 a[224:225], v196 offset:25600\n\t"
    "v_max3_f32 v222, v222, v35, v36\n\t"
    "v_mfma_f32_16x16x32_bf16 a[36:39], a[232:235], v[80:83], a[36:39]\n\t"
    "v_max3_f32 v223, v223, v51, v52\n\t"
    "v_max3_f32 v220, v220, v5, v6\n\t"
    "v_mfma_f32_16x16x32_bf16 v[228:231], v[244:247], v[64:67], v[228:231]\n\t"
    "v_max3_f32 v221, v221, v21, v22\n\t"
    "v_max3_f32 v222, v222, v37, v38\n\t"
    "v_mfma_f32_16x16x32_bf16 a[40:43], a[232:235], v[96:99], a[40:43]\n\t"
    "ds_read_b64_tr_b16 a[226:227], v196 offset:31744\n\t"
    "v_max3_f32 v223, v223, v53, v54\n\t"
    "v_mfma_f32_16x16x32_bf16 a[44:47], a[232:235], v[112:115], a[44:47]\n\t"
    "v_max3_f32 v220, v220, v7, v8\n\t"
    "v_max3_f32 v221, v221, v23, v24\n\t"
    "v_mfma_f32_16x16x32_bf16 a[60:63], a[236:239], v[112:115], a[60:63]\n\t"
    "ds_read_b64_tr_b16 a[228:229], v197 offset:25600\n\t"
    "v_max3_f32 v222, v222, v39, v40\n\t"
    "v_mfma_f32_16x16x32_bf16 a[56:59], a[236:239], v[96:99], a[56:59]\n\t"
    "v_max3_f32 v223, v223, v55, v56\n\t"
    "v_max3_f32 v220, v220, v9, v10\n\t"
    "v_mfma_f32_16x16x32_bf16 v[232:235], v[244:247], v[80:83], v[232:235]\n\t"
    "v_max3_f32 v221, v221, v25, v26\n\t"
    "v_max3_f32 v222, v222, v41, v42\n\t"
    "v_mfma_f32_16x16x32_bf16 a[52:55], a[236:239], v[80:83], a[52:55]\n\t"
    "s_add_u32 m0, s56, 0\n\t"
    "ds_read_b64_tr_b16 a[230:231], v197 offset:31744\n\t"
    "v_max3_f32 v223, v223, v57, v58\n\t"
    "v_mfma_f32_16x16x32_bf16 a[48:51], a[236:239], v[64:67], a[48:51]\n\t"
    "s_waitcnt lgkmcnt(8)\n\t"
    "buffer_load_dwordx4 v201, s[36:39], s52 offen offset:3072 lds\n\t"
    "v_max3_f32 v220, v220, v11, v12\n\t"
    "v_mfma_f32_16x16x32_bf16 a[0:3], a[240:243], v[72:75], a[0:3]\n\t"
    "ds_read_b64_tr_b16 a[232:233], v198 offset:25600\n\t"
    "v_max3_f32 v221, v221, v27, v28\n\t"
    "v_mfma_f32_16x16x32_bf16 a[4:7], a[240:243], v[88:91], a[4:7]\n\t"
    "v_max3_f32 v222, v222, v43, v44\n\t"
    "v_max3_f32 v223, v223, v59, v60\n\t"
    "v_mfma_f32_16x16x32_bf16 v[236:239], v[244:247], v[96:99], v[236:239]\n\t"
    "v_max3_f32 v220, v220, v13, v14\n\t"
    "v_max3_f32 v221, v221, v29, v30\n\t"
    "v_mfma_f32_16x16x32_bf16 a[8:11], a[240:243], v[104:107], a[8:11]\n\t"
    "ds_read_b64_tr_b16 a[234:235], v198 offset:31744\n\t"
    "v_max3_f32 v222, v222, v45, v46\n\t"
    "v_mfma_f32_16x16x32_bf16 a[12:15], a[240:243], v[120:123], a[12:15]\n\t"
    "v_max3_f32 v223, v223, v61, v62\n\t"
    "v_max_f32 v220, v220, v15\n\t"
    "v_mfma_f32_16x16x32_bf16 a[28:31], a[244:247], v[120:123], a[28:31]\n\t"
    "ds_read_b64_tr_b16 a[236:237], v199 offset:25600\n\t"
    "v_max_f32 v221, v221, v31\n\t"
    "v_mfma_f32_16x16x32_bf16 a[24:27], a[244:247], v[104:107], a[24:27]\n\t"
    "v_max_f32 v222, v222, v47\n\t"
    "v_max_f32 v223, v223, v63\n\t"
    "v_mfma_f32_16x16x32_bf16 v[240:243], v[244:247], v[112:115], v[240:243]\n\t"
    "v_cmp_gt_f32 vcc, v220, v216\n\t"
    "v_cmp_gt_f32 s[88:89], v221, v217\n\t"
    "v_mfma_f32_16x16x32_bf16 a[20:23], a[244:247], v[88:91], a[20:23]\n\t"
    "s_or_b64 vcc, vcc, s[88:89]\n\t"
    "ds_read_b64_tr_b16 a[238:239], v199 offset:31744\n\t"
    "v_cmp_gt_f32 s[90:91], v222, v218\n\t"
    "v_mfma_f32_16x16x32_bf16 a[16:19], a[244:247], v[72:75], a[16:19]\n\t"
    "s_waitcnt lgkmcnt(8)\n\t"
    "v_cmp_gt_f32 s[92:93], v223, v219\n\t"
    "s_or_b64 s[88:89], s[90:91], s[92:93]\n\t"
    "v_mfma_f32_16x16x32_bf16 a[32:35], a[248:251], v[72:75], a[32:35]\n\t"
    "s_or_b64 vcc, vcc, s[88:89]\n\t"
    "s_cbranch_scc1 SA4\n\t"
    "NSA4:\n\t"
    "ds_read_b64_tr_b16 a[240:241], v196 offset:37888\n\t"
    "v_mfma_f32_16x16x32_bf16 a[36:39], a[248:251], v[88:91], a[36:39]\n\t"
    "v_fma_f32 v0, v0, s62, v212\n\t"
    "v_fma_f32 v1, v1, s62, v212\n\t"
    "v_mfma_f32_16x16x32_bf16 a[40:43], a[248:251], v[104:107], a[40:43]\n\t"
    "s_add_u32 m0, s56, 896\n\t"
    "ds_read_b64_tr_b16 a[242:243], v196 offset:44032\n\t"
    "v_fma_f32 v2, v2, s62, v212\n\t"
    "v_mfma_f32_16x16x32_bf16 a[44:47], a[248:251], v[120:123], a[44:47]\n\t"
    "buffer_load_dwordx4 v200, s[36:39], s52 offen offset:128 lds\n\t"
    "v_fma_f32 v3, v3, s62, v212\n\t"
    "v_mfma_f32_16x16x32_bf16 a[60:63], a[252:255], v[120:123], a[60:63]\n\t"
    "ds_read_b64_tr_b16 a[244:245], v197 offset:37888\n\t"
    "v_fma_f32 v4, v4, s62, v212\n\t"
    "v_mfma_f32_16x16x32_bf16 a[56:59], a[252:255], v[104:107], a[56:59]\n\t"
    "v_exp_f32 v0, v0\n\t"
    "v_mfma_f32_16x16x32_bf16 v[228:231], v[244:247], v[72:75], v[228:231]\n\t"
    "v_exp_f32 v1, v1\n\t"
    "v_mfma_f32_16x16x32_bf16 a[52:55], a[252:255], v[88:91], a[52:55]\n\t"
    "ds_read_b64_tr_b16 a[246:247], v197 offset:44032\n\t"
    "v_cvt_pk_bf16_f32 v0, v0, v1\n\t"
    "v_mfma_f32_16x16x32_bf16 a[48:51], a[252:255], v[72:75], a[48:51]\n\t"
    "s_waitcnt lgkmcnt(8)\n\t"
    "v_exp_f32 v2, v2\n\t"
    "v_mfma_f32_16x16x32_bf16 a[64:67], a[224:227], v[64:67], a[64:67]\n\t"
    "ds_read_b64_tr_b16 a[248:249], v198 offset:37888\n\t"
    "v_fma_f32 v5, v5, s62, v212\n\t"
    "v_mfma_f32_16x16x32_bf16 a[68:71], a[224:227], v[80:83], a[68:71]\n\t"
    "v_exp_f32 v3, v3\n\t"
    "v_mfma_f32_16x16x32_bf16 v[232:235], v[244:247], v[88:91], v[232:235]\n\t"
    "v_exp_f32 v4, v4\n\t"
    "v_mfma_f32_16x16x32_bf16 a[72:75], a[224:227], v[96:99], a[72:75]\n\t"
    "ds_read_b64_tr_b16 a[250:251], v198 offset:44032\n\t"
    "v_cvt_pk_bf16_f32 v1, v2, v3\n\t"
    "v_mfma_f32_16x16x32_bf16 a[76:79], a[224:227], v[112:115], a[76:79]\n\t"
    "v_exp_f32 v5, v5\n\t"
    "v_mfma_f32_16x16x32_bf16 a[92:95], a[228:231], v[112:115], a[92:95]\n\t"
    "ds_read_b64_tr_b16 a[252:253], v199 offset:37888\n\t"
    "v_cvt_pk_bf16_f32 v2, v4, v5\n\t"
    "v_mfma_f32_16x16x32_bf16 a[88:91], a[228:231], v[96:99], a[88:91]\n\t"
    "v_fma_f32 v6, v6, s62, v212\n\t"
    "v_fma_f32 v7, v7, s62, v212\n\t"
    "v_mfma_f32_16x16x32_bf16 v[236:239], v[244:247], v[104:107], v[236:239]\n\t"
    "v_exp_f32 v6, v6\n\t"
    "v_mfma_f32_16x16x32_bf16 a[84:87], a[228:231], v[80:83], a[84:87]\n\t"
    "s_add_u32 m0, s56, 896\n\t"
    "ds_read_b64_tr_b16 a[254:255], v199 offset:44032\n\t"
    "v_fma_f32 v16, v16, s62, v213\n\t"
    "v_mfma_f32_16x16x32_bf16 a[80:83], a[228:231], v[64:67], a[80:83]\n\t"
    "s_waitcnt lgkmcnt(8)\n\t"
    "buffer_load_dwordx4 v201, s[36:39], s52 offen offset:3200 lds\n\t"
    "v_fma_f32 v17, v17, s62, v213\n\t"
    "v_mfma_f32_16x16x32_bf16 a[96:99], a[232:235], v[64:67], a[96:99]\n\t"
    "ds_read_b64_tr_b16 a[224:225], v196 offset:26624\n\t"
    "v_fma_f32 v18, v18, s62, v213\n\t"
    "v_mfma_f32_16x16x32_bf16 a[100:103], a[232:235], v[80:83], a[100:103]\n\t"
    "v_exp_f32 v7, v7\n\t"
    "v_mfma_f32_16x16x32_bf16 v[240:243], v[244:247], v[120:123], v[240:243]\n\t"
    "v_exp_f32 v16, v16\n\t"
    "v_mfma_f32_16x16x32_bf16 a[104:107], a[232:235], v[96:99], a[104:107]\n\t"
    "ds_read_b64_tr_b16 a[226:227], v196 offset:32768\n\t"
    "v_cvt_pk_bf16_f32 v3, v6, v7\n\t"
    "v_mfma_f32_16x16x32_bf16 a[108:111], a[232:235], v[112:115], a[108:111]\n\t"
    "v_exp_f32 v17, v17\n\t"
    "v_mfma_f32_16x16x32_bf16 a[124:127], a[236:239], v[112:115], a[124:127]\n\t"
    "s_add_u32 m0, s58, 6144\n\t"
    "ds_read_b64_tr_b16 a[228:229], v197 offset:26624\n\t"
    "v_cvt_pk_bf16_f32 v16, v16, v17\n\t"
    "v_mfma_f32_16x16x32_bf16 a[120:123], a[236:239], v[96:99], a[120:123]\n\t"
    "s_add_u32 s75, s54, s99\n\t"
    "buffer_load_dwordx4 v200, s[44:47], s75 offen offset:0 nt lds\n\t"
    "v_fma_f32 v19, v19, s62, v213\n\t"
    "v_mfma_f32_16x16x32_bf16 a[116:119], a[236:239], v[80:83], a[116:119]\n\t"
    "ds_read_b64_tr_b16 a[230:231], v197 offset:32768\n\t"
    "v_fma_f32 v20, v20, s62, v213\n\t"
    "v_mfma_f32_16x16x32_bf16 a[112:115], a[236:239], v[64:67], a[112:115]\n\t"
    "s_waitcnt lgkmcnt(8)\n\t"
    "v_exp_f32 v18, v18\n\t"
    "v_mfma_f32_16x16x32_bf16 a[64:67], a[240:243], v[72:75], a[64:67]\n\t"
    "ds_read_b64_tr_b16 a[232:233], v198 offset:26624\n\t"
    "v_fma_f32 v21, v21, s62, v213\n\t"
    "v_mfma_f32_16x16x32_bf16 a[68:71], a[240:243], v[88:91], a[68:71]\n\t"
    "v_exp_f32 v19, v19\n\t"
    "v_mfma_f32_16x16x32_bf16 a[72:75], a[240:243], v[104:107], a[72:75]\n\t"
    "s_add_u32 m0, s56, 1792\n\t"
    "ds_read_b64_tr_b16 a[234:235], v198 offset:32768\n\t"
    "v_cvt_pk_bf16_f32 v17, v18, v19\n\t"
    "v_mfma_f32_16x16x32_bf16 a[76:79], a[240:243], v[120:123], a[76:79]\n\t"
    "buffer_load_dwordx4 v200, s[36:39], s52 offen offset:256 lds\n\t"
    "v_fma_f32 v22, v22, s62, v213\n\t"
    "v_mfma_f32_16x16x32_bf16 a[92:95], a[244:247], v[120:123], a[92:95]\n\t"
    "ds_read_b64_tr_b16 a[236:237], v199 offset:26624\n\t"
    "v_fma_f32 v23, v23, s62, v213\n\t"
    "v_mfma_f32_16x16x32_bf16 a[88:91], a[244:247], v[104:107], a[88:91]\n\t"
    "v_exp_f32 v20, v20\n\t"
    "v_mfma_f32_16x16x32_bf16 a[84:87], a[244:247], v[88:91], a[84:87]\n\t"
    "ds_read_b64_tr_b16 a[238:239], v199 offset:32768\n\t"
    "v_fma_f32 v32, v32, s62, v214\n\t"
    "v_mfma_f32_16x16x32_bf16 a[80:83], a[244:247], v[72:75], a[80:83]\n\t"
    "s_waitcnt lgkmcnt(8)\n\t"
    "v_exp_f32 v21, v21\n\t"
    "v_mfma_f32_16x16x32_bf16 a[96:99], a[248:251], v[72:75], a[96:99]\n\t"
    "ds_read_b64_tr_b16 a[240:241], v196 offset:38912\n\t"
    "v_cvt_pk_bf16_f32 v18, v20, v21\n\t"
    "v_mfma_f32_16x16x32_bf16 a[100:103], a[248:251], v[88:91], a[100:103]\n\t"
    "v_exp_f32 v22, v22\n\t"
    "v_mfma_f32_16x16x32_bf16 a[104:107], a[248:251], v[104:107], a[104:107]\n\t"
    "ds_read_b64_tr_b16 a[242:243], v196 offset:45056\n\t"
    "v_fma_f32 v33, v33, s62, v214\n\t"
    "v_mfma_f32_16x16x32_bf16 a[108:111], a[248:251], v[120:123], a[108:111]\n\t"
    "v_exp_f32 v23, v23\n\t"
    "v_mfma_f32_16x16x32_bf16 a[124:127], a[252:255], v[120:123], a[124:127]\n\t"
    "ds_read_b64_tr_b16 a[244:245], v197 offset:38912\n\t"
    "v_cvt_pk_bf16_f32 v19, v22, v23\n\t"
    "v_mfma_f32_16x16x32_bf16 a[120:123], a[252:255], v[104:107], a[120:123]\n\t"
    "v_exp_f32 v32, v32\n\t"
    "v_mfma_f32_16x16x32_bf16 a[116:119], a[252:255], v[88:91], a[116:119]\n\t"
    "s_add_u32 m0, s56, 1792\n\t"
    "ds_read_b64_tr_b16 a[246:247], v197 offset:45056\n\t"
    "v_fma_f32 v34, v34, s62, v214\n\t"
    "v_mfma_f32_16x16x32_bf16 a[112:115], a[252:255], v[72:75], a[112:115]\n\t"
    "s_waitcnt lgkmcnt(8)\n\t"
    "buffer_load_dwordx4 v201, s[36:39], s52 offen offset:3328 lds\n\t"
    "v_fma_f32 v35, v35, s62, v214\n\t"
    "v_mfma_f32_16x16x32_bf16 v[128:131], a[224:227], v[64:67], v[128:131]\n\t"
    "ds_read_b64_tr_b16 a[248:249], v198 offset:38912\n\t"
    "v_fma_f32 v36, v36, s62, v214\n\t"
    "v_mfma_f32_16x16x32_bf16 v[132:135], a[224:227], v[80:83], v[132:135]\n\t"
    "v_exp_f32 v33, v33\n\t"
    "v_mfma_f32_16x16x32_bf16 v[136:139], a[224:227], v[96:99], v[136:139]\n\t"
    "ds_read_b64_tr_b16 a[250:251], v198 offset:45056\n\t"
    "v_cvt_pk_bf16_f32 v32, v32, v33\n\t"
    "v_mfma_f32_16x16x32_bf16 v[140:143], a[224:227], v[112:115], v[140:143]\n\t"
    "v_exp_f32 v34, v34\n\t"
    "v_mfma_f32_16x16x32_bf16 v[156:159], a[228:231], v[112:115], v[156:159]\n\t"
    "ds_read_b64_tr_b16 a[252:253], v199 offset:38912\n\t"
    "v_fma_f32 v37, v37, s62, v214\n\t"
    "v_mfma_f32_16x16x32_bf16 v[152:155], a[228:231], v[96:99], v[152:155]\n\t"
    "v_exp_f32 v35, v35\n\t"
    "v_mfma_f32_16x16x32_bf16 v[148:151], a[228:231], v[80:83], v[148:151]\n\t"
    "ds_read_b64_tr_b16 a[254:255], v199 offset:45056\n\t"
    "v_cvt_pk_bf16_f32 v33, v34, v35\n\t"
    "v_mfma_f32_16x16x32_bf16 v[144:147], a[228:231], v[64:67], v[144:147]\n\t"
    "s_waitcnt lgkmcnt(8)\n\t"
    "v_exp_f32 v36, v36\n\t"
    "v_mfma_f32_16x16x32_bf16 v[160:163], a[232:235], v[64:67], v[160:163]\n\t"
    "v_exp_f32 v37, v37\n\t"
    "v_mfma_f32_16x16x32_bf16 v[164:167], a[232:235], v[80:83], v[164:167]\n\t"
    "v_cvt_pk_bf16_f32 v34, v36, v37\n\t"
    "v_fma_f32 v38, v38, s62, v214\n\t"
    "v_mfma_f32_16x16x32_bf16 v[168:171], a[232:235], v[96:99], v[168:171]\n\t"
    "s_add_u32 s52, s52, 24576\n\t"
    "v_exp_f32 v38, v38\n\t"
    "v_mfma_f32_16x16x32_bf16 v[172:175], a[232:235], v[112:115], v[172:175]\n\t"
    "v_fma_f32 v39, v39, s62, v214\n\t"
    "v_fma_f32 v48, v48, s62, v215\n\t"
    "v_mfma_f32_16x16x32_bf16 v[188:191], a[236:239], v[112:115], v[188:191]\n\t"
    "v_exp_f32 v39, v39\n\t"
    "v_mfma_f32_16x16x32_bf16 v[184:187], a[236:239], v[96:99], v[184:187]\n\t"
    "v_exp_f32 v48, v48\n\t"
    "v_mfma_f32_16x16x32_bf16 v[180:183], a[236:239], v[80:83], v[180:183]\n\t"
    "s_waitcnt lgkmcnt(0) vmcnt(17)\n\t"
    "v_cvt_pk_bf16_f32 v35, v38, v39\n\t"
    "v_fma_f32 v49, v49, s62, v215\n\t"
    "v_mfma_f32_16x16x32_bf16 v[176:179], a[236:239], v[64:67], v[176:179]\n\t"
    "v_exp_f32 v49, v49\n\t"
    "v_mfma_f32_16x16x32_bf16 v[128:131], a[240:243], v[72:75], v[128:131]\n\t"
    "s_barrier\n\t"
    "ds_read_b128 a[224:227], v192 offset:24576\n\t"
    "v_cvt_pk_bf16_f32 v48, v48, v49\n\t"
    "v_mfma_f32_16x16x32_bf16 v[132:135], a[240:243], v[88:91], v[132:135]\n\t"
    "s_add_u32 m0, s58, 4096\n\t"
    "ds_read_b128 a[228:231], v192 offset:30720\n\t"
    "v_fma_f32 v50, v50, s62, v215\n\t"
    "v_mfma_f32_16x16x32_bf16 v[136:139], a[240:243], v[104:107], v[136:139]\n\t"
    "s_add_u32 s75, s54, s99\n\t"
    "ds_read_b128 a[232:235], v192 offset:36864\n\t"
    "v_fma_f32 v51, v51, s62, v215\n\t"
    "v_mfma_f32_16x16x32_bf16 v[140:143], a[240:243], v[120:123], v[140:143]\n\t"
    "buffer_load_dwordx4 v201, s[44:47], s75 offen offset:3072 nt lds\n\t"
    "v_fma_f32 v52, v52, s62, v215\n\t"
    "v_mfma_f32_16x16x32_bf16 v[156:159], a[244:247], v[120:123], v[156:159]\n\t"
    "v_exp_f32 v50, v50\n\t"
    "v_mfma_f32_16x16x32_bf16 v[152:155], a[244:247], v[104:107], v[152:155]\n\t"
    "v_exp_f32 v51, v51\n\t"
    "v_mfma_f32_16x16x32_bf16 v[148:151], a[244:247], v[88:91], v[148:151]\n\t"
    "ds_read_b128 a[236:239], v192 offset:43008\n\t"
    "v_cvt_pk_bf16_f32 v49, v50, v51\n\t"
    "v_mfma_f32_16x16x32_bf16 v[144:147], a[244:247], v[72:75], v[144:147]\n\t"
    "v_exp_f32 v52, v52\n\t"
    "v_mfma_f32_16x16x32_bf16 v[160:163], a[248:251], v[72:75], v[160:163]\n\t"
    "v_fma_f32 v53, v53, s62, v215\n\t"
    "v_fma_f32 v54, v54, s62, v215\n\t"
    "v_mfma_f32_16x16x32_bf16 v[164:167], a[248:251], v[88:91], v[164:167]\n\t"
    "v_exp_f32 v53, v53\n\t"
    "v_mfma_f32_16x16x32_bf16 v[168:171], a[248:251], v[104:107], v[168:171]\n\t"
    "ds_read_b128 a[240:243], v193 offset:24576\n\t"
    "v_cvt_pk_bf16_f32 v50, v52, v53\n\t"
    "v_mfma_f32_16x16x32_bf16 v[172:175], a[248:251], v[120:123], v[172:175]\n\t"
    "v_exp_f32 v54, v54\n\t"
    "v_mfma_f32_16x16x32_bf16 v[188:191], a[252:255], v[120:123], v[188:191]\n\t"
    "v_fma_f32 v55, v55, s62, v215\n\t"
    "v_fma_f32 v8, v8, s62, v212\n\t"
    "v_mfma_f32_16x16x32_bf16 v[184:187], a[252:255], v[104:107], v[184:187]\n\t"
    "v_exp_f32 v55, v55\n\t"
    "v_mfma_f32_16x16x32_bf16 v[180:183], a[252:255], v[88:91], v[180:183]\n\t"
    "ds_read_b128 a[244:247], v193 offset:30720\n\t"
    "v_cvt_pk_bf16_f32 v51, v54, v55\n\t"
    "v_mfma_f32_16x16x32_bf16 v[176:179], a[252:255], v[72:75], v[176:179]\n\t"
    "s_waitcnt lgkmcnt(4)\n\t"
    "v_exp_f32 v8, v8\n\t"
    "v_mfma_f32_16x16x32_bf16 v[64:67], a[224:227], a[128:131], v[224:227]\n\t"
    "s_cmp_lg_u32 s72, 0\n\t"
    "s_cbranch_scc1 SB4\n\t"
    "NSB4:\n\t"
    "v_fma_f32 v9, v9, s62, v212\n\t"
    "v_mfma_f32_16x16x32_bf16 v[80:83], a[224:227], a[132:135], v[224:227]\n\t"
    "v_exp_f32 v9, v9\n\t"
    "v_mfma_f32_16x16x32_bf16 v[96:99], a[224:227], a[136:139], v[224:227]\n\t"
    "s_add_u32 m0, s57, 24576\n\t"
    "ds_read_b128 a[248:251], v193 offset:36864\n\t"
    "v_cvt_pk_bf16_f32 v8, v8, v9\n\t"
    "v_mfma_f32_16x16x32_bf16 v[112:115], a[224:227], a[140:143], v[224:227]\n\t"
    "buffer_load_dwordx4 v202, s[40:43], s53 offen offset:0 lds\n\t"
    "v_fma_f32 v10, v10, s62, v212\n\t"
    "v_mfma_f32_16x16x32_bf16 v[116:119], a[228:231], a[140:143], v[224:227]\n\t"
    "v_exp_f32 v10, v10\n\t"
    "v_mfma_f32_16x16x32_bf16 v[100:103], a[228:231], a[136:139], v[224:227]\n\t"
    "v_fma_f32 v11, v11, s62, v212\n\t"
    "v_fma_f32 v12, v12, s62, v212\n\t"
    "v_mfma_f32_16x16x32_bf16 v[84:87], a[228:231], a[132:135], v[224:227]\n\t"
    "ds_read_b128 a[252:255], v193 offset:43008\n\t"
    "v_fma_f32 v13, v13, s62, v212\n\t"
    "v_mfma_f32_16x16x32_bf16 v[68:71], a[228:231], a[128:131], v[224:227]\n\t"
    "s_waitcnt lgkmcnt(4)\n\t"
    "v_exp_f32 v11, v11\n\t"
    "v_mfma_f32_16x16x32_bf16 v[72:75], a[232:235], a[128:131], v[224:227]\n\t"
    "v_exp_f32 v12, v12\n\t"
    "v_mfma_f32_16x16x32_bf16 v[88:91], a[232:235], a[132:135], v[224:227]\n\t"
    "v_exp_f32 v13, v13\n\t"
    "v_mfma_f32_16x16x32_bf16 v[104:107], a[232:235], a[136:139], v[224:227]\n\t"
    "ds_read_b128 a[224:227], v192 offset:25600\n\t"
    "v_cvt_pk_bf16_f32 v9, v10, v11\n\t"
    "v_mfma_f32_16x16x32_bf16 v[120:123], a[232:235], a[140:143], v[224:227]\n\t"
    "v_cvt_pk_bf16_f32 v10, v12, v13\n\t"
    "v_fma_f32 v14, v14, s62, v212\n\t"
    "v_mfma_f32_16x16x32_bf16 v[124:127], a[236:239], a[140:143], v[224:227]\n\t"
    "v_exp_f32 v14, v14\n\t"
    "v_mfma_f32_16x16x32_bf16 v[108:111], a[236:239], a[136:139], v[224:227]\n\t"
    "v_fma_f32 v15, v15, s62, v212\n\t"
    "v_fma_f32 v24, v24, s62, v213\n\t"
    "v_mfma_f32_16x16x32_bf16 v[92:95], a[236:239], a[132:135], v[224:227]\n\t"
    "s_add_u32 m0, s57, 24576\n\t"
    "ds_read_b128 a[228:231], v192 offset:31744\n\t"
    "v_fma_f32 v25, v25, s62, v213\n\t"
    "v_mfma_f32_16x16x32_bf16 v[76:79], a[236:239], a[128:131], v[224:227]\n\t"
    "s_waitcnt lgkmcnt(4)\n\t"
    "buffer_load_dwordx4 v202, s[40:43], s53 offen offset:3072 lds\n\t"
    "v_fma_f32 v26, v26, s62, v213\n\t"
    "v_mfma_f32_16x16x32_bf16 v[64:67], a[240:243], a[144:147], v[64:67]\n\t"
    "v_exp_f32 v15, v15\n\t"
    "v_mfma_f32_16x16x32_bf16 v[80:83], a[240:243], a[148:151], v[80:83]\n\t"
    "v_exp_f32 v24, v24\n\t"
    "v_mfma_f32_16x16x32_bf16 v[96:99], a[240:243], a[152:155], v[96:99]\n\t"
    "ds_read_b128 a[232:235], v192 offset:37888\n\t"
    "v_cvt_pk_bf16_f32 v11, v14, v15\n\t"
    "v_mfma_f32_16x16x32_bf16 v[112:115], a[240:243], a[156:159], v[112:115]\n\t"
    "v_exp_f32 v25, v25\n\t"
    "v_mfma_f32_16x16x32_bf16 v[116:119], a[244:247], a[156:159], v[116:119]\n\t"
    "v_exp_f32 v26, v26\n\t"
    "v_mfma_f32_16x16x32_bf16 v[100:103], a[244:247], a[152:155], v[100:103]\n\t"
    "v_cvt_pk_bf16_f32 v24, v24, v25\n\t"
    "v_fma_f32 v27, v27, s62, v213\n\t"
    "v_mfma_f32_16x16x32_bf16 v[84:87], a[244:247], a[148:151], v[84:87]\n\t"
    "ds_read_b128 a[236:239], v192 offset:44032\n\t"
    "v_fma_f32 v28, v28, s62, v213\n\t"
    "v_mfma_f32_16x16x32_bf16 v[68:71], a[244:247], a[144:147], v[68:71]\n\t"
    "s_waitcnt lgkmcnt(4)\n\t"
    "v_exp_f32 v27, v27\n\t"
    "v_mfma_f32_16x16x32_bf16 v[72:75], a[248:251], a[144:147], v[72:75]\n\t"
    "v_exp_f32 v28, v28\n\t"
    "v_mfma_f32_16x16x32_bf16 v[88:91], a[248:251], a[148:151], v[88:91]\n\t"
    "v_cvt_pk_bf16_f32 v25, v26, v27\n\t"
    "v_fma_f32 v29, v29, s62, v213\n\t"
    "v_mfma_f32_16x16x32_bf16 v[104:107], a[248:251], a[152:155], v[104:107]\n\t"
    "s_add_u32 m0, s57, 25472\n\t"
    "ds_read_b128 a[240:243], v193 offset:25600\n\t"
    "v_fma_f32 v30, v30, s62, v213\n\t"
    "v_mfma_f32_16x16x32_bf16 v[120:123], a[248:251], a[156:159], v[120:123]\n\t"
    "buffer_load_dwordx4 v202, s[40:43], s53 offen offset:128 lds\n\t"
    "v_fma_f32 v31, v31, s62, v213\n\t"
    "v_mfma_f32_16x16x32_bf16 v[124:127], a[252:255], a[156:159], v[124:127]\n\t"
    "v_exp_f32 v29, v29\n\t"
    "v_mfma_f32_16x16x32_bf16 v[108:111], a[252:255], a[152:155], v[108:111]\n\t"
    "v_exp_f32 v30, v30\n\t"
    "v_mfma_f32_16x16x32_bf16 v[92:95], a[252:255], a[148:151], v[92:95]\n\t"
    "ds_read_b128 a[244:247], v193 offset:31744\n\t"
    "v_cvt_pk_bf16_f32 v26, v28, v29\n\t"
    "v_mfma_f32_16x16x32_bf16 v[76:79], a[252:255], a[144:147], v[76:79]\n\t"
    "s_waitcnt lgkmcnt(4)\n\t"
    "v_exp_f32 v31, v31\n\t"
    "v_mfma_f32_16x16x32_bf16 v[64:67], a[224:227], a[160:163], v[64:67]\n\t"
    "v_cvt_pk_bf16_f32 v27, v30, v31\n\t"
    "v_fma_f32 v40, v40, s62, v214\n\t"
    "v_mfma_f32_16x16x32_bf16 v[80:83], a[224:227], a[164:167], v[80:83]\n\t"
    "v_exp_f32 v40, v40\n\t"
    "v_mfma_f32_16x16x32_bf16 v[96:99], a[224:227], a[168:171], v[96:99]\n\t"
    "ds_read_b128 a[248:251], v193 offset:37888\n\t"
    "v_fma_f32 v41, v41, s62, v214\n\t"
    "v_mfma_f32_16x16x32_bf16 v[112:115], a[224:227], a[172:175], v[112:115]\n\t"
    "v_exp_f32 v41, v41\n\t"
    "v_mfma_f32_16x16x32_bf16 v[116:119], a[228:231], a[172:175], v[116:119]\n\t"
    "v_cvt_pk_bf16_f32 v40, v40, v41\n\t"
    "v_fma_f32 v42, v42, s62, v214\n\t"
    "v_mfma_f32_16x16x32_bf16 v[100:103], a[228:231], a[168:171], v[100:103]\n\t"
    "v_exp_f32 v42, v42\n\t"
    "v_mfma_f32_16x16x32_bf16 v[84:87], a[228:231], a[164:167], v[84:87]\n\t"
    "s_add_u32 m0, s57, 25472\n\t"
    "ds_read_b128 a[252:255], v193 offset:44032\n\t"
    "v_fma_f32 v43, v43, s62, v214\n\t"
    "v_mfma_f32_16x16x32_bf16 v[68:71], a[228:231], a[160:163], v[68:71]\n\t"
    "s_waitcnt lgkmcnt(4)\n\t"
    "buffer_load_dwordx4 v202, s[40:43], s53 offen offset:3200 lds\n\t"
    "v_fma_f32 v44, v44, s62, v214\n\t"
    "v_mfma_f32_16x16x32_bf16 v[72:75], a[232:235], a[160:163], v[72:75]\n\t"
    "v_exp_f32 v43, v43\n\t"
    "v_mfma_f32_16x16x32_bf16 v[88:91], a[232:235], a[164:167], v[88:91]\n\t"
    "v_exp_f32 v44, v44\n\t"
    "v_mfma_f32_16x16x32_bf16 v[104:107], a[232:235], a[168:171], v[104:107]\n\t"
    "ds_read_b128 a[224:227], v192 offset:26624\n\t"
    "v_cvt_pk_bf16_f32 v41, v42, v43\n\t"
    "v_mfma_f32_16x16x32_bf16 v[120:123], a[232:235], a[172:175], v[120:123]\n\t"
    "v_fma_f32 v45, v45, s62, v214\n\t"
    "v_fma_f32 v46, v46, s62, v214\n\t"
    "v_mfma_f32_16x16x32_bf16 v[124:127], a[236:239], a[172:175], v[124:127]\n\t"
    "s_add_u32 m0, s58, 8064\n\t"
    "v_exp_f32 v45, v45\n\t"
    "v_mfma_f32_16x16x32_bf16 v[108:111], a[236:239], a[168:171], v[108:111]\n\t"
    "s_add_u32 s75, s54, s96\n\t"
    "buffer_load_dwordx4 v200, s[44:47], s75 offen offset:128 nt lds\n\t"
    "v_cvt_pk_bf16_f32 v42, v44, v45\n\t"
    "v_mfma_f32_16x16x32_bf16 v[92:95], a[236:239], a[164:167], v[92:95]\n\t"
    "ds_read_b128 a[228:231], v192 offset:32768\n\t"
    "v_fma_f32 v47, v47, s62, v214\n\t"
    "v_mfma_f32_16x16x32_bf16 v[76:79], a[236:239], a[160:163], v[76:79]\n\t"
    "s_waitcnt lgkmcnt(4)\n\t"
    "v_exp_f32 v46, v46\n\t"
    "v_mfma_f32_16x16x32_bf16 v[64:67], a[240:243], a[176:179], v[64:67]\n\t"
    "v_exp_f32 v47, v47\n\t"
    "v_mfma_f32_16x16x32_bf16 v[80:83], a[240:243], a[180:183], v[80:83]\n\t"
    "v_cvt_pk_bf16_f32 v43, v46, v47\n\t"
    "v_fma_f32 v56, v56, s62, v215\n\t"
    "v_mfma_f32_16x16x32_bf16 v[96:99], a[240:243], a[184:187], v[96:99]\n\t"
    "s_add_u32 m0, s57, 26368\n\t"
    "ds_read_b128 a[232:235], v192 offset:38912\n\t"
    "v_fma_f32 v57, v57, s62, v215\n\t"
    "v_mfma_f32_16x16x32_bf16 v[112:115], a[240:243], a[188:191], v[112:115]\n\t"
    "buffer_load_dwordx4 v202, s[40:43], s53 offen offset:256 lds\n\t"
    "v_fma_f32 v58, v58, s62, v215\n\t"
    "v_mfma_f32_16x16x32_bf16 v[116:119], a[244:247], a[188:191], v[116:119]\n\t"
    "v_exp_f32 v56, v56\n\t"
    "v_mfma_f32_16x16x32_bf16 v[100:103], a[244:247], a[184:187], v[100:103]\n\t"
    "v_exp_f32 v57, v57\n\t"
    "v_mfma_f32_16x16x32_bf16 v[84:87], a[244:247], a[180:183], v[84:87]\n\t"
    "ds_read_b128 a[236:239], v192 offset:45056\n\t"
    "v_cvt_pk_bf16_f32 v56, v56, v57\n\t"
    "v_mfma_f32_16x16x32_bf16 v[68:71], a[244:247], a[176:179], v[68:71]\n\t"
    "s_waitcnt lgkmcnt(4)\n\t"
    "v_exp_f32 v58, v58\n\t"
    "v_mfma_f32_16x16x32_bf16 v[72:75], a[248:251], a[176:179], v[72:75]\n\t"
    "v_fma_f32 v59, v59, s62, v215\n\t"
    "v_fma_f32 v60, v60, s62, v215\n\t"
    "v_mfma_f32_16x16x32_bf16 v[88:91], a[248:251], a[180:183], v[88:91]\n\t"
    "v_exp_f32 v59, v59\n\t"
    "v_mfma_f32_16x16x32_bf16 v[104:107], a[248:251], a[184:187], v[104:107]\n\t"
    "ds_read_b128 a[240:243], v193 offset:26624\n\t"
    "v_cvt_pk_bf16_f32 v57, v58, v59\n\t"
    "v_mfma_f32_16x16x32_bf16 v[120:123], a[248:251], a[188:191], v[120:123]\n\t"
    "v_exp_f32 v60, v60\n\t"
    "v_mfma_f32_16x16x32_bf16 v[124:127], a[252:255], a[188:191], v[124:127]\n\t"
    "v_fma_f32 v61, v61, s62, v215\n\t"
    "v_fma_f32 v62, v62, s62, v215\n\t"
    "v_mfma_f32_16x16x32_bf16 v[108:111], a[252:255], a[184:187], v[108:111]\n\t"
    "v_exp_f32 v61, v61\n\t"
    "v_mfma_f32_16x16x32_bf16 v[92:95], a[252:255], a[180:183], v[92:95]\n\t"
    "s_add_u32 m0, s57, 26368\n\t"
    "ds_read_b128 a[244:247], v193 offset:32768\n\t"
    "v_cvt_pk_bf16_f32 v58, v60, v61\n\t"
    "v_mfma_f32_16x16x32_bf16 v[76:79], a[252:255], a[176:179], v[76:79]\n\t"
    "s_waitcnt lgkmcnt(4)\n\t"
    "buffer_load_dwordx4 v202, s[40:43], s53 offen offset:3328 lds\n\t"
    "v_fma_f32 v63, v63, s62, v215\n\t"
    "v_mfma_f32_16x16x32_bf16 v[64:67], a[224:227], a[192:195], v[64:67]\n\t"
    "v_exp_f32 v62, v62\n\t"
    "v_mfma_f32_16x16x32_bf16 v[80:83], a[224:227], a[196:199], v[80:83]\n\t"
    "v_exp_f32 v63, v63\n\t"
    "v_mfma_f32_16x16x32_bf16 v[96:99], a[224:227], a[200:203], v[96:99]\n\t"
    "ds_read_b128 a[248:251], v193 offset:38912\n\t"
    "v_cvt_pk_bf16_f32 v59, v62, v63\n\t"
    "v_mfma_f32_16x16x32_bf16 v[112:115], a[224:227], a[204:207], v[112:115]\n\t"
    "v_mfma_f32_16x16x32_bf16 v[116:119], a[228:231], a[204:207], v[116:119]\n\t"
    "v_mfma_f32_16x16x32_bf16 v[100:103], a[228:231], a[200:203], v[100:103]\n\t"
    "v_mfma_f32_16x16x32_bf16 v[84:87], a[228:231], a[196:199], v[84:87]\n\t"
    "ds_read_b128 a[252:255], v193 offset:45056\n\t"
    "v_mfma_f32_16x16x32_bf16 v[68:71], a[228:231], a[192:195], v[68:71]\n\t"
    "s_waitcnt lgkmcnt(4)\n\t"
    "v_mfma_f32_16x16x32_bf16 v[72:75], a[232:235], a[192:195], v[72:75]\n\t"
    "v_mfma_f32_16x16x32_bf16 v[88:91], a[232:235], a[196:199], v[88:91]\n\t"
    "v_mfma_f32_16x16x32_bf16 v[104:107], a[232:235], a[200:203], v[104:107]\n\t"
    "s_add_u32 s53, s53, 24576\n\t"
    "v_mfma_f32_16x16x32_bf16 v[120:123], a[232:235], a[204:207], v[120:123]\n\t"
    "v_mfma_f32_16x16x32_bf16 v[124:127], a[236:239], a[204:207], v[124:127]\n\t"
    "v_mfma_f32_16x16x32_bf16 v[108:111], a[236:239], a[200:203], v[108:111]\n\t"
    "v_mfma_f32_16x16x32_bf16 v[92:95], a[236:239], a[196:199], v[92:95]\n\t"
    "s_waitcnt lgkmcnt(0) vmcnt(16)\n\t"
    "v_mfma_f32_16x16x32_bf16 v[76:79], a[236:239], a[192:195], v[76:79]\n\t"
    "v_mfma_f32_16x16x32_bf16 v[64:67], a[240:243], a[208:211], v[64:67]\n\t"
    "s_barrier\n\t"
    "ds_read_b64_tr_b16 a[224:225], v196 offset:0\n\t"
    "v_mfma_f32_16x16x32_bf16 v[80:83], a[240:243], a[212:215], v[80:83]\n\t"
    "s_add_u32 m0, s58, 6016\n\t"
    "ds_read_b64_tr_b16 a[226:227], v196 offset:6144\n\t"
    "v_mfma_f32_16x16x32_bf16 v[96:99], a[240:243], a[216:219], v[96:99]\n\t"
    "s_add_u32 s75, s54, s96\n\t"
    "ds_read_b64_tr_b16 a[228:229], v197 offset:0\n\t"
    "v_mfma_f32_16x16x32_bf16 v[112:115], a[240:243], a[220:223], v[112:115]\n\t"
    "ds_read_b64_tr_b16 a[230:231], v197 offset:6144\n\t"
    "v_mfma_f32_16x16x32_bf16 v[116:119], a[244:247], a[220:223], v[116:119]\n\t"
    "ds_read_b64_tr_b16 a[232:233], v198 offset:0\n\t"
    "v_mfma_f32_16x16x32_bf16 v[100:103], a[244:247], a[216:219], v[100:103]\n\t"
    "ds_read_b64_tr_b16 a[234:235], v198 offset:6144\n\t"
    "v_mfma_f32_16x16x32_bf16 v[84:87], a[244:247], a[212:215], v[84:87]\n\t"
    "ds_read_b64_tr_b16 a[236:237], v199 offset:0\n\t"
    "v_mfma_f32_16x16x32_bf16 v[68:71], a[244:247], a[208:211], v[68:71]\n\t"
    "ds_read_b64_tr_b16 a[238:239], v199 offset:6144\n\t"
    "v_mfma_f32_16x16x32_bf16 v[72:75], a[248:251], a[208:211], v[72:75]\n\t"
    "ds_read_b64_tr_b16 a[240:241], v196 offset:12288\n\t"
    "v_mfma_f32_16x16x32_bf16 v[88:91], a[248:251], a[212:215], v[88:91]\n\t"
    "buffer_load_dwordx4 v201, s[44:47], s75 offen offset:3200 nt lds\n\t"
    "v_mfma_f32_16x16x32_bf16 v[104:107], a[248:251], a[216:219], v[104:107]\n\t"
    "ds_read_b64_tr_b16 a[242:243], v196 offset:18432\n\t"
    "v_mfma_f32_16x16x32_bf16 v[120:123], a[248:251], a[220:223], v[120:123]\n\t"
    "v_mfma_f32_16x16x32_bf16 v[124:127], a[252:255], a[220:223], v[124:127]\n\t"
    "ds_read_b64_tr_b16 a[244:245], v197 offset:12288\n\t"
    "v_mfma_f32_16x16x32_bf16 v[108:111], a[252:255], a[216:219], v[108:111]\n\t"
    "v_mfma_f32_16x16x32_bf16 v[92:95], a[252:255], a[212:215], v[92:95]\n\t"
    "ds_read_b64_tr_b16 a[246:247], v197 offset:18432\n\t"
    "v_mfma_f32_16x16x32_bf16 v[76:79], a[252:255], a[208:211], v[76:79]\n\t"
    "s_waitcnt lgkmcnt(8)\n\t"
    "v_mfma_f32_16x16x32_bf16 a[0:3], a[224:227], v[0:3], a[0:3]\n\t"
    "ds_read_b64_tr_b16 a[248:249], v198 offset:12288\n\t"
    "v_mfma_f32_16x16x32_bf16 a[4:7], a[224:227], v[16:19], a[4:7]\n\t"
    "v_mfma_f32_16x16x32_bf16 a[8:11], a[224:227], v[32:35], a[8:11]\n\t"
    "s_add_u32 m0, s56, 24576\n\t"
    "ds_read_b64_tr_b16 a[250:251], v198 offset:18432\n\t"
    "v_mfma_f32_16x16x32_bf16 a[12:15], a[224:227], v[48:51], a[12:15]\n\t"
    "buffer_load_dwordx4 v200, s[36:39], s52 offen offset:0 lds\n\t"
    "v_mfma_f32_16x16x32_bf16 a[28:31], a[228:231], v[48:51], a[28:31]\n\t"
    "ds_read_b64_tr_b16 a[252:253], v199 offset:12288\n\t"
    "v_max3_f32 v220, v64, v65, v66\n\t"
    "v_mfma_f32_16x16x32_bf16 a[24:27], a[228:231], v[32:35], a[24:27]\n\t"
    "v_max3_f32 v221, v80, v81, v82\n\t"
    "v_max3_f32 v222, v96, v97, v98\n\t"
    "v_mfma_f32_16x16x32_bf16 a[20:23], a[228:231], v[16:19], a[20:23]\n\t"
    "ds_read_b64_tr_b16 a[254:255], v199 offset:18432\n\t"
    "v_max3_f32 v223, v112, v113, v114\n\t"
    "v_mfma_f32_16x16x32_bf16 a[16:19], a[228:231], v[0:3], a[16:19]\n\t"
    "s_waitcnt lgkmcnt(8)\n\t"
    "v_max3_f32 v220, v220, v67, v68\n\t"
    "v_max3_f32 v221, v221, v83, v84\n\t"
    "v_mfma_f32_16x16x32_bf16 a[32:35], a[232:235], v[0:3], a[32:35]\n\t"
    "ds_read_b64_tr_b16 a[224:225], v196 offset:1024\n\t"
    "v_max3_f32 v222, v222, v99, v100\n\t"
    "v_mfma_f32_16x16x32_bf16 a[36:39], a[232:235], v[16:19], a[36:39]\n\t"
    "v_max3_f32 v223, v223, v115, v116\n\t"
    "v_max3_f32 v220, v220, v69, v70\n\t"
    "v_mfma_f32_16x16x32_bf16 v[228:231], v[244:247], v[0:3], v[228:231]\n\t"
    "v_max3_f32 v221, v221, v85, v86\n\t"
    "v_max3_f32 v222, v222, v101, v102\n\t"
    "v_mfma_f32_16x16x32_bf16 a[40:43], a[232:235], v[32:35], a[40:43]\n\t"
    "ds_read_b64_tr_b16 a[226:227], v196 offset:7168\n\t"
    "v_max3_f32 v223, v223, v117, v118\n\t"
    "v_mfma_f32_16x16x32_bf16 a[44:47], a[232:235], v[48:51], a[44:47]\n\t"
    "v_max3_f32 v220, v220, v71, v72\n\t"
    "v_max3_f32 v221, v221, v87, v88\n\t"
    "v_mfma_f32_16x16x32_bf16 a[60:63], a[236:239], v[48:51], a[60:63]\n\t"
    "ds_read_b64_tr_b16 a[228:229], v197 offset:1024\n\t"
    "v_max3_f32 v222, v222, v103, v104\n\t"
    "v_mfma_f32_16x16x32_bf16 a[56:59], a[236:239], v[32:35], a[56:59]\n\t"
    "v_max3_f32 v223, v223, v119, v120\n\t"
    "v_max3_f32 v220, v220, v73, v74\n\t"
    "v_mfma_f32_16x16x32_bf16 v[232:235], v[244:247], v[16:19], v[232:235]\n\t"
    "v_max3_f32 v221, v221, v89, v90\n\t"
    "v_max3_f32 v222, v222, v105, v106\n\t"
    "v_mfma_f32_16x16x32_bf16 a[52:55], a[236:239], v[16:19], a[52:55]\n\t"
    "s_add_u32 m0, s56, 24576\n\t"
    "ds_read_b64_tr_b16 a[230:231], v197 offset:7168\n\t"
    "v_max3_f32 v223, v223, v121, v122\n\t"
    "v_mfma_f32_16x16x32_bf16 a[48:51], a[236:239], v[0:3], a[48:51]\n\t"
    "s_waitcnt lgkmcnt(8)\n\t"
    "buffer_load_dwordx4 v201, s[36:39], s52 offen offset:3072 lds\n\t"
    "v_max3_f32 v220, v220, v75, v76\n\t"
    "v_mfma_f32_16x16x32_bf16 a[0:3], a[240:243], v[8:11], a[0:3]\n\t"
    "ds_read_b64_tr_b16 a[232:233], v198 offset:1024\n\t"
    "v_max3_f32 v221, v221, v91, v92\n\t"
    "v_mfma_f32_16x16x32_bf16 a[4:7], a[240:243], v[24:27], a[4:7]\n\t"
    "v_max3_f32 v222, v222, v107, v108\n\t"
    "v_max3_f32 v223, v223, v123, v124\n\t"
    "v_mfma_f32_16x16x32_bf16 v[236:239], v[244:247], v[32:35], v[236:239]\n\t"
    "v_max3_f32 v220, v220, v77, v78\n\t"
    "v_max3_f32 v221, v221, v93, v94\n\t"
    "v_mfma_f32_16x16x32_bf16 a[8:11], a[240:243], v[40:43], a[8:11]\n\t"
    "ds_read_b64_tr_b16 a[234:235], v198 offset:7168\n\t"
    "v_max3_f32 v222, v222, v109, v110\n\t"
    "v_mfma_f32_16x16x32_bf16 a[12:15], a[240:243], v[56:59], a[12:15]\n\t"
    "v_max3_f32 v223, v223, v125, v126\n\t"
    "v_max_f32 v220, v220, v79\n\t"
    "v_mfma_f32_16x16x32_bf16 a[28:31], a[244:247], v[56:59], a[28:31]\n\t"
    "ds_read_b64_tr_b16 a[236:237], v199 offset:1024\n\t"
    "v_max_f32 v221, v221, v95\n\t"
    "v_mfma_f32_16x16x32_bf16 a[24:27], a[244:247], v[40:43], a[24:27]\n\t"
    "v_max_f32 v222, v222, v111\n\t"
    "v_max_f32 v223, v223, v127\n\t"
    "v_mfma_f32_16x16x32_bf16 v[240:243], v[244:247], v[48:51], v[240:243]\n\t"
    "v_cmp_gt_f32 vcc, v220, v216\n\t"
    "v_cmp_gt_f32 s[88:89], v221, v217\n\t"
    "v_mfma_f32_16x16x32_bf16 a[20:23], a[244:247], v[24:27], a[20:23]\n\t"
    "s_or_b64 vcc, vcc, s[88:89]\n\t"
    "ds_read_b64_tr_b16 a[238:239], v199 offset:7168\n\t"
    "v_cmp_gt_f32 s[90:91], v222, v218\n\t"
    "v_mfma_f32_16x16x32_bf16 a[16:19], a[244:247], v[8:11], a[16:19]\n\t"
    "s_waitcnt lgkmcnt(8)\n\t"
    "v_cmp_gt_f32 s[92:93], v223, v219\n\t"
    "s_or_b64 s[88:89], s[90:91], s[92:93]\n\t"
    "v_mfma_f32_16x16x32_bf16 a[32:35], a[248:251], v[8:11], a[32:35]\n\t"
    "s_or_b64 vcc, vcc, s[88:89]\n\t"
    "s_cbranch_scc1 SA5\n\t"
    "NSA5:\n\t"
    "ds_read_b64_tr_b16 a[240:241], v196 offset:13312\n\t"
    "v_mfma_f32_16x16x32_bf16 a[36:39], a[248:251], v[24:27], a[36:39]\n\t"
    "v_fma_f32 v64, v64, s62, v212\n\t"
    "v_fma_f32 v65, v65, s62, v212\n\t"
    "v_mfma_f32_16x16x32_bf16 a[40:43], a[248:251], v[40:43], a[40:43]\n\t"
    "s_add_u32 m0, s56, 25472\n\t"
    "ds_read_b64_tr_b16 a[242:243], v196 offset:19456\n\t"
    "v_fma_f32 v66, v66, s62, v212\n\t"
    "v_mfma_f32_16x16x32_bf16 a[44:47], a[248:251], v[56:59], a[44:47]\n\t"
    "buffer_load_dwordx4 v200, s[36:39], s52 offen offset:128 lds\n\t"
    "v_fma_f32 v67, v67, s62, v212\n\t"
    "v_mfma_f32_16x16x32_bf16 a[60:63], a[252:255], v[56:59], a[60:63]\n\t"
    "ds_read_b64_tr_b16 a[244:245], v197 offset:13312\n\t"
    "v_fma_f32 v68, v68, s62, v212\n\t"
    "v_mfma_f32_16x16x32_bf16 a[56:59], a[252:255], v[40:43], a[56:59]\n\t"
    "v_exp_f32 v64, v64\n\t"
    "v_mfma_f32_16x16x32_bf16 v[228:231], v[244:247], v[8:11], v[228:231]\n\t"
    "v_exp_f32 v65, v65\n\t"
    "v_mfma_f32_16x16x32_bf16 a[52:55], a[252:255], v[24:27], a[52:55]\n\t"
    "ds_read_b64_tr_b16 a[246:247], v197 offset:19456\n\t"
    "v_cvt_pk_bf16_f32 v64, v64, v65\n\t"
    "v_mfma_f32_16x16x32_bf16 a[48:51], a[252:255], v[8:11], a[48:51]\n\t"
    "s_waitcnt lgkmcnt(8)\n\t"
    "v_exp_f32 v66, v66\n\t"
    "v_mfma_f32_16x16x32_bf16 a[64:67], a[224:227], v[0:3], a[64:67]\n\t"
    "ds_read_b64_tr_b16 a[248:249], v198 offset:13312\n\t"
    "v_fma_f32 v69, v69, s62, v212\n\t"
    "v_mfma_f32_16x16x32_bf16 a[68:71], a[224:227], v[16:19], a[68:71]\n\t"
    "v_exp_f32 v67, v67\n\t"
    "v_mfma_f32_16x16x32_bf16 v[232:235], v[244:247], v[24:27], v[232:235]\n\t"
    "v_exp_f32 v68, v68\n\t"
    "v_mfma_f32_16x16x32_bf16 a[72:75], a[224:227], v[32:35], a[72:75]\n\t"
    "ds_read_b64_tr_b16 a[250:251], v198 offset:19456\n\t"
    "v_cvt_pk_bf16_f32 v65, v66, v67\n\t"
    "v_mfma_f32_16x16x32_bf16 a[76:79], a[224:227], v[48:51], a[76:79]\n\t"
    "v_exp_f32 v69, v69\n\t"
    "v_mfma_f32_16x16x32_bf16 a[92:95], a[228:231], v[48:51], a[92:95]\n\t"
    "ds_read_b64_tr_b16 a[252:253], v199 offset:13312\n\t"
    "v_cvt_pk_bf16_f32 v66, v68, v69\n\t"
    "v_mfma_f32_16x16x32_bf16 a[88:91], a[228:231], v[32:35], a[88:91]\n\t"
    "v_fma_f32 v70, v70, s62, v212\n\t"
    "v_fma_f32 v71, v71, s62, v212\n\t"
    "v_mfma_f32_16x16x32_bf16 v[236:239], v[244:247], v[40:43], v[236:239]\n\t"
    "v_exp_f32 v70, v70\n\t"
    "v_mfma_f32_16x16x32_bf16 a[84:87], a[228:231], v[16:19], a[84:87]\n\t"
    "s_add_u32 m0, s56, 25472\n\t"
    "ds_read_b64_tr_b16 a[254:255], v199 offset:19456\n\t"
    "v_fma_f32 v80, v80, s62, v213\n\t"
    "v_mfma_f32_16x16x32_bf16 a[80:83], a[228:231], v[0:3], a[80:83]\n\t"
    "s_waitcnt lgkmcnt(8)\n\t"
    "buffer_load_dwordx4 v201, s[36:39], s52 offen offset:3200 lds\n\t"
    "v_fma_f32 v81, v81, s62, v213\n\t"
    "v_mfma_f32_16x16x32_bf16 a[96:99], a[232:235], v[0:3], a[96:99]\n\t"
    "ds_read_b64_tr_b16 a[224:225], v196 offset:2048\n\t"
    "v_fma_f32 v82, v82, s62, v213\n\t"
    "v_mfma_f32_16x16x32_bf16 a[100:103], a[232:235], v[16:19], a[100:103]\n\t"
    "v_exp_f32 v71, v71\n\t"
    "v_mfma_f32_16x16x32_bf16 v[240:243], v[244:247], v[56:59], v[240:243]\n\t"
    "v_exp_f32 v80, v80\n\t"
    "v_mfma_f32_16x16x32_bf16 a[104:107], a[232:235], v[32:35], a[104:107]\n\t"
    "ds_read_b64_tr_b16 a[226:227], v196 offset:8192\n\t"
    "v_cvt_pk_bf16_f32 v67, v70, v71\n\t"
    "v_mfma_f32_16x16x32_bf16 a[108:111], a[232:235], v[48:51], a[108:111]\n\t"
    "v_exp_f32 v81, v81\n\t"
    "v_mfma_f32_16x16x32_bf16 a[124:127], a[236:239], v[48:51], a[124:127]\n\t"
    "s_add_u32 m0, s58, 10112\n\t"
    "ds_read_b64_tr_b16 a[228:229], v197 offset:2048\n\t"
    "v_cvt_pk_bf16_f32 v80, v80, v81\n\t"
    "v_mfma_f32_16x16x32_bf16 a[120:123], a[236:239], v[32:35], a[120:123]\n\t"
    "s_add_u32 s75, s54, s97\n\t"
    "buffer_load_dwordx4 v200, s[44:47], s75 offen offset:128 nt lds\n\t"
    "v_fma_f32 v83, v83, s62, v213\n\t"
    "v_mfma_f32_16x16x32_bf16 a[116:119], a[236:239], v[16:19], a[116:119]\n\t"
    "ds_read_b64_tr_b16 a[230:231], v197 offset:8192\n\t"
    "v_fma_f32 v84, v84, s62, v213\n\t"
    "v_mfma_f32_16x16x32_bf16 a[112:115], a[236:239], v[0:3], a[112:115]\n\t"
    "s_waitcnt lgkmcnt(8)\n\t"
    "v_exp_f32 v82, v82\n\t"
    "v_mfma_f32_16x16x32_bf16 a[64:67], a[240:243], v[8:11], a[64:67]\n\t"
    "ds_read_b64_tr_b16 a[232:233], v198 offset:2048\n\t"
    "v_fma_f32 v85, v85, s62, v213\n\t"
    "v_mfma_f32_16x16x32_bf16 a[68:71], a[240:243], v[24:27], a[68:71]\n\t"
    "v_exp_f32 v83, v83\n\t"
    "v_mfma_f32_16x16x32_bf16 a[72:75], a[240:243], v[40:43], a[72:75]\n\t"
    "s_add_u32 m0, s56, 26368\n\t"
    "ds_read_b64_tr_b16 a[234:235], v198 offset:8192\n\t"
    "v_cvt_pk_bf16_f32 v81, v82, v83\n\t"
    "v_mfma_f32_16x16x32_bf16 a[76:79], a[240:243], v[56:59], a[76:79]\n\t"
    "buffer_load_dwordx4 v200, s[36:39], s52 offen offset:256 lds\n\t"
    "v_fma_f32 v86, v86, s62, v213\n\t"
    "v_mfma_f32_16x16x32_bf16 a[92:95], a[244:247], v[56:59], a[92:95]\n\t"
    "ds_read_b64_tr_b16 a[236:237], v199 offset:2048\n\t"
    "v_fma_f32 v87, v87, s62, v213\n\t"
    "v_mfma_f32_16x16x32_bf16 a[88:91], a[244:247], v[40:43], a[88:91]\n\t"
    "v_exp_f32 v84, v84\n\t"
    "v_mfma_f32_16x16x32_bf16 a[84:87], a[244:247], v[24:27], a[84:87]\n\t"
    "ds_read_b64_tr_b16 a[238:239], v199 offset:8192\n\t"
    "v_fma_f32 v96, v96, s62, v214\n\t"
    "v_mfma_f32_16x16x32_bf16 a[80:83], a[244:247], v[8:11], a[80:83]\n\t"
    "s_waitcnt lgkmcnt(8)\n\t"
    "v_exp_f32 v85, v85\n\t"
    "v_mfma_f32_16x16x32_bf16 a[96:99], a[248:251], v[8:11], a[96:99]\n\t"
    "ds_read_b64_tr_b16 a[240:241], v196 offset:14336\n\t"
    "v_cvt_pk_bf16_f32 v82, v84, v85\n\t"
    "v_mfma_f32_16x16x32_bf16 a[100:103], a[248:251], v[24:27], a[100:103]\n\t"
    "v_exp_f32 v86, v86\n\t"
    "v_mfma_f32_16x16x32_bf16 a[104:107], a[248:251], v[40:43], a[104:107]\n\t"
    "ds_read_b64_tr_b16 a[242:243], v196 offset:20480\n\t"
    "v_fma_f32 v97, v97, s62, v214\n\t"
    "v_mfma_f32_16x16x32_bf16 a[108:111], a[248:251], v[56:59], a[108:111]\n\t"
    "v_exp_f32 v87, v87\n\t"
    "v_mfma_f32_16x16x32_bf16 a[124:127], a[252:255], v[56:59], a[124:127]\n\t"
    "ds_read_b64_tr_b16 a[244:245], v197 offset:14336\n\t"
    "v_cvt_pk_bf16_f32 v83, v86, v87\n\t"
    "v_mfma_f32_16x16x32_bf16 a[120:123], a[252:255], v[40:43], a[120:123]\n\t"
    "v_exp_f32 v96, v96\n\t"
    "v_mfma_f32_16x16x32_bf16 a[116:119], a[252:255], v[24:27], a[116:119]\n\t"
    "s_add_u32 m0, s56, 26368\n\t"
    "ds_read_b64_tr_b16 a[246:247], v197 offset:20480\n\t"
    "v_fma_f32 v98, v98, s62, v214\n\t"
    "v_mfma_f32_16x16x32_bf16 a[112:115], a[252:255], v[8:11], a[112:115]\n\t"
    "s_waitcnt lgkmcnt(8)\n\t"
    "buffer_load_dwordx4 v201, s[36:39], s52 offen offset:3328 lds\n\t"
    "v_fma_f32 v99, v99, s62, v214\n\t"
    "v_mfma_f32_16x16x32_bf16 v[128:131], a[224:227], v[0:3], v[128:131]\n\t"
    "ds_read_b64_tr_b16 a[248:249], v198 offset:14336\n\t"
    "v_fma_f32 v100, v100, s62, v214\n\t"
    "v_mfma_f32_16x16x32_bf16 v[132:135], a[224:227], v[16:19], v[132:135]\n\t"
    "v_exp_f32 v97, v97\n\t"
    "v_mfma_f32_16x16x32_bf16 v[136:139], a[224:227], v[32:35], v[136:139]\n\t"
    "ds_read_b64_tr_b16 a[250:251], v198 offset:20480\n\t"
    "v_cvt_pk_bf16_f32 v96, v96, v97\n\t"
    "v_mfma_f32_16x16x32_bf16 v[140:143], a[224:227], v[48:51], v[140:143]\n\t"
    "v_exp_f32 v98, v98\n\t"
    "v_mfma_f32_16x16x32_bf16 v[156:159], a[228:231], v[48:51], v[156:159]\n\t"
    "ds_read_b64_tr_b16 a[252:253], v199 offset:14336\n\t"
    "v_fma_f32 v101, v101, s62, v214\n\t"
    "v_mfma_f32_16x16x32_bf16 v[152:155], a[228:231], v[32:35], v[152:155]\n\t"
    "v_exp_f32 v99, v99\n\t"
    "v_mfma_f32_16x16x32_bf16 v[148:151], a[228:231], v[16:19], v[148:151]\n\t"
    "ds_read_b64_tr_b16 a[254:255], v199 offset:20480\n\t"
    "v_cvt_pk_bf16_f32 v97, v98, v99\n\t"
    "v_mfma_f32_16x16x32_bf16 v[144:147], a[228:231], v[0:3], v[144:147]\n\t"
    "s_waitcnt lgkmcnt(8)\n\t"
    "v_exp_f32 v100, v100\n\t"
    "v_mfma_f32_16x16x32_bf16 v[160:163], a[232:235], v[0:3], v[160:163]\n\t"
    "v_exp_f32 v101, v101\n\t"
    "v_mfma_f32_16x16x32_bf16 v[164:167], a[232:235], v[16:19], v[164:167]\n\t"
    "v_cvt_pk_bf16_f32 v98, v100, v101\n\t"
    "v_fma_f32 v102, v102, s62, v214\n\t"
    "v_mfma_f32_16x16x32_bf16 v[168:171], a[232:235], v[32:35], v[168:171]\n\t"
    "s_add_u32 s52, s52, 24993792\n\t"
    "v_exp_f32 v102, v102\n\t"
    "v_mfma_f32_16x16x32_bf16 v[172:175], a[232:235], v[48:51], v[172:175]\n\t"
    "v_fma_f32 v103, v103, s62, v214\n\t"
    "v_fma_f32 v112, v112, s62, v215\n\t"
    "v_mfma_f32_16x16x32_bf16 v[188:191], a[236:239], v[48:51], v[188:191]\n\t"
    "v_exp_f32 v103, v103\n\t"
    "v_mfma_f32_16x16x32_bf16 v[184:187], a[236:239], v[32:35], v[184:187]\n\t"
    "v_exp_f32 v112, v112\n\t"
    "v_mfma_f32_16x16x32_bf16 v[180:183], a[236:239], v[16:19], v[180:183]\n\t"
    "s_waitcnt lgkmcnt(0) vmcnt(16)\n\t"
    "v_cvt_pk_bf16_f32 v99, v102, v103\n\t"
    "v_fma_f32 v113, v113, s62, v215\n\t"
    "v_mfma_f32_16x16x32_bf16 v[176:179], a[236:239], v[0:3], v[176:179]\n\t"
    "v_exp_f32 v113, v113\n\t"
    "v_mfma_f32_16x16x32_bf16 v[128:131], a[240:243], v[8:11], v[128:131]\n\t"
    "s_barrier\n\t"
    "ds_read_b128 a[224:227], v192 offset:0\n\t"
    "v_cvt_pk_bf16_f32 v112, v112, v113\n\t"
    "v_mfma_f32_16x16x32_bf16 v[132:135], a[240:243], v[24:27], v[132:135]\n\t"
    "s_add_u32 m0, s58, 8064\n\t"
    "ds_read_b128 a[228:231], v192 offset:6144\n\t"
    "v_fma_f32 v114, v114, s62, v215\n\t"
    "v_mfma_f32_16x16x32_bf16 v[136:139], a[240:243], v[40:43], v[136:139]\n\t"
    "s_add_u32 s75, s54, s97\n\t"
    "ds_read_b128 a[232:235], v192 offset:12288\n\t"
    "v_fma_f32 v115, v115, s62, v215\n\t"
    "v_mfma_f32_16x16x32_bf16 v[140:143], a[240:243], v[56:59], v[140:143]\n\t"
    "buffer_load_dwordx4 v201, s[44:47], s75 offen offset:3200 nt lds\n\t"
    "v_fma_f32 v116, v116, s62, v215\n\t"
    "v_mfma_f32_16x16x32_bf16 v[156:159], a[244:247], v[56:59], v[156:159]\n\t"
    "v_exp_f32 v114, v114\n\t"
    "v_mfma_f32_16x16x32_bf16 v[152:155], a[244:247], v[40:43], v[152:155]\n\t"
    "v_exp_f32 v115, v115\n\t"
    "v_mfma_f32_16x16x32_bf16 v[148:151], a[244:247], v[24:27], v[148:151]\n\t"
    "ds_read_b128 a[236:239], v192 offset:18432\n\t"
    "v_cvt_pk_bf16_f32 v113, v114, v115\n\t"
    "v_mfma_f32_16x16x32_bf16 v[144:147], a[244:247], v[8:11], v[144:147]\n\t"
    "v_exp_f32 v116, v116\n\t"
    "v_mfma_f32_16x16x32_bf16 v[160:163], a[248:251], v[8:11], v[160:163]\n\t"
    "v_fma_f32 v117, v117, s62, v215\n\t"
    "v_fma_f32 v118, v118, s62, v215\n\t"
    "v_mfma_f32_16x16x32_bf16 v[164:167], a[248:251], v[24:27], v[164:167]\n\t"
    "v_exp_f32 v117, v117\n\t"
    "v_mfma_f32_16x16x32_bf16 v[168:171], a[248:251], v[40:43], v[168:171]\n\t"
    "ds_read_b128 a[240:243], v193 offset:0\n\t"
    "v_cvt_pk_bf16_f32 v114, v116, v117\n\t"
    "v_mfma_f32_16x16x32_bf16 v[172:175], a[248:251], v[56:59], v[172:175]\n\t"
    "v_exp_f32 v118, v118\n\t"
    "v_mfma_f32_16x16x32_bf16 v[188:191], a[252:255], v[56:59], v[188:191]\n\t"
    "v_fma_f32 v119, v119, s62, v215\n\t"
    "v_fma_f32 v72, v72, s62, v212\n\t"
    "v_mfma_f32_16x16x32_bf16 v[184:187], a[252:255], v[40:43], v[184:187]\n\t"
    "v_exp_f32 v119, v119\n\t"
    "v_mfma_f32_16x16x32_bf16 v[180:183], a[252:255], v[24:27], v[180:183]\n\t"
    "ds_read_b128 a[244:247], v193 offset:6144\n\t"
    "v_cvt_pk_bf16_f32 v115, v118, v119\n\t"
    "v_mfma_f32_16x16x32_bf16 v[176:179], a[252:255], v[8:11], v[176:179]\n\t"
    "s_waitcnt lgkmcnt(4)\n\t"
    "v_exp_f32 v72, v72\n\t"
    "v_mfma_f32_16x16x32_bf16 v[0:3], a[224:227], a[128:131], v[224:227]\n\t"
    "s_cmp_lg_u32 s72, 0\n\t"
    "s_cbranch_scc1 SB5\n\t"
    "NSB5:\n\t"
    "v_fma_f32 v73, v73, s62, v212\n\t"
    "v_mfma_f32_16x16x32_bf16 v[16:19], a[224:227], a[132:135], v[224:227]\n\t"
    "v_exp_f32 v73, v73\n\t"
    "v_mfma_f32_16x16x32_bf16 v[32:35], a[224:227], a[136:139], v[224:227]\n\t"
    "s_add_u32 m0, s57, 0\n\t"
    "ds_read_b128 a[248:251], v193 offset:12288\n\t"
    "v_cvt_pk_bf16_f32 v72, v72, v73\n\t"
    "v_mfma_f32_16x16x32_bf16 v[48:51], a[224:227], a[140:143], v[224:227]\n\t"
    "buffer_load_dwordx4 v202, s[40:43], s53 offen offset:0 lds\n\t"
    "v_fma_f32 v74, v74, s62, v212\n\t"
    "v_mfma_f32_16x16x32_bf16 v[52:55], a[228:231], a[140:143], v[224:227]\n\t"
    "v_exp_f32 v74, v74\n\t"
    "v_mfma_f32_16x16x32_bf16 v[36:39], a[228:231], a[136:139], v[224:227]\n\t"
    "v_fma_f32 v75, v75, s62, v212\n\t"
    "v_fma_f32 v76, v76, s62, v212\n\t"
    "v_mfma_f32_16x16x32_bf16 v[20:23], a[228:231], a[132:135], v[224:227]\n\t"
    "ds_read_b128 a[252:255], v193 offset:18432\n\t"
    "v_fma_f32 v77, v77, s62, v212\n\t"
    "v_mfma_f32_16x16x32_bf16 v[4:7], a[228:231], a[128:131], v[224:227]\n\t"
    "s_waitcnt lgkmcnt(4)\n\t"
    "v_exp_f32 v75, v75\n\t"
    "v_mfma_f32_16x16x32_bf16 v[8:11], a[232:235], a[128:131], v[224:227]\n\t"
    "v_exp_f32 v76, v76\n\t"
    "v_mfma_f32_16x16x32_bf16 v[24:27], a[232:235], a[132:135], v[224:227]\n\t"
    "v_exp_f32 v77, v77\n\t"
    "v_mfma_f32_16x16x32_bf16 v[40:43], a[232:235], a[136:139], v[224:227]\n\t"
    "ds_read_b128 a[224:227], v192 offset:1024\n\t"
    "v_cvt_pk_bf16_f32 v73, v74, v75\n\t"
    "v_mfma_f32_16x16x32_bf16 v[56:59], a[232:235], a[140:143], v[224:227]\n\t"
    "v_cvt_pk_bf16_f32 v74, v76, v77\n\t"
    "v_fma_f32 v78, v78, s62, v212\n\t"
    "v_mfma_f32_16x16x32_bf16 v[60:63], a[236:239], a[140:143], v[224:227]\n\t"
    "v_exp_f32 v78, v78\n\t"
    "v_mfma_f32_16x16x32_bf16 v[44:47], a[236:239], a[136:139], v[224:227]\n\t"
    "v_fma_f32 v79, v79, s62, v212\n\t"
    "v_fma_f32 v88, v88, s62, v213\n\t"
    "v_mfma_f32_16x16x32_bf16 v[28:31], a[236:239], a[132:135], v[224:227]\n\t"
    "s_add_u32 m0, s57, 0\n\t"
    "ds_read_b128 a[228:231], v192 offset:7168\n\t"
    "v_fma_f32 v89, v89, s62, v213\n\t"
    "v_mfma_f32_16x16x32_bf16 v[12:15], a[236:239], a[128:131], v[224:227]\n\t"
    "s_waitcnt lgkmcnt(4)\n\t"
    "buffer_load_dwordx4 v202, s[40:43], s53 offen offset:3072 lds\n\t"
    "v_fma_f32 v90, v90, s62, v213\n\t"
    "v_mfma_f32_16x16x32_bf16 v[0:3], a[240:243], a[144:147], v[0:3]\n\t"
    "v_exp_f32 v79, v79\n\t"
    "v_mfma_f32_16x16x32_bf16 v[16:19], a[240:243], a[148:151], v[16:19]\n\t"
    "v_exp_f32 v88, v88\n\t"
    "v_mfma_f32_16x16x32_bf16 v[32:35], a[240:243], a[152:155], v[32:35]\n\t"
    "ds_read_b128 a[232:235], v192 offset:13312\n\t"
    "v_cvt_pk_bf16_f32 v75, v78, v79\n\t"
    "v_mfma_f32_16x16x32_bf16 v[48:51], a[240:243], a[156:159], v[48:51]\n\t"
    "v_exp_f32 v89, v89\n\t"
    "v_mfma_f32_16x16x32_bf16 v[52:55], a[244:247], a[156:159], v[52:55]\n\t"
    "s_add_u32 m0, s58, 12160\n\t"
    "v_exp_f32 v90, v90\n\t"
    "v_mfma_f32_16x16x32_bf16 v[36:39], a[244:247], a[152:155], v[36:39]\n\t"
    "s_add_u32 s75, s54, s98\n\t"
    "buffer_load_dwordx4 v200, s[44:47], s75 offen offset:128 nt lds\n\t"
    "v_cvt_pk_bf16_f32 v88, v88, v89\n\t"
    "v_mfma_f32_16x16x32_bf16 v[20:23], a[244:247], a[148:151], v[20:23]\n\t"
    "ds_read_b128 a[236:239], v192 offset:19456\n\t"
    "v_fma_f32 v91, v91, s62, v213\n\t"
    "v_mfma_f32_16x16x32_bf16 v[4:7], a[244:247], a[144:147], v[4:7]\n\t"
    "s_waitcnt lgkmcnt(4)\n\t"
    "v_exp_f32 v91, v91\n\t"
    "v_mfma_f32_16x16x32_bf16 v[8:11], a[248:251], a[144:147], v[8:11]\n\t"
    "v_cvt_pk_bf16_f32 v89, v90, v91\n\t"
    "v_fma_f32 v92, v92, s62, v213\n\t"
    "v_mfma_f32_16x16x32_bf16 v[24:27], a[248:251], a[148:151], v[24:27]\n\t"
    "v_exp_f32 v92, v92\n\t"
    "v_mfma_f32_16x16x32_bf16 v[40:43], a[248:251], a[152:155], v[40:43]\n\t"
    "s_add_u32 m0, s57, 896\n\t"
    "ds_read_b128 a[240:243], v193 offset:1024\n\t"
    "v_fma_f32 v93, v93, s62, v213\n\t"
    "v_mfma_f32_16x16x32_bf16 v[56:59], a[248:251], a[156:159], v[56:59]\n\t"
    "buffer_load_dwordx4 v202, s[40:43], s53 offen offset:128 lds\n\t"
    "v_fma_f32 v94, v94, s62, v213\n\t"
    "v_mfma_f32_16x16x32_bf16 v[60:63], a[252:255], a[156:159], v[60:63]\n\t"
    "v_exp_f32 v93, v93\n\t"
    "v_mfma_f32_16x16x32_bf16 v[44:47], a[252:255], a[152:155], v[44:47]\n\t"
    "v_exp_f32 v94, v94\n\t"
    "v_mfma_f32_16x16x32_bf16 v[28:31], a[252:255], a[148:151], v[28:31]\n\t"
    "ds_read_b128 a[244:247], v193 offset:7168\n\t"
    "v_cvt_pk_bf16_f32 v90, v92, v93\n\t"
    "v_mfma_f32_16x16x32_bf16 v[12:15], a[252:255], a[144:147], v[12:15]\n\t"
    "s_waitcnt lgkmcnt(4)\n\t"
    "v_fma_f32 v95, v95, s62, v213\n\t"
    "v_fma_f32 v104, v104, s62, v214\n\t"
    "v_mfma_f32_16x16x32_bf16 v[0:3], a[224:227], a[160:163], v[0:3]\n\t"
    "v_exp_f32 v95, v95\n\t"
    "v_mfma_f32_16x16x32_bf16 v[16:19], a[224:227], a[164:167], v[16:19]\n\t"
    "v_exp_f32 v104, v104\n\t"
    "v_mfma_f32_16x16x32_bf16 v[32:35], a[224:227], a[168:171], v[32:35]\n\t"
    "ds_read_b128 a[248:251], v193 offset:13312\n\t"
    "v_cvt_pk_bf16_f32 v91, v94, v95\n\t"
    "v_mfma_f32_16x16x32_bf16 v[48:51], a[224:227], a[172:175], v[48:51]\n\t"
    "v_fma_f32 v105, v105, s62, v214\n\t"
    "v_fma_f32 v106, v106, s62, v214\n\t"
    "v_mfma_f32_16x16x32_bf16 v[52:55], a[228:231], a[172:175], v[52:55]\n\t"
    "v_exp_f32 v105, v105\n\t"
    "v_mfma_f32_16x16x32_bf16 v[36:39], a[228:231], a[168:171], v[36:39]\n\t"
    "v_exp_f32 v106, v106\n\t"
    "v_mfma_f32_16x16x32_bf16 v[20:23], a[228:231], a[164:167], v[20:23]\n\t"
    "s_add_u32 m0, s57, 896\n\t"
    "ds_read_b128 a[252:255], v193 offset:19456\n\t"
    "v_cvt_pk_bf16_f32 v104, v104, v105\n\t"
    "v_mfma_f32_16x16x32_bf16 v[4:7], a[228:231], a[160:163], v[4:7]\n\t"
    "s_waitcnt lgkmcnt(4)\n\t"
    "buffer_load_dwordx4 v202, s[40:43], s53 offen offset:3200 lds\n\t"
    "v_fma_f32 v107, v107, s62, v214\n\t"
    "v_mfma_f32_16x16x32_bf16 v[8:11], a[232:235], a[160:163], v[8:11]\n\t"
    "v_exp_f32 v107, v107\n\t"
    "v_mfma_f32_16x16x32_bf16 v[24:27], a[232:235], a[164:167], v[24:27]\n\t"
    "v_cvt_pk_bf16_f32 v105, v106, v107\n\t"
    "v_fma_f32 v108, v108, s62, v214\n\t"
    "v_mfma_f32_16x16x32_bf16 v[40:43], a[232:235], a[168:171], v[40:43]\n\t"
    "ds_read_b128 a[224:227], v192 offset:2048\n\t"
    "v_fma_f32 v109, v109, s62, v214\n\t"
    "v_mfma_f32_16x16x32_bf16 v[56:59], a[232:235], a[172:175], v[56:59]\n\t"
    "v_exp_f32 v108, v108\n\t"
    "v_mfma_f32_16x16x32_bf16 v[60:63], a[236:239], a[172:175], v[60:63]\n\t"
    "s_add_u32 m0, s58, 10112\n\t"
    "v_exp_f32 v109, v109\n\t"
    "v_mfma_f32_16x16x32_bf16 v[44:47], a[236:239], a[168:171], v[44:47]\n\t"
    "s_add_u32 s75, s54, s98\n\t"
    "buffer_load_dwordx4 v201, s[44:47], s75 offen offset:3200 nt lds\n\t"
    "v_cvt_pk_bf16_f32 v106, v108, v109\n\t"
    "v_mfma_f32_16x16x32_bf16 v[28:31], a[236:239], a[164:167], v[28:31]\n\t"
    "ds_read_b128 a[228:231], v192 offset:8192\n\t"
    "v_fma_f32 v110, v110, s62, v214\n\t"
    "v_mfma_f32_16x16x32_bf16 v[12:15], a[236:239], a[160:163], v[12:15]\n\t"
    "s_waitcnt lgkmcnt(4)\n\t"
    "v_exp_f32 v110, v110\n\t"
    "v_mfma_f32_16x16x32_bf16 v[0:3], a[240:243], a[176:179], v[0:3]\n\t"
    "v_fma_f32 v111, v111, s62, v214\n\t"
    "v_fma_f32 v120, v120, s62, v215\n\t"
    "v_mfma_f32_16x16x32_bf16 v[16:19], a[240:243], a[180:183], v[16:19]\n\t"
    "v_exp_f32 v111, v111\n\t"
    "v_mfma_f32_16x16x32_bf16 v[32:35], a[240:243], a[184:187], v[32:35]\n\t"
    "s_add_u32 m0, s57, 1792\n\t"
    "ds_read_b128 a[232:235], v192 offset:14336\n\t"
    "v_cvt_pk_bf16_f32 v107, v110, v111\n\t"
    "v_mfma_f32_16x16x32_bf16 v[48:51], a[240:243], a[188:191], v[48:51]\n\t"
    "buffer_load_dwordx4 v202, s[40:43], s53 offen offset:256 lds\n\t"
    "v_fma_f32 v121, v121, s62, v215\n\t"
    "v_mfma_f32_16x16x32_bf16 v[52:55], a[244:247], a[188:191], v[52:55]\n\t"
    "v_exp_f32 v120, v120\n\t"
    "v_mfma_f32_16x16x32_bf16 v[36:39], a[244:247], a[184:187], v[36:39]\n\t"
    "v_exp_f32 v121, v121\n\t"
    "v_mfma_f32_16x16x32_bf16 v[20:23], a[244:247], a[180:183], v[20:23]\n\t"
    "ds_read_b128 a[236:239], v192 offset:20480\n\t"
    "v_cvt_pk_bf16_f32 v120, v120, v121\n\t"
    "v_mfma_f32_16x16x32_bf16 v[4:7], a[244:247], a[176:179], v[4:7]\n\t"
    "s_waitcnt lgkmcnt(4)\n\t"
    "v_fma_f32 v122, v122, s62, v215\n\t"
    "v_fma_f32 v123, v123, s62, v215\n\t"
    "v_mfma_f32_16x16x32_bf16 v[8:11], a[248:251], a[176:179], v[8:11]\n\t"
    "s_add_u32 m0, s58, 14208\n\t"
    "v_exp_f32 v122, v122\n\t"
    "v_mfma_f32_16x16x32_bf16 v[24:27], a[248:251], a[180:183], v[24:27]\n\t"
    "s_add_u32 s75, s54, s99\n\t"
    "buffer_load_dwordx4 v200, s[44:47], s75 offen offset:128 nt lds\n\t"
    "v_fma_f32 v124, v124, s62, v215\n\t"
    "v_mfma_f32_16x16x32_bf16 v[40:43], a[248:251], a[184:187], v[40:43]\n\t"
    "ds_read_b128 a[240:243], v193 offset:2048\n\t"
    "v_fma_f32 v125, v125, s62, v215\n\t"
    "v_mfma_f32_16x16x32_bf16 v[56:59], a[248:251], a[188:191], v[56:59]\n\t"
    "v_exp_f32 v123, v123\n\t"
    "v_mfma_f32_16x16x32_bf16 v[60:63], a[252:255], a[188:191], v[60:63]\n\t"
    "v_exp_f32 v124, v124\n\t"
    "v_mfma_f32_16x16x32_bf16 v[44:47], a[252:255], a[184:187], v[44:47]\n\t"
    "v_exp_f32 v125, v125\n\t"
    "v_mfma_f32_16x16x32_bf16 v[28:31], a[252:255], a[180:183], v[28:31]\n\t"
    "s_add_u32 m0, s57, 1792\n\t"
    "ds_read_b128 a[244:247], v193 offset:8192\n\t"
    "v_cvt_pk_bf16_f32 v121, v122, v123\n\t"
    "v_mfma_f32_16x16x32_bf16 v[12:15], a[252:255], a[176:179], v[12:15]\n\t"
    "s_waitcnt lgkmcnt(4)\n\t"
    "buffer_load_dwordx4 v202, s[40:43], s53 offen offset:3328 lds\n\t"
    "v_cvt_pk_bf16_f32 v122, v124, v125\n\t"
    "v_mfma_f32_16x16x32_bf16 v[0:3], a[224:227], a[192:195], v[0:3]\n\t"
    "v_fma_f32 v126, v126, s62, v215\n\t"
    "v_fma_f32 v127, v127, s62, v215\n\t"
    "v_mfma_f32_16x16x32_bf16 v[16:19], a[224:227], a[196:199], v[16:19]\n\t"
    "v_exp_f32 v126, v126\n\t"
    "v_mfma_f32_16x16x32_bf16 v[32:35], a[224:227], a[200:203], v[32:35]\n\t"
    "ds_read_b128 a[248:251], v193 offset:14336\n\t"
    "v_mfma_f32_16x16x32_bf16 v[48:51], a[224:227], a[204:207], v[48:51]\n\t"
    "v_exp_f32 v127, v127\n\t"
    "v_mfma_f32_16x16x32_bf16 v[52:55], a[228:231], a[204:207], v[52:55]\n\t"
    "v_cvt_pk_bf16_f32 v123, v126, v127\n\t"
    "v_mfma_f32_16x16x32_bf16 v[36:39], a[228:231], a[200:203], v[36:39]\n\t"
    "v_mfma_f32_16x16x32_bf16 v[20:23], a[228:231], a[196:199], v[20:23]\n\t"
    "ds_read_b128 a[252:255], v193 offset:20480\n\t"
    "v_mfma_f32_16x16x32_bf16 v[4:7], a[228:231], a[192:195], v[4:7]\n\t"
    "s_waitcnt lgkmcnt(4)\n\t"
    "v_mfma_f32_16x16x32_bf16 v[8:11], a[232:235], a[192:195], v[8:11]\n\t"
    "s_cmp_eq_u32 s69, 3\n\t"
    "s_cselect_b32 s38, 0, s38\n\t"
    "v_mfma_f32_16x16x32_bf16 v[24:27], a[232:235], a[196:199], v[24:27]\n\t"
    "v_mfma_f32_16x16x32_bf16 v[40:43], a[232:235], a[200:203], v[40:43]\n\t"
    "s_add_u32 s53, s53, 24576\n\t"
    "v_mfma_f32_16x16x32_bf16 v[56:59], a[232:235], a[204:207], v[56:59]\n\t"
    "v_mfma_f32_16x16x32_bf16 v[60:63], a[236:239], a[204:207], v[60:63]\n\t"
    "v_mfma_f32_16x16x32_bf16 v[44:47], a[236:239], a[200:203], v[44:47]\n\t"
    "v_mfma_f32_16x16x32_bf16 v[28:31], a[236:239], a[196:199], v[28:31]\n\t"
    "s_waitcnt lgkmcnt(0) vmcnt(18)\n\t"
    "v_mfma_f32_16x16x32_bf16 v[12:15], a[236:239], a[192:195], v[12:15]\n\t"
    "v_mfma_f32_16x16x32_bf16 v[0:3], a[240:243], a[208:211], v[0:3]\n\t"
    "s_barrier\n\t"
    "ds_read_b64_tr_b16 a[224:225], v196 offset:24576\n\t"
    "v_mfma_f32_16x16x32_bf16 v[16:19], a[240:243], a[212:215], v[16:19]\n\t"
    "s_add_u32 m0, s58, 12160\n\t"
    "ds_read_b64_tr_b16 a[226:227], v196 offset:30720\n\t"
    "v_mfma_f32_16x16x32_bf16 v[32:35], a[240:243], a[216:219], v[32:35]\n\t"
    "s_add_u32 s75, s54, s99\n\t"
    "ds_read_b64_tr_b16 a[228:229], v197 offset:24576\n\t"
    "v_mfma_f32_16x16x32_bf16 v[48:51], a[240:243], a[220:223], v[48:51]\n\t"
    "ds_read_b64_tr_b16 a[230:231], v197 offset:30720\n\t"
    "v_mfma_f32_16x16x32_bf16 v[52:55], a[244:247], a[220:223], v[52:55]\n\t"
    "ds_read_b64_tr_b16 a[232:233], v198 offset:24576\n\t"
    "v_mfma_f32_16x16x32_bf16 v[36:39], a[244:247], a[216:219], v[36:39]\n\t"
    "ds_read_b64_tr_b16 a[234:235], v198 offset:30720\n\t"
    "v_mfma_f32_16x16x32_bf16 v[20:23], a[244:247], a[212:215], v[20:23]\n\t"
    "ds_read_b64_tr_b16 a[236:237], v199 offset:24576\n\t"
    "v_mfma_f32_16x16x32_bf16 v[4:7], a[244:247], a[208:211], v[4:7]\n\t"
    "ds_read_b64_tr_b16 a[238:239], v199 offset:30720\n\t"
    "v_mfma_f32_16x16x32_bf16 v[8:11], a[248:251], a[208:211], v[8:11]\n\t"
    "ds_read_b64_tr_b16 a[240:241], v196 offset:36864\n\t"
    "v_mfma_f32_16x16x32_bf16 v[24:27], a[248:251], a[212:215], v[24:27]\n\t"
    "buffer_load_dwordx4 v201, s[44:47], s75 offen offset:3200 nt lds\n\t"
    "v_mfma_f32_16x16x32_bf16 v[40:43], a[248:251], a[216:219], v[40:43]\n\t"
    "ds_read_b64_tr_b16 a[242:243], v196 offset:43008\n\t"
    "v_mfma_f32_16x16x32_bf16 v[56:59], a[248:251], a[220:223], v[56:59]\n\t"
    "v_mfma_f32_16x16x32_bf16 v[60:63], a[252:255], a[220:223], v[60:63]\n\t"
    "ds_read_b64_tr_b16 a[244:245], v197 offset:36864\n\t"
    "v_mfma_f32_16x16x32_bf16 v[44:47], a[252:255], a[216:219], v[44:47]\n\t"
    "v_mfma_f32_16x16x32_bf16 v[28:31], a[252:255], a[212:215], v[28:31]\n\t"
    "ds_read_b64_tr_b16 a[246:247], v197 offset:43008\n\t"
    "v_mfma_f32_16x16x32_bf16 v[12:15], a[252:255], a[208:211], v[12:15]\n\t"
    "s_waitcnt lgkmcnt(8)\n\t"
    "v_mfma_f32_16x16x32_bf16 a[0:3], a[224:227], v[64:67], a[0:3]\n\t"
    "ds_read_b64_tr_b16 a[248:249], v198 offset:36864\n\t"
    "v_mfma_f32_16x16x32_bf16 a[4:7], a[224:227], v[80:83], a[4:7]\n\t"
    "v_mfma_f32_16x16x32_bf16 a[8:11], a[224:227], v[96:99], a[8:11]\n\t"
    "s_add_u32 m0, s56, 0\n\t"
    "ds_read_b64_tr_b16 a[250:251], v198 offset:43008\n\t"
    "v_mfma_f32_16x16x32_bf16 a[12:15], a[224:227], v[112:115], a[12:15]\n\t"
    "buffer_load_dwordx4 v200, s[36:39], s52 offen offset:0 lds\n\t"
    "v_mfma_f32_16x16x32_bf16 a[28:31], a[228:231], v[112:115], a[28:31]\n\t"
    "ds_read_b64_tr_b16 a[252:253], v199 offset:36864\n\t"
    "v_max3_f32 v220, v0, v1, v2\n\t"
    "v_mfma_f32_16x16x32_bf16 a[24:27], a[228:231], v[96:99], a[24:27]\n\t"
    "v_max3_f32 v221, v16, v17, v18\n\t"
    "v_max3_f32 v222, v32, v33, v34\n\t"
    "v_mfma_f32_16x16x32_bf16 a[20:23], a[228:231], v[80:83], a[20:23]\n\t"
    "ds_read_b64_tr_b16 a[254:255], v199 offset:43008\n\t"
    "v_max3_f32 v223, v48, v49, v50\n\t"
    "v_mfma_f32_16x16x32_bf16 a[16:19], a[228:231], v[64:67], a[16:19]\n\t"
    "s_waitcnt lgkmcnt(8)\n\t"
    "v_max3_f32 v220, v220, v3, v4\n\t"
    "v_max3_f32 v221, v221, v19, v20\n\t"
    "v_mfma_f32_16x16x32_bf16 a[32:35], a[232:235], v[64:67], a[32:35]\n\t"
    "ds_read_b64_tr_b16 a[224:225], v196 offset:25600\n\t"
    "v_max3_f32 v222, v222, v35, v36\n\t"
    "v_mfma_f32_16x16x32_bf16 a[36:39], a[232:235], v[80:83], a[36:39]\n\t"
    "v_max3_f32 v223, v223, v51, v52\n\t"
    "v_max3_f32 v220, v220, v5, v6\n\t"
    "v_mfma_f32_16x16x32_bf16 v[228:231], v[244:247], v[64:67], v[228:231]\n\t"
    "v_max3_f32 v221, v221, v21, v22\n\t"
    "v_max3_f32 v222, v222, v37, v38\n\t"
    "v_mfma_f32_16x16x32_bf16 a[40:43], a[232:235], v[96:99], a[40:43]\n\t"
    "ds_read_b64_tr_b16 a[226:227], v196 offset:31744\n\t"
    "v_max3_f32 v223, v223, v53, v54\n\t"
    "v_mfma_f32_16x16x32_bf16 a[44:47], a[232:235], v[112:115], a[44:47]\n\t"
    "v_max3_f32 v220, v220, v7, v8\n\t"
    "v_max3_f32 v221, v221, v23, v24\n\t"
    "v_mfma_f32_16x16x32_bf16 a[60:63], a[236:239], v[112:115], a[60:63]\n\t"
    "ds_read_b64_tr_b16 a[228:229], v197 offset:25600\n\t"
    "v_max3_f32 v222, v222, v39, v40\n\t"
    "v_mfma_f32_16x16x32_bf16 a[56:59], a[236:239], v[96:99], a[56:59]\n\t"
    "v_max3_f32 v223, v223, v55, v56\n\t"
    "v_max3_f32 v220, v220, v9, v10\n\t"
    "v_mfma_f32_16x16x32_bf16 v[232:235], v[244:247], v[80:83], v[232:235]\n\t"
    "v_max3_f32 v221, v221, v25, v26\n\t"
    "v_max3_f32 v222, v222, v41, v42\n\t"
    "v_mfma_f32_16x16x32_bf16 a[52:55], a[236:239], v[80:83], a[52:55]\n\t"
    "s_add_u32 m0, s56, 0\n\t"
    "ds_read_b64_tr_b16 a[230:231], v197 offset:31744\n\t"
    "v_max3_f32 v223, v223, v57, v58\n\t"
    "v_mfma_f32_16x16x32_bf16 a[48:51], a[236:239], v[64:67], a[48:51]\n\t"
    "s_waitcnt lgkmcnt(8)\n\t"
    "buffer_load_dwordx4 v201, s[36:39], s52 offen offset:3072 lds\n\t"
    "v_max3_f32 v220, v220, v11, v12\n\t"
    "v_mfma_f32_16x16x32_bf16 a[0:3], a[240:243], v[72:75], a[0:3]\n\t"
    "ds_read_b64_tr_b16 a[232:233], v198 offset:25600\n\t"
    "v_max3_f32 v221, v221, v27, v28\n\t"
    "v_mfma_f32_16x16x32_bf16 a[4:7], a[240:243], v[88:91], a[4:7]\n\t"
    "v_max3_f32 v222, v222, v43, v44\n\t"
    "v_max3_f32 v223, v223, v59, v60\n\t"
    "v_mfma_f32_16x16x32_bf16 v[236:239], v[244:247], v[96:99], v[236:239]\n\t"
    "v_max3_f32 v220, v220, v13, v14\n\t"
    "v_max3_f32 v221, v221, v29, v30\n\t"
    "v_mfma_f32_16x16x32_bf16 a[8:11], a[240:243], v[104:107], a[8:11]\n\t"
    "ds_read_b64_tr_b16 a[234:235], v198 offset:31744\n\t"
    "v_max3_f32 v222, v222, v45, v46\n\t"
    "v_mfma_f32_16x16x32_bf16 a[12:15], a[240:243], v[120:123], a[12:15]\n\t"
    "v_max3_f32 v223, v223, v61, v62\n\t"
    "v_max_f32 v220, v220, v15\n\t"
    "v_mfma_f32_16x16x32_bf16 a[28:31], a[244:247], v[120:123], a[28:31]\n\t"
    "ds_read_b64_tr_b16 a[236:237], v199 offset:25600\n\t"
    "v_max_f32 v221, v221, v31\n\t"
    "v_mfma_f32_16x16x32_bf16 a[24:27], a[244:247], v[104:107], a[24:27]\n\t"
    "v_max_f32 v222, v222, v47\n\t"
    "v_max_f32 v223, v223, v63\n\t"
    "v_mfma_f32_16x16x32_bf16 v[240:243], v[244:247], v[112:115], v[240:243]\n\t"
    "v_cmp_gt_f32 vcc, v220, v216\n\t"
    "v_cmp_gt_f32 s[88:89], v221, v217\n\t"
    "v_mfma_f32_16x16x32_bf16 a[20:23], a[244:247], v[88:91], a[20:23]\n\t"
    "s_or_b64 vcc, vcc, s[88:89]\n\t"
    "ds_read_b64_tr_b16 a[238:239], v199 offset:31744\n\t"
    "v_cmp_gt_f32 s[90:91], v222, v218\n\t"
    "v_mfma_f32_16x16x32_bf16 a[16:19], a[244:247], v[72:75], a[16:19]\n\t"
    "s_waitcnt lgkmcnt(8)\n\t"
    "v_cmp_gt_f32 s[92:93], v223, v219\n\t"
    "s_or_b64 s[88:89], s[90:91], s[92:93]\n\t"
    "v_mfma_f32_16x16x32_bf16 a[32:35], a[248:251], v[72:75], a[32:35]\n\t"
    "s_or_b64 vcc, vcc, s[88:89]\n\t"
    "s_cbranch_scc1 SA6\n\t"
    "NSA6:\n\t"
    "ds_read_b64_tr_b16 a[240:241], v196 offset:37888\n\t"
    "v_mfma_f32_16x16x32_bf16 a[36:39], a[248:251], v[88:91], a[36:39]\n\t"
    "v_fma_f32 v0, v0, s62, v212\n\t"
    "v_fma_f32 v1, v1, s62, v212\n\t"
    "v_mfma_f32_16x16x32_bf16 a[40:43], a[248:251], v[104:107], a[40:43]\n\t"
    "s_add_u32 m0, s56, 896\n\t"
    "ds_read_b64_tr_b16 a[242:243], v196 offset:44032\n\t"
    "v_fma_f32 v2, v2, s62, v212\n\t"
    "v_mfma_f32_16x16x32_bf16 a[44:47], a[248:251], v[120:123], a[44:47]\n\t"
    "buffer_load_dwordx4 v200, s[36:39], s52 offen offset:128 lds\n\t"
    "v_fma_f32 v3, v3, s62, v212\n\t"
    "v_mfma_f32_16x16x32_bf16 a[60:63], a[252:255], v[120:123], a[60:63]\n\t"
    "ds_read_b64_tr_b16 a[244:245], v197 offset:37888\n\t"
    "v_fma_f32 v4, v4, s62, v212\n\t"
    "v_mfma_f32_16x16x32_bf16 a[56:59], a[252:255], v[104:107], a[56:59]\n\t"
    "v_exp_f32 v0, v0\n\t"
    "v_mfma_f32_16x16x32_bf16 v[228:231], v[244:247], v[72:75], v[228:231]\n\t"
    "v_exp_f32 v1, v1\n\t"
    "v_mfma_f32_16x16x32_bf16 a[52:55], a[252:255], v[88:91], a[52:55]\n\t"
    "ds_read_b64_tr_b16 a[246:247], v197 offset:44032\n\t"
    "v_cvt_pk_bf16_f32 v0, v0, v1\n\t"
    "v_mfma_f32_16x16x32_bf16 a[48:51], a[252:255], v[72:75], a[48:51]\n\t"
    "s_waitcnt lgkmcnt(8)\n\t"
    "v_exp_f32 v2, v2\n\t"
    "v_mfma_f32_16x16x32_bf16 a[64:67], a[224:227], v[64:67], a[64:67]\n\t"
    "ds_read_b64_tr_b16 a[248:249], v198 offset:37888\n\t"
    "v_fma_f32 v5, v5, s62, v212\n\t"
    "v_mfma_f32_16x16x32_bf16 a[68:71], a[224:227], v[80:83], a[68:71]\n\t"
    "v_exp_f32 v3, v3\n\t"
    "v_mfma_f32_16x16x32_bf16 v[232:235], v[244:247], v[88:91], v[232:235]\n\t"
    "v_exp_f32 v4, v4\n\t"
    "v_mfma_f32_16x16x32_bf16 a[72:75], a[224:227], v[96:99], a[72:75]\n\t"
    "ds_read_b64_tr_b16 a[250:251], v198 offset:44032\n\t"
    "v_cvt_pk_bf16_f32 v1, v2, v3\n\t"
    "v_mfma_f32_16x16x32_bf16 a[76:79], a[224:227], v[112:115], a[76:79]\n\t"
    "v_exp_f32 v5, v5\n\t"
    "v_mfma_f32_16x16x32_bf16 a[92:95], a[228:231], v[112:115], a[92:95]\n\t"
    "ds_read_b64_tr_b16 a[252:253], v199 offset:37888\n\t"
    "v_cvt_pk_bf16_f32 v2, v4, v5\n\t"
    "v_mfma_f32_16x16x32_bf16 a[88:91], a[228:231], v[96:99], a[88:91]\n\t"
    "v_fma_f32 v6, v6, s62, v212\n\t"
    "v_fma_f32 v7, v7, s62, v212\n\t"
    "v_mfma_f32_16x16x32_bf16 v[236:239], v[244:247], v[104:107], v[236:239]\n\t"
    "v_exp_f32 v6, v6\n\t"
    "v_mfma_f32_16x16x32_bf16 a[84:87], a[228:231], v[80:83], a[84:87]\n\t"
    "s_add_u32 m0, s56, 896\n\t"
    "ds_read_b64_tr_b16 a[254:255], v199 offset:44032\n\t"
    "v_fma_f32 v16, v16, s62, v213\n\t"
    "v_mfma_f32_16x16x32_bf16 a[80:83], a[228:231], v[64:67], a[80:83]\n\t"
    "s_waitcnt lgkmcnt(8)\n\t"
    "buffer_load_dwordx4 v201, s[36:39], s52 offen offset:3200 lds\n\t"
    "v_fma_f32 v17, v17, s62, v213\n\t"
    "v_mfma_f32_16x16x32_bf16 a[96:99], a[232:235], v[64:67], a[96:99]\n\t"
    "ds_read_b64_tr_b16 a[224:225], v196 offset:26624\n\t"
    "v_fma_f32 v18, v18, s62, v213\n\t"
    "v_mfma_f32_16x16x32_bf16 a[100:103], a[232:235], v[80:83], a[100:103]\n\t"
    "v_exp_f32 v7, v7\n\t"
    "v_mfma_f32_16x16x32_bf16 v[240:243], v[244:247], v[120:123], v[240:243]\n\t"
    "v_exp_f32 v16, v16\n\t"
    "v_mfma_f32_16x16x32_bf16 a[104:107], a[232:235], v[96:99], a[104:107]\n\t"
    "ds_read_b64_tr_b16 a[226:227], v196 offset:32768\n\t"
    "v_cvt_pk_bf16_f32 v3, v6, v7\n\t"
    "v_mfma_f32_16x16x32_bf16 a[108:111], a[232:235], v[112:115], a[108:111]\n\t"
    "v_exp_f32 v17, v17\n\t"
    "v_mfma_f32_16x16x32_bf16 a[124:127], a[236:239], v[112:115], a[124:127]\n\t"
    "ds_read_b64_tr_b16 a[228:229], v197 offset:26624\n\t"
    "v_cvt_pk_bf16_f32 v16, v16, v17\n\t"
    "v_mfma_f32_16x16x32_bf16 a[120:123], a[236:239], v[96:99], a[120:123]\n\t"
    "v_exp_f32 v18, v18\n\t"
    "v_mfma_f32_16x16x32_bf16 a[116:119], a[236:239], v[80:83], a[116:119]\n\t"
    "ds_read_b64_tr_b16 a[230:231], v197 offset:32768\n\t"
    "v_fma_f32 v19, v19, s62, v213\n\t"
    "v_mfma_f32_16x16x32_bf16 a[112:115], a[236:239], v[64:67], a[112:115]\n\t"
    "s_waitcnt lgkmcnt(8)\n\t"
    "v_exp_f32 v19, v19\n\t"
    "v_mfma_f32_16x16x32_bf16 a[64:67], a[240:243], v[72:75], a[64:67]\n\t"
    "ds_read_b64_tr_b16 a[232:233], v198 offset:26624\n\t"
    "v_cvt_pk_bf16_f32 v17, v18, v19\n\t"
    "v_mfma_f32_16x16x32_bf16 a[68:71], a[240:243], v[88:91], a[68:71]\n\t"
    "v_fma_f32 v20, v20, s62, v213\n\t"
    "v_fma_f32 v21, v21, s62, v213\n\t"
    "v_mfma_f32_16x16x32_bf16 a[72:75], a[240:243], v[104:107], a[72:75]\n\t"
    "s_add_u32 m0, s56, 1792\n\t"
    "ds_read_b64_tr_b16 a[234:235], v198 offset:32768\n\t"
    "v_fma_f32 v22, v22, s62, v213\n\t"
    "v_mfma_f32_16x16x32_bf16 a[76:79], a[240:243], v[120:123], a[76:79]\n\t"
    "buffer_load_dwordx4 v200, s[36:39], s52 offen offset:256 lds\n\t"
    "v_fma_f32 v23, v23, s62, v213\n\t"
    "v_mfma_f32_16x16x32_bf16 a[92:95], a[244:247], v[120:123], a[92:95]\n\t"
    "ds_read_b64_tr_b16 a[236:237], v199 offset:26624\n\t"
    "v_fma_f32 v32, v32, s62, v214\n\t"
    "v_mfma_f32_16x16x32_bf16 a[88:91], a[244:247], v[104:107], a[88:91]\n\t"
    "v_exp_f32 v20, v20\n\t"
    "v_mfma_f32_16x16x32_bf16 a[84:87], a[244:247], v[88:91], a[84:87]\n\t"
    "ds_read_b64_tr_b16 a[238:239], v199 offset:32768\n\t"
    "v_fma_f32 v33, v33, s62, v214\n\t"
    "v_mfma_f32_16x16x32_bf16 a[80:83], a[244:247], v[72:75], a[80:83]\n\t"
    "s_waitcnt lgkmcnt(8)\n\t"
    "v_exp_f32 v21, v21\n\t"
    "v_mfma_f32_16x16x32_bf16 a[96:99], a[248:251], v[72:75], a[96:99]\n\t"
    "ds_read_b64_tr_b16 a[240:241], v196 offset:38912\n\t"
    "v_cvt_pk_bf16_f32 v18, v20, v21\n\t"
    "v_mfma_f32_16x16x32_bf16 a[100:103], a[248:251], v[88:91], a[100:103]\n\t"
    "v_exp_f32 v22, v22\n\t"
    "v_mfma_f32_16x16x32_bf16 a[104:107], a[248:251], v[104:107], a[104:107]\n\t"
    "ds_read_b64_tr_b16 a[242:243], v196 offset:45056\n\t"
    "v_fma_f32 v34, v34, s62, v214\n\t"
    "v_mfma_f32_16x16x32_bf16 a[108:111], a[248:251], v[120:123], a[108:111]\n\t"
    "v_exp_f32 v23, v23\n\t"
    "v_mfma_f32_16x16x32_bf16 a[124:127], a[252:255], v[120:123], a[124:127]\n\t"
    "ds_read_b64_tr_b16 a[244:245], v197 offset:38912\n\t"
    "v_cvt_pk_bf16_f32 v19, v22, v23\n\t"
    "v_mfma_f32_16x16x32_bf16 a[120:123], a[252:255], v[104:107], a[120:123]\n\t"
    "v_exp_f32 v32, v32\n\t"
    "v_mfma_f32_16x16x32_bf16 a[116:119], a[252:255], v[88:91], a[116:119]\n\t"
    "s_add_u32 m0, s56, 1792\n\t"
    "ds_read_b64_tr_b16 a[246:247], v197 offset:45056\n\t"
    "v_fma_f32 v35, v35, s62, v214\n\t"
    "v_mfma_f32_16x16x32_bf16 a[112:115], a[252:255], v[72:75], a[112:115]\n\t"
    "s_waitcnt lgkmcnt(8)\n\t"
    "buffer_load_dwordx4 v201, s[36:39], s52 offen offset:3328 lds\n\t"
    "v_fma_f32 v36, v36, s62, v214\n\t"
    "v_mfma_f32_16x16x32_bf16 v[128:131], a[224:227], v[64:67], v[128:131]\n\t"
    "ds_read_b64_tr_b16 a[248:249], v198 offset:38912\n\t"
    "v_fma_f32 v37, v37, s62, v214\n\t"
    "v_mfma_f32_16x16x32_bf16 v[132:135], a[224:227], v[80:83], v[132:135]\n\t"
    "v_exp_f32 v33, v33\n\t"
    "v_mfma_f32_16x16x32_bf16 v[136:139], a[224:227], v[96:99], v[136:139]\n\t"
    "ds_read_b64_tr_b16 a[250:251], v198 offset:45056\n\t"
    "v_cvt_pk_bf16_f32 v32, v32, v33\n\t"
    "v_mfma_f32_16x16x32_bf16 v[140:143], a[224:227], v[112:115], v[140:143]\n\t"
    "v_exp_f32 v34, v34\n\t"
    "v_mfma_f32_16x16x32_bf16 v[156:159], a[228:231], v[112:115], v[156:159]\n\t"
    "ds_read_b64_tr_b16 a[252:253], v199 offset:38912\n\t"
    "v_fma_f32 v38, v38, s62, v214\n\t"
    "v_mfma_f32_16x16x32_bf16 v[152:155], a[228:231], v[96:99], v[152:155]\n\t"
    "v_exp_f32 v35, v35\n\t"
    "v_mfma_f32_16x16x32_bf16 v[148:151], a[228:231], v[80:83], v[148:151]\n\t"
    "ds_read_b64_tr_b16 a[254:255], v199 offset:45056\n\t"
    "v_cvt_pk_bf16_f32 v33, v34, v35\n\t"
    "v_mfma_f32_16x16x32_bf16 v[144:147], a[228:231], v[64:67], v[144:147]\n\t"
    "s_waitcnt lgkmcnt(8)\n\t"
    "v_exp_f32 v36, v36\n\t"
    "v_mfma_f32_16x16x32_bf16 v[160:163], a[232:235], v[64:67], v[160:163]\n\t"
    "v_exp_f32 v37, v37\n\t"
    "v_mfma_f32_16x16x32_bf16 v[164:167], a[232:235], v[80:83], v[164:167]\n\t"
    "v_exp_f32 v38, v38\n\t"
    "v_mfma_f32_16x16x32_bf16 v[168:171], a[232:235], v[96:99], v[168:171]\n\t"
    "s_add_u32 s52, s52, 24576\n\t"
    "v_cvt_pk_bf16_f32 v34, v36, v37\n\t"
    "v_fma_f32 v39, v39, s62, v214\n\t"
    "v_mfma_f32_16x16x32_bf16 v[172:175], a[232:235], v[112:115], v[172:175]\n\t"
    "v_exp_f32 v39, v39\n\t"
    "v_mfma_f32_16x16x32_bf16 v[188:191], a[236:239], v[112:115], v[188:191]\n\t"
    "v_cvt_pk_bf16_f32 v35, v38, v39\n\t"
    "v_fma_f32 v48, v48, s62, v215\n\t"
    "v_mfma_f32_16x16x32_bf16 v[184:187], a[236:239], v[96:99], v[184:187]\n\t"
    "v_exp_f32 v48, v48\n\t"
    "v_mfma_f32_16x16x32_bf16 v[180:183], a[236:239], v[80:83], v[180:183]\n\t"
    "s_waitcnt lgkmcnt(0) vmcnt(17)\n\t"
    "v_fma_f32 v49, v49, s62, v215\n\t"
    "v_fma_f32 v50, v50, s62, v215\n\t"
    "v_mfma_f32_16x16x32_bf16 v[176:179], a[236:239], v[64:67], v[176:179]\n\t"
    "v_exp_f32 v49, v49\n\t"
    "v_mfma_f32_16x16x32_bf16 v[128:131], a[240:243], v[72:75], v[128:131]\n\t"
    "s_barrier\n\t"
    "ds_read_b128 a[224:227], v192 offset:24576\n\t"
    "v_cvt_pk_bf16_f32 v48, v48, v49\n\t"
    "v_mfma_f32_16x16x32_bf16 v[132:135], a[240:243], v[88:91], v[132:135]\n\t"
    "ds_read_b128 a[228:231], v192 offset:30720\n\t"
    "v_fma_f32 v51, v51, s62, v215\n\t"
    "v_mfma_f32_16x16x32_bf16 v[136:139], a[240:243], v[104:107], v[136:139]\n\t"
    "ds_read_b128 a[232:235], v192 offset:36864\n\t"
    "v_fma_f32 v52, v52, s62, v215\n\t"
    "v_mfma_f32_16x16x32_bf16 v[140:143], a[240:243], v[120:123], v[140:143]\n\t"
    "v_exp_f32 v50, v50\n\t"
    "v_mfma_f32_16x16x32_bf16 v[156:159], a[244:247], v[120:123], v[156:159]\n\t"
    "v_exp_f32 v51, v51\n\t"
    "v_mfma_f32_16x16x32_bf16 v[152:155], a[244:247], v[104:107], v[152:155]\n\t"
    "v_exp_f32 v52, v52\n\t"
    "v_mfma_f32_16x16x32_bf16 v[148:151], a[244:247], v[88:91], v[148:151]\n\t"
    "ds_read_b128 a[236:239], v192 offset:43008\n\t"
    "v_cvt_pk_bf16_f32 v49, v50, v51\n\t"
    "v_mfma_f32_16x16x32_bf16 v[144:147], a[244:247], v[72:75], v[144:147]\n\t"
    "v_fma_f32 v53, v53, s62, v215\n\t"
    "v_fma_f32 v54, v54, s62, v215\n\t"
    "v_mfma_f32_16x16x32_bf16 v[160:163], a[248:251], v[72:75], v[160:163]\n\t"
    "v_exp_f32 v53, v53\n\t"
    "v_mfma_f32_16x16x32_bf16 v[164:167], a[248:251], v[88:91], v[164:167]\n\t"
    "v_exp_f32 v54, v54\n\t"
    "v_mfma_f32_16x16x32_bf16 v[168:171], a[248:251], v[104:107], v[168:171]\n\t"
    "ds_read_b128 a[240:243], v193 offset:24576\n\t"
    "v_cvt_pk_bf16_f32 v50, v52, v53\n\t"
    "v_mfma_f32_16x16x32_bf16 v[172:175], a[248:251], v[120:123], v[172:175]\n\t"
    "v_fma_f32 v55, v55, s62, v215\n\t"
    "v_fma_f32 v8, v8, s62, v212\n\t"
    "v_mfma_f32_16x16x32_bf16 v[188:191], a[252:255], v[120:123], v[188:191]\n\t"
    "v_exp_f32 v55, v55\n\t"
    "v_mfma_f32_16x16x32_bf16 v[184:187], a[252:255], v[104:107], v[184:187]\n\t"
    "v_exp_f32 v8, v8\n\t"
    "v_mfma_f32_16x16x32_bf16 v[180:183], a[252:255], v[88:91], v[180:183]\n\t"
    "ds_read_b128 a[244:247], v193 offset:30720\n\t"
    "v_cvt_pk_bf16_f32 v51, v54, v55\n\t"
    "v_mfma_f32_16x16x32_bf16 v[176:179], a[252:255], v[72:75], v[176:179]\n\t"
    "s_waitcnt lgkmcnt(4)\n\t"
    "v_fma_f32 v9, v9, s62, v212\n\t"
    "v_fma_f32 v10, v10, s62, v212\n\t"
    "v_mfma_f32_16x16x32_bf16 v[64:67], a[224:227], a[128:131], v[224:227]\n\t"
    "s_cmp_lg_u32 s72, 0\n\t"
    "s_cbranch_scc1 SB6\n\t"
    "NSB6:\n\t"
    "v_fma_f32 v11, v11, s62, v212\n\t"
    "v_mfma_f32_16x16x32_bf16 v[80:83], a[224:227], a[132:135], v[224:227]\n\t"
    "v_exp_f32 v9, v9\n\t"
    "v_mfma_f32_16x16x32_bf16 v[96:99], a[224:227], a[136:139], v[224:227]\n\t"
    "s_add_u32 m0, s57, 24576\n\t"
    "ds_read_b128 a[248:251], v193 offset:36864\n\t"
    "v_cvt_pk_bf16_f32 v8, v8, v9\n\t"
    "v_mfma_f32_16x16x32_bf16 v[112:115], a[224:227], a[140:143], v[224:227]\n\t"
    "buffer_load_dwordx4 v202, s[40:43], s53 offen offset:0 lds\n\t"
    "v_fma_f32 v12, v12, s62, v212\n\t"
    "v_mfma_f32_16x16x32_bf16 v[116:119], a[228:231], a[140:143], v[224:227]\n\t"
    "v_exp_f32 v10, v10\n\t"
    "v_mfma_f32_16x16x32_bf16 v[100:103], a[228:231], a[136:139], v[224:227]\n\t"
    "v_exp_f32 v11, v11\n\t"
    "v_mfma_f32_16x16x32_bf16 v[84:87], a[228:231], a[132:135], v[224:227]\n\t"
    "ds_read_b128 a[252:255], v193 offset:43008\n\t"
    "v_cvt_pk_bf16_f32 v9, v10, v11\n\t"
    "v_mfma_f32_16x16x32_bf16 v[68:71], a[228:231], a[128:131], v[224:227]\n\t"
    "s_waitcnt lgkmcnt(4)\n\t"
    "v_exp_f32 v12, v12\n\t"
    "v_mfma_f32_16x16x32_bf16 v[72:75], a[232:235], a[128:131], v[224:227]\n\t"
    "v_fma_f32 v13, v13, s62, v212\n\t"
    "v_fma_f32 v14, v14, s62, v212\n\t"
    "v_mfma_f32_16x16x32_bf16 v[88:91], a[232:235], a[132:135], v[224:227]\n\t"
    "v_exp_f32 v13, v13\n\t"
    "v_mfma_f32_16x16x32_bf16 v[104:107], a[232:235], a[136:139], v[224:227]\n\t"
    "ds_read_b128 a[224:227], v192 offset:25600\n\t"
    "v_cvt_pk_bf16_f32 v10, v12, v13\n\t"
    "v_mfma_f32_16x16x32_bf16 v[120:123], a[232:235], a[140:143], v[224:227]\n\t"
    "v_exp_f32 v14, v14\n\t"
    "v_mfma_f32_16x16x32_bf16 v[124:127], a[236:239], a[140:143], v[224:227]\n\t"
    "v_fma_f32 v15, v15, s62, v212\n\t"
    "v_fma_f32 v24, v24, s62, v213\n\t"
    "v_mfma_f32_16x16x32_bf16 v[108:111], a[236:239], a[136:139], v[224:227]\n\t"
    "v_exp_f32 v15, v15\n\t"
    "v_mfma_f32_16x16x32_bf16 v[92:95], a[236:239], a[132:135], v[224:227]\n\t"
    "s_add_u32 m0, s57, 24576\n\t"
    "ds_read_b128 a[228:231], v192 offset:31744\n\t"
    "v_cvt_pk_bf16_f32 v11, v14, v15\n\t"
    "v_mfma_f32_16x16x32_bf16 v[76:79], a[236:239], a[128:131], v[224:227]\n\t"
    "s_waitcnt lgkmcnt(4)\n\t"
    "buffer_load_dwordx4 v202, s[40:43], s53 offen offset:3072 lds\n\t"
    "v_fma_f32 v25, v25, s62, v213\n\t"
    "v_mfma_f32_16x16x32_bf16 v[64:67], a[240:243], a[144:147], v[64:67]\n\t"
    "ds_read_b128 a[128:131], v204 offset:0\n\t"
    "v_fma_f32 v26, v26, s62, v213\n\t"
    "v_mfma_f32_16x16x32_bf16 v[80:83], a[240:243], a[148:151], v[80:83]\n\t"
    "v_exp_f32 v24, v24\n\t"
    "v_mfma_f32_16x16x32_bf16 v[96:99], a[240:243], a[152:155], v[96:99]\n\t"
    "ds_read_b128 a[232:235], v192 offset:37888\n\t"
    "v_fma_f32 v27, v27, s62, v213\n\t"
    "v_mfma_f32_16x16x32_bf16 v[112:115], a[240:243], a[156:159], v[112:115]\n\t"
    "ds_read_b128 a[132:135], v204 offset:2048\n\t"
    "v_fma_f32 v28, v28, s62, v213\n\t"
    "v_mfma_f32_16x16x32_bf16 v[116:119], a[244:247], a[156:159], v[116:119]\n\t"
    "ds_read_b128 a[136:139], v204 offset:4096\n\t"
    "v_fma_f32 v29, v29, s62, v213\n\t"
    "v_mfma_f32_16x16x32_bf16 v[100:103], a[244:247], a[152:155], v[100:103]\n\t"
    "v_exp_f32 v25, v25\n\t"
    "v_mfma_f32_16x16x32_bf16 v[84:87], a[244:247], a[148:151], v[84:87]\n\t"
    "ds_read_b128 a[236:239], v192 offset:44032\n\t"
    "v_cvt_pk_bf16_f32 v24, v24, v25\n\t"
    "v_mfma_f32_16x16x32_bf16 v[68:71], a[244:247], a[144:147], v[68:71]\n\t"
    "s_waitcnt lgkmcnt(7)\n\t"
    "ds_read_b128 a[140:143], v204 offset:6144\n\t"
    "v_fma_f32 v30, v30, s62, v213\n\t"
    "v_mfma_f32_16x16x32_bf16 v[72:75], a[248:251], a[144:147], v[72:75]\n\t"
    "v_exp_f32 v26, v26\n\t"
    "v_mfma_f32_16x16x32_bf16 v[88:91], a[248:251], a[148:151], v[88:91]\n\t"
    "v_exp_f32 v27, v27\n\t"
    "v_mfma_f32_16x16x32_bf16 v[104:107], a[248:251], a[152:155], v[104:107]\n\t"
    "s_add_u32 m0, s57, 25472\n\t"
    "ds_read_b128 a[240:243], v193 offset:25600\n\t"
    "v_cvt_pk_bf16_f32 v25, v26, v27\n\t"
    "v_mfma_f32_16x16x32_bf16 v[120:123], a[248:251], a[156:159], v[120:123]\n\t"
    "buffer_load_dwordx4 v202, s[40:43], s53 offen offset:128 lds\n\t"
    "v_fma_f32 v31, v31, s62, v213\n\t"
    "v_mfma_f32_16x16x32_bf16 v[124:127], a[252:255], a[156:159], v[124:127]\n\t"
    "v_exp_f32 v28, v28\n\t"
    "v_mfma_f32_16x16x32_bf16 v[108:111], a[252:255], a[152:155], v[108:111]\n\t"
    "v_exp_f32 v29, v29\n\t"
    "v_mfma_f32_16x16x32_bf16 v[92:95], a[252:255], a[148:151], v[92:95]\n\t"
    "ds_read_b128 a[244:247], v193 offset:31744\n\t"
    "v_cvt_pk_bf16_f32 v26, v28, v29\n\t"
    "v_mfma_f32_16x16x32_bf16 v[76:79], a[252:255], a[144:147], v[76:79]\n\t"
    "s_waitcnt lgkmcnt(8)\n\t"
    "v_exp_f32 v30, v30\n\t"
    "v_mfma_f32_16x16x32_bf16 v[64:67], a[224:227], a[160:163], v[64:67]\n\t"
    "ds_read_b128 a[144:147], v205 offset:0\n\t"
    "v_fma_f32 v40, v40, s62, v214\n\t"
    "v_mfma_f32_16x16x32_bf16 v[80:83], a[224:227], a[164:167], v[80:83]\n\t"
    "v_exp_f32 v31, v31\n\t"
    "v_mfma_f32_16x16x32_bf16 v[96:99], a[224:227], a[168:171], v[96:99]\n\t"
    "ds_read_b128 a[248:251], v193 offset:37888\n\t"
    "v_cvt_pk_bf16_f32 v27, v30, v31\n\t"
    "v_mfma_f32_16x16x32_bf16 v[112:115], a[224:227], a[172:175], v[112:115]\n\t"
    "ds_read_b128 a[148:151], v205 offset:2048\n\t"
    "v_fma_f32 v41, v41, s62, v214\n\t"
    "v_mfma_f32_16x16x32_bf16 v[116:119], a[228:231], a[172:175], v[116:119]\n\t"
    "ds_read_b128 a[152:155], v205 offset:4096\n\t"
    "v_fma_f32 v42, v42, s62, v214\n\t"
    "v_mfma_f32_16x16x32_bf16 v[100:103], a[228:231], a[168:171], v[100:103]\n\t"
    "v_exp_f32 v40, v40\n\t"
    "v_mfma_f32_16x16x32_bf16 v[84:87], a[228:231], a[164:167], v[84:87]\n\t"
    "s_add_u32 m0, s57, 25472\n\t"
    "ds_read_b128 a[252:255], v193 offset:44032\n\t"
    "v_fma_f32 v43, v43, s62, v214\n\t"
    "v_mfma_f32_16x16x32_bf16 v[68:71], a[228:231], a[160:163], v[68:71]\n\t"
    "s_waitcnt lgkmcnt(8)\n\t"
    "buffer_load_dwordx4 v202, s[40:43], s53 offen offset:3200 lds\n\t"
    "v_fma_f32 v44, v44, s62, v214\n\t"
    "v_mfma_f32_16x16x32_bf16 v[72:75], a[232:235], a[160:163], v[72:75]\n\t"
    "ds_read_b128 a[156:159], v205 offset:6144\n\t"
    "v_fma_f32 v45, v45, s62, v214\n\t"
    "v_mfma_f32_16x16x32_bf16 v[88:91], a[232:235], a[164:167], v[88:91]\n\t"
    "v_exp_f32 v41, v41\n\t"
    "v_mfma_f32_16x16x32_bf16 v[104:107], a[232:235], a[168:171], v[104:107]\n\t"
    "ds_read_b128 a[224:227], v192 offset:26624\n\t"
    "v_cvt_pk_bf16_f32 v40, v40, v41\n\t"
    "v_mfma_f32_16x16x32_bf16 v[120:123], a[232:235], a[172:175], v[120:123]\n\t"
    "v_exp_f32 v42, v42\n\t"
    "v_mfma_f32_16x16x32_bf16 v[124:127], a[236:239], a[172:175], v[124:127]\n\t"
    "s_waitcnt lgkmcnt(1)\n\t"
    "v_exp_f32 v43, v43\n\t"
    "v_mfma_f32_16x16x32_bf16 v[108:111], a[236:239], a[168:171], v[108:111]\n\t"
    "s_add_u32 m0, s58, 4294967040\n\t"
    "v_exp_f32 v44, v44\n\t"
    "v_mfma_f32_16x16x32_bf16 v[92:95], a[236:239], a[164:167], v[92:95]\n\t"
    "s_add_u32 s75, s54, s96\n\t"
    "ds_read_b128 a[228:231], v192 offset:32768\n\t"
    "v_cvt_pk_bf16_f32 v41, v42, v43\n\t"
    "v_mfma_f32_16x16x32_bf16 v[76:79], a[236:239], a[160:163], v[76:79]\n\t"
    "buffer_load_dwordx4 v200, s[44:47], s75 offen offset:256 nt lds\n\t"
    "v_fma_f32 v46, v46, s62, v214\n\t"
    "v_mfma_f32_16x16x32_bf16 v[64:67], a[240:243], a[176:179], v[64:67]\n\t"
    "s_waitcnt vmcnt(11)\n\t"
    "ds_read_b128 a[160:163], v204 offset:8192\n\t"
    "v_fma_f32 v47, v47, s62, v214\n\t"
    "v_mfma_f32_16x16x32_bf16 v[80:83], a[240:243], a[180:183], v[80:83]\n\t"
    "v_exp_f32 v45, v45\n\t"
    "v_mfma_f32_16x16x32_bf16 v[96:99], a[240:243], a[184:187], v[96:99]\n\t"
    "s_add_u32 m0, s57, 26368\n\t"
    "ds_read_b128 a[232:235], v192 offset:38912\n\t"
    "v_cvt_pk_bf16_f32 v42, v44, v45\n\t"
    "v_mfma_f32_16x16x32_bf16 v[112:115], a[240:243], a[188:191], v[112:115]\n\t"
    "buffer_load_dwordx4 v202, s[40:43], s53 offen offset:256 lds\n\t"
    "v_fma_f32 v56, v56, s62, v215\n\t"
    "v_mfma_f32_16x16x32_bf16 v[116:119], a[244:247], a[188:191], v[116:119]\n\t"
    "ds_read_b128 a[164:167], v204 offset:10240\n\t"
    "v_fma_f32 v57, v57, s62, v215\n\t"
    "v_mfma_f32_16x16x32_bf16 v[100:103], a[244:247], a[184:187], v[100:103]\n\t"
    "ds_read_b128 a[168:171], v204 offset:12288\n\t"
    "v_fma_f32 v58, v58, s62, v215\n\t"
    "v_mfma_f32_16x16x32_bf16 v[84:87], a[244:247], a[180:183], v[84:87]\n\t"
    "ds_read_b128 a[236:239], v192 offset:45056\n\t"
    "v_fma_f32 v59, v59, s62, v215\n\t"
    "v_mfma_f32_16x16x32_bf16 v[68:71], a[244:247], a[176:179], v[68:71]\n\t"
    "ds_read_b128 a[172:175], v204 offset:14336\n\t"
    "v_mfma_f32_16x16x32_bf16 v[72:75], a[248:251], a[176:179], v[72:75]\n\t"
    "s_add_u32 m0, s58, 4294964992\n\t"
    "v_exp_f32 v46, v46\n\t"
    "v_mfma_f32_16x16x32_bf16 v[88:91], a[248:251], a[180:183], v[88:91]\n\t"
    "s_add_u32 s75, s54, s96\n\t"
    "buffer_load_dwordx4 v201, s[44:47], s75 offen offset:3328 nt lds\n\t"
    "v_fma_f32 v60, v60, s62, v215\n\t"
    "v_mfma_f32_16x16x32_bf16 v[104:107], a[248:251], a[184:187], v[104:107]\n\t"
    "ds_read_b128 a[240:243], v193 offset:26624\n\t"
    "v_mfma_f32_16x16x32_bf16 v[120:123], a[248:251], a[188:191], v[120:123]\n\t"
    "v_exp_f32 v47, v47\n\t"
    "v_mfma_f32_16x16x32_bf16 v[124:127], a[252:255], a[188:191], v[124:127]\n\t"
    "v_exp_f32 v56, v56\n\t"
    "v_mfma_f32_16x16x32_bf16 v[108:111], a[252:255], a[184:187], v[108:111]\n\t"
    "v_exp_f32 v57, v57\n\t"
    "v_mfma_f32_16x16x32_bf16 v[92:95], a[252:255], a[180:183], v[92:95]\n\t"
    "s_add_u32 m0, s57, 26368\n\t"
    "ds_read_b128 a[244:247], v193 offset:32768\n\t"
    "v_cvt_pk_bf16_f32 v43, v46, v47\n\t"
    "v_mfma_f32_16x16x32_bf16 v[76:79], a[252:255], a[176:179], v[76:79]\n\t"
    "s_waitcnt lgkmcnt(8)\n\t"
    "buffer_load_dwordx4 v202, s[40:43], s53 offen offset:3328 lds\n\t"
    "v_cvt_pk_bf16_f32 v56, v56, v57\n\t"
    "v_mfma_f32_16x16x32_bf16 v[64:67], a[224:227], a[192:195], v[64:67]\n\t"
    "ds_read_b128 a[176:179], v205 offset:8192\n\t"
    "v_fma_f32 v61, v61, s62, v215\n\t"
    "v_mfma_f32_16x16x32_bf16 v[80:83], a[224:227], a[196:199], v[80:83]\n\t"
    "v_exp_f32 v58, v58\n\t"
    "v_mfma_f32_16x16x32_bf16 v[96:99], a[224:227], a[200:203], v[96:99]\n\t"
    "ds_read_b128 a[248:251], v193 offset:38912\n\t"
    "v_fma_f32 v62, v62, s62, v215\n\t"
    "v_mfma_f32_16x16x32_bf16 v[112:115], a[224:227], a[204:207], v[112:115]\n\t"
    "ds_read_b128 a[180:183], v205 offset:10240\n\t"
    "v_fma_f32 v63, v63, s62, v215\n\t"
    "v_mfma_f32_16x16x32_bf16 v[116:119], a[228:231], a[204:207], v[116:119]\n\t"
    "ds_read_b128 a[184:187], v205 offset:12288\n\t"
    "v_mfma_f32_16x16x32_bf16 v[100:103], a[228:231], a[200:203], v[100:103]\n\t"
    "s_add_u32 m0, s58, 1792\n\t"
    "v_exp_f32 v59, v59\n\t"
    "v_mfma_f32_16x16x32_bf16 v[84:87], a[228:231], a[196:199], v[84:87]\n\t"
    "s_add_u32 s75, s54, s97\n\t"
    "ds_read_b128 a[252:255], v193 offset:45056\n\t"
    "v_cvt_pk_bf16_f32 v57, v58, v59\n\t"
    "v_mfma_f32_16x16x32_bf16 v[68:71], a[228:231], a[192:195], v[68:71]\n\t"
    "s_waitcnt lgkmcnt(8)\n\t"
    "buffer_load_dwordx4 v200, s[44:47], s75 offen offset:256 nt lds\n\t"
    "v_mfma_f32_16x16x32_bf16 v[72:75], a[232:235], a[192:195], v[72:75]\n\t"
    "ds_read_b128 a[188:191], v205 offset:14336\n\t"
    "v_mfma_f32_16x16x32_bf16 v[88:91], a[232:235], a[196:199], v[88:91]\n\t"
    "v_exp_f32 v60, v60\n\t"
    "v_mfma_f32_16x16x32_bf16 v[104:107], a[232:235], a[200:203], v[104:107]\n\t"
    "s_add_u32 s53, s53, 24993792\n\t"
    "v_exp_f32 v61, v61\n\t"
    "v_mfma_f32_16x16x32_bf16 v[120:123], a[232:235], a[204:207], v[120:123]\n\t"
    "s_add_u32 m0, s58, 4294967040\n\t"
    "v_exp_f32 v62, v62\n\t"
    "v_mfma_f32_16x16x32_bf16 v[124:127], a[236:239], a[204:207], v[124:127]\n\t"
    "s_add_u32 s75, s54, s97\n\t"
    "buffer_load_dwordx4 v201, s[44:47], s75 offen offset:3328 nt lds\n\t"
    "v_cvt_pk_bf16_f32 v58, v60, v61\n\t"
    "v_mfma_f32_16x16x32_bf16 v[108:111], a[236:239], a[200:203], v[108:111]\n\t"
    "v_exp_f32 v63, v63\n\t"
    "v_mfma_f32_16x16x32_bf16 v[92:95], a[236:239], a[196:199], v[92:95]\n\t"
    "s_waitcnt lgkmcnt(1)\n\t"
    "v_cvt_pk_bf16_f32 v59, v62, v63\n\t"
    "v_mfma_f32_16x16x32_bf16 v[76:79], a[236:239], a[192:195], v[76:79]\n\t"
    "v_mfma_f32_16x16x32_bf16 v[64:67], a[240:243], a[208:211], v[64:67]\n\t"
    "s_barrier\n\t"
    "ds_read_b64_tr_b16 a[224:225], v196 offset:0\n\t"
    "v_mfma_f32_16x16x32_bf16 v[80:83], a[240:243], a[212:215], v[80:83]\n\t"
    "s_add_u32 m0, s58, 3840\n\t"
    "ds_read_b64_tr_b16 a[226:227], v196 offset:6144\n\t"
    "v_mfma_f32_16x16x32_bf16 v[96:99], a[240:243], a[216:219], v[96:99]\n\t"
    "s_add_u32 s75, s54, s98\n\t"
    "ds_read_b64_tr_b16 a[228:229], v197 offset:0\n\t"
    "v_mfma_f32_16x16x32_bf16 v[112:115], a[240:243], a[220:223], v[112:115]\n\t"
    "ds_read_b64_tr_b16 a[230:231], v197 offset:6144\n\t"
    "v_mfma_f32_16x16x32_bf16 v[116:119], a[244:247], a[220:223], v[116:119]\n\t"
    "ds_read_b64_tr_b16 a[232:233], v198 offset:0\n\t"
    "v_mfma_f32_16x16x32_bf16 v[100:103], a[244:247], a[216:219], v[100:103]\n\t"
    "ds_read_b64_tr_b16 a[234:235], v198 offset:6144\n\t"
    "v_mfma_f32_16x16x32_bf16 v[84:87], a[244:247], a[212:215], v[84:87]\n\t"
    "ds_read_b64_tr_b16 a[236:237], v199 offset:0\n\t"
    "v_mfma_f32_16x16x32_bf16 v[68:71], a[244:247], a[208:211], v[68:71]\n\t"
    "ds_read_b64_tr_b16 a[238:239], v199 offset:6144\n\t"
    "v_mfma_f32_16x16x32_bf16 v[72:75], a[248:251], a[208:211], v[72:75]\n\t"
    "ds_read_b64_tr_b16 a[240:241], v196 offset:12288\n\t"
    "v_mfma_f32_16x16x32_bf16 v[88:91], a[248:251], a[212:215], v[88:91]\n\t"
    "buffer_load_dwordx4 v200, s[44:47], s75 offen offset:256 nt lds\n\t"
    "v_mfma_f32_16x16x32_bf16 v[104:107], a[248:251], a[216:219], v[104:107]\n\t"
    "s_add_u32 m0, s58, 1792\n\t"
    "ds_read_b64_tr_b16 a[242:243], v196 offset:18432\n\t"
    "v_mfma_f32_16x16x32_bf16 v[120:123], a[248:251], a[220:223], v[120:123]\n\t"
    "s_add_u32 s75, s54, s98\n\t"
    "buffer_load_dwordx4 v201, s[44:47], s75 offen offset:3328 nt lds\n\t"
    "v_mfma_f32_16x16x32_bf16 v[124:127], a[252:255], a[220:223], v[124:127]\n\t"
    "s_add_u32 m0, s58, 5888\n\t"
    "ds_read_b64_tr_b16 a[244:245], v197 offset:12288\n\t"
    "v_mfma_f32_16x16x32_bf16 v[108:111], a[252:255], a[216:219], v[108:111]\n\t"
    "s_add_u32 s75, s54, s99\n\t"
    "buffer_load_dwordx4 v200, s[44:47], s75 offen offset:256 nt lds\n\t"
    "v_mfma_f32_16x16x32_bf16 v[92:95], a[252:255], a[212:215], v[92:95]\n\t"
    "ds_read_b64_tr_b16 a[246:247], v197 offset:18432\n\t"
    "v_mfma_f32_16x16x32_bf16 v[76:79], a[252:255], a[208:211], v[76:79]\n\t"
    "s_waitcnt lgkmcnt(8)\n\t"
    "v_mfma_f32_16x16x32_bf16 a[0:3], a[224:227], v[0:3], a[0:3]\n\t"
    "ds_read_b64_tr_b16 a[248:249], v198 offset:12288\n\t"
    "v_mfma_f32_16x16x32_bf16 a[4:7], a[224:227], v[16:19], a[4:7]\n\t"
    "v_mfma_f32_16x16x32_bf16 a[8:11], a[224:227], v[32:35], a[8:11]\n\t"
    "s_add_u32 m0, s56, 24576\n\t"
    "ds_read_b64_tr_b16 a[250:251], v198 offset:18432\n\t"
    "v_mfma_f32_16x16x32_bf16 a[12:15], a[224:227], v[48:51], a[12:15]\n\t"
    "buffer_load_dwordx4 v200, s[36:39], s52 offen offset:0 lds\n\t"
    "v_mfma_f32_16x16x32_bf16 a[28:31], a[228:231], v[48:51], a[28:31]\n\t"
    "ds_read_b64_tr_b16 a[252:253], v199 offset:12288\n\t"
    "v_max3_f32 v220, v64, v65, v66\n\t"
    "v_mfma_f32_16x16x32_bf16 a[24:27], a[228:231], v[32:35], a[24:27]\n\t"
    "v_max3_f32 v221, v80, v81, v82\n\t"
    "v_max3_f32 v222, v96, v97, v98\n\t"
    "v_mfma_f32_16x16x32_bf16 a[20:23], a[228:231], v[16:19], a[20:23]\n\t"
    "ds_read_b64_tr_b16 a[254:255], v199 offset:18432\n\t"
    "v_max3_f32 v223, v112, v113, v114\n\t"
    "v_mfma_f32_16x16x32_bf16 a[16:19], a[228:231], v[0:3], a[16:19]\n\t"
    "s_waitcnt lgkmcnt(8)\n\t"
    "v_max3_f32 v220, v220, v67, v68\n\t"
    "v_max3_f32 v221, v221, v83, v84\n\t"
    "v_mfma_f32_16x16x32_bf16 a[32:35], a[232:235], v[0:3], a[32:35]\n\t"
    "s_add_u32 m0, s58, 3840\n\t"
    "ds_read_b64_tr_b16 a[224:225], v196 offset:1024\n\t"
    "v_max3_f32 v222, v222, v99, v100\n\t"
    "v_mfma_f32_16x16x32_bf16 a[36:39], a[232:235], v[16:19], a[36:39]\n\t"
    "s_add_u32 s75, s54, s99\n\t"
    "buffer_load_dwordx4 v201, s[44:47], s75 offen offset:3328 nt lds\n\t"
    "v_max3_f32 v223, v223, v115, v116\n\t"
    "v_mfma_f32_16x16x32_bf16 v[228:231], v[244:247], v[0:3], v[228:231]\n\t"
    "v_max3_f32 v220, v220, v69, v70\n\t"
    "v_max3_f32 v221, v221, v85, v86\n\t"
    "v_mfma_f32_16x16x32_bf16 a[40:43], a[232:235], v[32:35], a[40:43]\n\t"
    "ds_read_b64_tr_b16 a[226:227], v196 offset:7168\n\t"
    "v_max3_f32 v222, v222, v101, v102\n\t"
    "v_mfma_f32_16x16x32_bf16 a[44:47], a[232:235], v[48:51], a[44:47]\n\t"
    "v_max3_f32 v223, v223, v117, v118\n\t"
    "v_max3_f32 v220, v220, v71, v72\n\t"
    "v_mfma_f32_16x16x32_bf16 a[60:63], a[236:239], v[48:51], a[60:63]\n\t"
    "ds_read_b64_tr_b16 a[228:229], v197 offset:1024\n\t"
    "v_max3_f32 v221, v221, v87, v88\n\t"
    "v_mfma_f32_16x16x32_bf16 a[56:59], a[236:239], v[32:35], a[56:59]\n\t"
    "v_max3_f32 v222, v222, v103, v104\n\t"
    "v_max3_f32 v223, v223, v119, v120\n\t"
    "v_mfma_f32_16x16x32_bf16 v[232:235], v[244:247], v[16:19], v[232:235]\n\t"
    "v_max3_f32 v220, v220, v73, v74\n\t"
    "v_max3_f32 v221, v221, v89, v90\n\t"
    "v_mfma_f32_16x16x32_bf16 a[52:55], a[236:239], v[16:19], a[52:55]\n\t"
    "s_add_u32 m0, s56, 24576\n\t"
    "ds_read_b64_tr_b16 a[230:231], v197 offset:7168\n\t"
    "v_max3_f32 v222, v222, v105, v106\n\t"
    "v_mfma_f32_16x16x32_bf16 a[48:51], a[236:239], v[0:3], a[48:51]\n\t"
    "s_waitcnt lgkmcnt(8)\n\t"
    "buffer_load_dwordx4 v201, s[36:39], s52 offen offset:3072 lds\n\t"
    "v_max3_f32 v223, v223, v121, v122\n\t"
    "v_mfma_f32_16x16x32_bf16 a[0:3], a[240:243], v[8:11], a[0:3]\n\t"
    "ds_read_b64_tr_b16 a[232:233], v198 offset:1024\n\t"
    "v_max3_f32 v220, v220, v75, v76\n\t"
    "v_mfma_f32_16x16x32_bf16 a[4:7], a[240:243], v[24:27], a[4:7]\n\t"
    "v_max3_f32 v221, v221, v91, v92\n\t"
    "v_max3_f32 v222, v222, v107, v108\n\t"
    "v_mfma_f32_16x16x32_bf16 v[236:239], v[244:247], v[32:35], v[236:239]\n\t"
    "v_max3_f32 v223, v223, v123, v124\n\t"
    "v_max3_f32 v220, v220, v77, v78\n\t"
    "v_mfma_f32_16x16x32_bf16 a[8:11], a[240:243], v[40:43], a[8:11]\n\t"
    "ds_read_b64_tr_b16 a[234:235], v198 offset:7168\n\t"
    "v_max3_f32 v221, v221, v93, v94\n\t"
    "v_mfma_f32_16x16x32_bf16 a[12:15], a[240:243], v[56:59], a[12:15]\n\t"
    "v_max3_f32 v222, v222, v109, v110\n\t"
    "v_max3_f32 v223, v223, v125, v126\n\t"
    "v_mfma_f32_16x16x32_bf16 a[28:31], a[244:247], v[56:59], a[28:31]\n\t"
    "ds_read_b64_tr_b16 a[236:237], v199 offset:1024\n\t"
    "v_max_f32 v220, v220, v79\n\t"
    "v_mfma_f32_16x16x32_bf16 a[24:27], a[244:247], v[40:43], a[24:27]\n\t"
    "v_max_f32 v221, v221, v95\n\t"
    "v_max_f32 v222, v222, v111\n\t"
    "v_mfma_f32_16x16x32_bf16 v[240:243], v[244:247], v[48:51], v[240:243]\n\t"
    "v_max_f32 v223, v223, v127\n\t"
    "v_cmp_gt_f32 vcc, v220, v216\n\t"
    "v_mfma_f32_16x16x32_bf16 a[20:23], a[244:247], v[24:27], a[20:23]\n\t"
    "ds_read_b64_tr_b16 a[238:239], v199 offset:7168\n\t"
    "v_cmp_gt_f32 s[88:89], v221, v217\n\t"
    "v_mfma_f32_16x16x32_bf16 a[16:19], a[244:247], v[8:11], a[16:19]\n\t"
    "s_waitcnt lgkmcnt(8)\n\t"
    "s_or_b64 vcc, vcc, s[88:89]\n\t"
    "v_cmp_gt_f32 s[90:91], v222, v218\n\t"
    "v_mfma_f32_16x16x32_bf16 a[32:35], a[248:251], v[8:11], a[32:35]\n\t"
    "ds_read_b64_tr_b16 a[240:241], v196 offset:13312\n\t"
    "v_cmp_gt_f32 s[92:93], v223, v219\n\t"
    "v_mfma_f32_16x16x32_bf16 a[36:39], a[248:251], v[24:27], a[36:39]\n\t"
    "s_or_b64 s[88:89], s[90:91], s[92:93]\n\t"
    "s_or_b64 vcc, vcc, s[88:89]\n\t"
    "s_cbranch_scc1 SA7\n\t"
    "NSA7:\n\t"
    "v_mfma_f32_16x16x32_bf16 a[40:43], a[248:251], v[40:43], a[40:43]\n\t"
    "s_add_u32 m0, s56, 25472\n\t"
    "ds_read_b64_tr_b16 a[242:243], v196 offset:19456\n\t"
    "v_fma_f32 v64, v64, s62, v212\n\t"
    "v_mfma_f32_16x16x32_bf16 a[44:47], a[248:251], v[56:59], a[44:47]\n\t"
    "buffer_load_dwordx4 v200, s[36:39], s52 offen offset:128 lds\n\t"
    "v_fma_f32 v65, v65, s62, v212\n\t"
    "v_mfma_f32_16x16x32_bf16 a[60:63], a[252:255], v[56:59], a[60:63]\n\t"
    "ds_read_b64_tr_b16 a[244:245], v197 offset:13312\n\t"
    "v_fma_f32 v66, v66, s62, v212\n\t"
    "v_mfma_f32_16x16x32_bf16 a[56:59], a[252:255], v[40:43], a[56:59]\n\t"
    "v_exp_f32 v64, v64\n\t"
    "v_mfma_f32_16x16x32_bf16 v[228:231], v[244:247], v[8:11], v[228:231]\n\t"
    "v_exp_f32 v65, v65\n\t"
    "v_mfma_f32_16x16x32_bf16 a[52:55], a[252:255], v[24:27], a[52:55]\n\t"
    "ds_read_b64_tr_b16 a[246:247], v197 offset:19456\n\t"
    "v_cvt_pk_bf16_f32 v64, v64, v65\n\t"
    "v_mfma_f32_16x16x32_bf16 a[48:51], a[252:255], v[8:11], a[48:51]\n\t"
    "s_waitcnt lgkmcnt(8)\n\t"
    "v_exp_f32 v66, v66\n\t"
    "v_mfma_f32_16x16x32_bf16 a[64:67], a[224:227], v[0:3], a[64:67]\n\t"
    "ds_read_b64_tr_b16 a[248:249], v198 offset:13312\n\t"
    "v_fma_f32 v67, v67, s62, v212\n\t"
    "v_mfma_f32_16x16x32_bf16 a[68:71], a[224:227], v[16:19], a[68:71]\n\t"
    "v_exp_f32 v67, v67\n\t"
    "v_mfma_f32_16x16x32_bf16 v[232:235], v[244:247], v[24:27], v[232:235]\n\t"
    "v_cvt_pk_bf16_f32 v65, v66, v67\n\t"
    "v_fma_f32 v68, v68, s62, v212\n\t"
    "v_mfma_f32_16x16x32_bf16 a[72:75], a[224:227], v[32:35], a[72:75]\n\t"
    "ds_read_b64_tr_b16 a[250:251], v198 offset:19456\n\t"
    "v_fma_f32 v69, v69, s62, v212\n\t"
    "v_mfma_f32_16x16x32_bf16 a[76:79], a[224:227], v[48:51], a[76:79]\n\t"
    "v_exp_f32 v68, v68\n\t"
    "v_mfma_f32_16x16x32_bf16 a[92:95], a[228:231], v[48:51], a[92:95]\n\t"
    "ds_read_b64_tr_b16 a[252:253], v199 offset:13312\n\t"
    "v_fma_f32 v70, v70, s62, v212\n\t"
    "v_mfma_f32_16x16x32_bf16 a[88:91], a[228:231], v[32:35], a[88:91]\n\t"
    "v_exp_f32 v69, v69\n\t"
    "v_mfma_f32_16x16x32_bf16 v[236:239], v[244:247], v[40:43], v[236:239]\n\t"
    "v_exp_f32 v70, v70\n\t"
    "v_mfma_f32_16x16x32_bf16 a[84:87], a[228:231], v[16:19], a[84:87]\n\t"
    "s_add_u32 m0, s56, 25472\n\t"
    "ds_read_b64_tr_b16 a[254:255], v199 offset:19456\n\t"
    "v_cvt_pk_bf16_f32 v66, v68, v69\n\t"
    "v_mfma_f32_16x16x32_bf16 a[80:83], a[228:231], v[0:3], a[80:83]\n\t"
    "s_waitcnt lgkmcnt(8)\n\t"
    "buffer_load_dwordx4 v201, s[36:39], s52 offen offset:3200 lds\n\t"
    "v_fma_f32 v71, v71, s62, v212\n\t"
    "v_mfma_f32_16x16x32_bf16 a[96:99], a[232:235], v[0:3], a[96:99]\n\t"
    "ds_read_b64_tr_b16 a[224:225], v196 offset:2048\n\t"
    "v_fma_f32 v80, v80, s62, v213\n\t"
    "v_mfma_f32_16x16x32_bf16 a[100:103], a[232:235], v[16:19], a[100:103]\n\t"
    "v_exp_f32 v71, v71\n\t"
    "v_mfma_f32_16x16x32_bf16 v[240:243], v[244:247], v[56:59], v[240:243]\n\t"
    "v_exp_f32 v80, v80\n\t"
    "v_mfma_f32_16x16x32_bf16 a[104:107], a[232:235], v[32:35], a[104:107]\n\t"
    "ds_read_b64_tr_b16 a[226:227], v196 offset:8192\n\t"
    "v_cvt_pk_bf16_f32 v67, v70, v71\n\t"
    "v_mfma_f32_16x16x32_bf16 a[108:111], a[232:235], v[48:51], a[108:111]\n\t"
    "v_fma_f32 v81, v81, s62, v213\n\t"
    "v_fma_f32 v82, v82, s62, v213\n\t"
    "v_mfma_f32_16x16x32_bf16 a[124:127], a[236:239], v[48:51], a[124:127]\n\t"
    "ds_read_b64_tr_b16 a[228:229], v197 offset:2048\n\t"
    "v_fma_f32 v83, v83, s62, v213\n\t"
    "v_mfma_f32_16x16x32_bf16 a[120:123], a[236:239], v[32:35], a[120:123]\n\t"
    "v_exp_f32 v81, v81\n\t"
    "v_mfma_f32_16x16x32_bf16 a[116:119], a[236:239], v[16:19], a[116:119]\n\t"
    "ds_read_b64_tr_b16 a[230:231], v197 offset:8192\n\t"
    "v_cvt_pk_bf16_f32 v80, v80, v81\n\t"
    "v_mfma_f32_16x16x32_bf16 a[112:115], a[236:239], v[0:3], a[112:115]\n\t"
    "s_waitcnt lgkmcnt(8)\n\t"
    "v_exp_f32 v82, v82\n\t"
    "v_mfma_f32_16x16x32_bf16 a[64:67], a[240:243], v[8:11], a[64:67]\n\t"
    "ds_read_b64_tr_b16 a[232:233], v198 offset:2048\n\t"
    "v_fma_f32 v84, v84, s62, v213\n\t"
    "v_mfma_f32_16x16x32_bf16 a[68:71], a[240:243], v[24:27], a[68:71]\n\t"
    "v_exp_f32 v83, v83\n\t"
    "v_mfma_f32_16x16x32_bf16 a[72:75], a[240:243], v[40:43], a[72:75]\n\t"
    "s_add_u32 m0, s56, 26368\n\t"
    "ds_read_b64_tr_b16 a[234:235], v198 offset:8192\n\t"
    "v_cvt_pk_bf16_f32 v81, v82, v83\n\t"
    "v_mfma_f32_16x16x32_bf16 a[76:79], a[240:243], v[56:59], a[76:79]\n\t"
    "buffer_load_dwordx4 v200, s[36:39], s52 offen offset:256 lds\n\t"
    "v_fma_f32 v85, v85, s62, v213\n\t"
    "v_mfma_f32_16x16x32_bf16 a[92:95], a[244:247], v[56:59], a[92:95]\n\t"
    "ds_read_b64_tr_b16 a[236:237], v199 offset:2048\n\t"
    "v_fma_f32 v86, v86, s62, v213\n\t"
    "v_mfma_f32_16x16x32_bf16 a[88:91], a[244:247], v[40:43], a[88:91]\n\t"
    "v_exp_f32 v84, v84\n\t"
    "v_mfma_f32_16x16x32_bf16 a[84:87], a[244:247], v[24:27], a[84:87]\n\t"
    "ds_read_b64_tr_b16 a[238:239], v199 offset:8192\n\t"
    "v_fma_f32 v87, v87, s62, v213\n\t"
    "v_mfma_f32_16x16x32_bf16 a[80:83], a[244:247], v[8:11], a[80:83]\n\t"
    "s_waitcnt lgkmcnt(8)\n\t"
    "v_exp_f32 v85, v85\n\t"
    "v_mfma_f32_16x16x32_bf16 a[96:99], a[248:251], v[8:11], a[96:99]\n\t"
    "ds_read_b64_tr_b16 a[240:241], v196 offset:14336\n\t"
    "v_cvt_pk_bf16_f32 v82, v84, v85\n\t"
    "v_mfma_f32_16x16x32_bf16 a[100:103], a[248:251], v[24:27], a[100:103]\n\t"
    "v_exp_f32 v86, v86\n\t"
    "v_mfma_f32_16x16x32_bf16 a[104:107], a[248:251], v[40:43], a[104:107]\n\t"
    "ds_read_b64_tr_b16 a[242:243], v196 offset:20480\n\t"
    "v_fma_f32 v96, v96, s62, v214\n\t"
    "v_mfma_f32_16x16x32_bf16 a[108:111], a[248:251], v[56:59], a[108:111]\n\t"
    "v_exp_f32 v87, v87\n\t"
    "v_mfma_f32_16x16x32_bf16 a[124:127], a[252:255], v[56:59], a[124:127]\n\t"
    "ds_read_b64_tr_b16 a[244:245], v197 offset:14336\n\t"
    "v_cvt_pk_bf16_f32 v83, v86, v87\n\t"
    "v_mfma_f32_16x16x32_bf16 a[120:123], a[252:255], v[40:43], a[120:123]\n\t"
    "v_exp_f32 v96, v96\n\t"
    "v_mfma_f32_16x16x32_bf16 a[116:119], a[252:255], v[24:27], a[116:119]\n\t"
    "s_add_u32 m0, s56, 26368\n\t"
    "ds_read_b64_tr_b16 a[246:247], v197 offset:20480\n\t"
    "v_fma_f32 v97, v97, s62, v214\n\t"
    "v_mfma_f32_16x16x32_bf16 a[112:115], a[252:255], v[8:11], a[112:115]\n\t"
    "s_waitcnt lgkmcnt(8)\n\t"
    "buffer_load_dwordx4 v201, s[36:39], s52 offen offset:3328 lds\n\t"
    "v_fma_f32 v98, v98, s62, v214\n\t"
    "v_mfma_f32_16x16x32_bf16 v[128:131], a[224:227], v[0:3], v[128:131]\n\t"
    "ds_read_b64_tr_b16 a[248:249], v198 offset:14336\n\t"
    "v_fma_f32 v99, v99, s62, v214\n\t"
    "v_mfma_f32_16x16x32_bf16 v[132:135], a[224:227], v[16:19], v[132:135]\n\t"
    "v_exp_f32 v97, v97\n\t"
    "v_mfma_f32_16x16x32_bf16 v[136:139], a[224:227], v[32:35], v[136:139]\n\t"
    "ds_read_b64_tr_b16 a[250:251], v198 offset:20480\n\t"
    "v_cvt_pk_bf16_f32 v96, v96, v97\n\t"
    "v_mfma_f32_16x16x32_bf16 v[140:143], a[224:227], v[48:51], v[140:143]\n\t"
    "v_exp_f32 v98, v98\n\t"
    "v_mfma_f32_16x16x32_bf16 v[156:159], a[228:231], v[48:51], v[156:159]\n\t"
    "ds_read_b64_tr_b16 a[252:253], v199 offset:14336\n\t"
    "v_fma_f32 v100, v100, s62, v214\n\t"
    "v_mfma_f32_16x16x32_bf16 v[152:155], a[228:231], v[32:35], v[152:155]\n\t"
    "v_exp_f32 v99, v99\n\t"
    "v_mfma_f32_16x16x32_bf16 v[148:151], a[228:231], v[16:19], v[148:151]\n\t"
    "ds_read_b64_tr_b16 a[254:255], v199 offset:20480\n\t"
    "v_cvt_pk_bf16_f32 v97, v98, v99\n\t"
    "v_mfma_f32_16x16x32_bf16 v[144:147], a[228:231], v[0:3], v[144:147]\n\t"
    "s_waitcnt lgkmcnt(8)\n\t"
    "v_exp_f32 v100, v100\n\t"
    "v_mfma_f32_16x16x32_bf16 v[160:163], a[232:235], v[0:3], v[160:163]\n\t"
    "v_fma_f32 v101, v101, s62, v214\n\t"
    "v_fma_f32 v102, v102, s62, v214\n\t"
    "v_mfma_f32_16x16x32_bf16 v[164:167], a[232:235], v[16:19], v[164:167]\n\t"
    "v_exp_f32 v101, v101\n\t"
    "v_mfma_f32_16x16x32_bf16 v[168:171], a[232:235], v[32:35], v[168:171]\n\t"
    "s_add_u32 s52, s52, 24576\n\t"
    "v_exp_f32 v102, v102\n\t"
    "v_mfma_f32_16x16x32_bf16 v[172:175], a[232:235], v[48:51], v[172:175]\n\t"
    "v_cvt_pk_bf16_f32 v98, v100, v101\n\t"
    "v_fma_f32 v103, v103, s62, v214\n\t"
    "v_mfma_f32_16x16x32_bf16 v[188:191], a[236:239], v[48:51], v[188:191]\n\t"
    "v_exp_f32 v103, v103\n\t"
    "v_mfma_f32_16x16x32_bf16 v[184:187], a[236:239], v[32:35], v[184:187]\n\t"
    "v_cvt_pk_bf16_f32 v99, v102, v103\n\t"
    "v_fma_f32 v112, v112, s62, v215\n\t"
    "v_mfma_f32_16x16x32_bf16 v[180:183], a[236:239], v[16:19], v[180:183]\n\t"
    "s_waitcnt lgkmcnt(0) vmcnt(20)\n\t"
    "v_exp_f32 v112, v112\n\t"
    "v_mfma_f32_16x16x32_bf16 v[176:179], a[236:239], v[0:3], v[176:179]\n\t"
    "v_fma_f32 v113, v113, s62, v215\n\t"
    "v_fma_f32 v114, v114, s62, v215\n\t"
    "v_mfma_f32_16x16x32_bf16 v[128:131], a[240:243], v[8:11], v[128:131]\n\t"
    "s_barrier\n\t"
    "ds_read_b128 a[224:227], v192 offset:0\n\t"
    "v_fma_f32 v115, v115, s62, v215\n\t"
    "v_mfma_f32_16x16x32_bf16 v[132:135], a[240:243], v[24:27], v[132:135]\n\t"
    "ds_read_b128 a[228:231], v192 offset:6144\n\t"
    "v_fma_f32 v116, v116, s62, v215\n\t"
    "v_mfma_f32_16x16x32_bf16 v[136:139], a[240:243], v[40:43], v[136:139]\n\t"
    "ds_read_b128 a[232:235], v192 offset:12288\n\t"
    "v_fma_f32 v117, v117, s62, v215\n\t"
    "v_mfma_f32_16x16x32_bf16 v[140:143], a[240:243], v[56:59], v[140:143]\n\t"
    "v_exp_f32 v113, v113\n\t"
    "v_mfma_f32_16x16x32_bf16 v[156:159], a[244:247], v[56:59], v[156:159]\n\t"
    "v_exp_f32 v114, v114\n\t"
    "v_mfma_f32_16x16x32_bf16 v[152:155], a[244:247], v[40:43], v[152:155]\n\t"
    "v_exp_f32 v115, v115\n\t"
    "v_mfma_f32_16x16x32_bf16 v[148:151], a[244:247], v[24:27], v[148:151]\n\t"
    "ds_read_b128 a[236:239], v192 offset:18432\n\t"
    "v_cvt_pk_bf16_f32 v112, v112, v113\n\t"
    "v_mfma_f32_16x16x32_bf16 v[144:147], a[244:247], v[8:11], v[144:147]\n\t"
    "v_exp_f32 v116, v116\n\t"
    "v_mfma_f32_16x16x32_bf16 v[160:163], a[248:251], v[8:11], v[160:163]\n\t"
    "v_exp_f32 v117, v117\n\t"
    "v_mfma_f32_16x16x32_bf16 v[164:167], a[248:251], v[24:27], v[164:167]\n\t"
    "v_cvt_pk_bf16_f32 v113, v114, v115\n\t"
    "v_cvt_pk_bf16_f32 v114, v116, v117\n\t"
    "v_mfma_f32_16x16x32_bf16 v[168:171], a[248:251], v[40:43], v[168:171]\n\t"
    "ds_read_b128 a[240:243], v193 offset:0\n\t"
    "v_fma_f32 v118, v118, s62, v215\n\t"
    "v_mfma_f32_16x16x32_bf16 v[172:175], a[248:251], v[56:59], v[172:175]\n\t"
    "v_exp_f32 v118, v118\n\t"
    "v_mfma_f32_16x16x32_bf16 v[188:191], a[252:255], v[56:59], v[188:191]\n\t"
    "s_add_u32 s54, s54, 25165824\n\t"
    "v_fma_f32 v119, v119, s62, v215\n\t"
    "v_fma_f32 v72, v72, s62, v212\n\t"
    "v_mfma_f32_16x16x32_bf16 v[184:187], a[252:255], v[40:43], v[184:187]\n\t"
    "v_exp_f32 v119, v119\n\t"
    "v_mfma_f32_16x16x32_bf16 v[180:183], a[252:255], v[24:27], v[180:183]\n\t"
    "ds_read_b128 a[244:247], v193 offset:6144\n\t"
    "v_cvt_pk_bf16_f32 v115, v118, v119\n\t"
    "v_mfma_f32_16x16x32_bf16 v[176:179], a[252:255], v[8:11], v[176:179]\n\t"
    "s_waitcnt lgkmcnt(4)\n\t"
    "v_exp_f32 v72, v72\n\t"
    "s_cmp_eq_u32 s69, 3\n\t"
    "s_cbranch_scc1 LAST7\n\t"
    "v_mfma_f32_16x16x32_bf16 v[0:3], a[224:227], a[128:131], v[224:227]\n\t"
    "s_cmp_lg_u32 s72, 0\n\t"
    "s_cbranch_scc1 SB7\n\t"
    "NSB7:\n\t"
    "v_fma_f32 v73, v73, s62, v212\n\t"
    "v_mfma_f32_16x16x32_bf16 v[16:19], a[224:227], a[132:135], v[224:227]\n\t"
    "v_exp_f32 v73, v73\n\t"
    "v_mfma_f32_16x16x32_bf16 v[32:35], a[224:227], a[136:139], v[224:227]\n\t"
    "s_add_u32 m0, s57, 0\n\t"
    "ds_read_b128 a[248:251], v193 offset:12288\n\t"
    "v_cvt_pk_bf16_f32 v72, v72, v73\n\t"
    "v_mfma_f32_16x16x32_bf16 v[48:51], a[224:227], a[140:143], v[224:227]\n\t"
    "buffer_load_dwordx4 v202, s[40:43], s53 offen offset:0 lds\n\t"
    "v_fma_f32 v74, v74, s62, v212\n\t"
    "v_mfma_f32_16x16x32_bf16 v[52:55], a[228:231], a[140:143], v[224:227]\n\t"
    "v_exp_f32 v74, v74\n\t"
    "v_mfma_f32_16x16x32_bf16 v[36:39], a[228:231], a[136:139], v[224:227]\n\t"
    "v_fma_f32 v75, v75, s62, v212\n\t"
    "v_fma_f32 v76, v76, s62, v212\n\t"
    "v_mfma_f32_16x16x32_bf16 v[20:23], a[228:231], a[132:135], v[224:227]\n\t"
    "ds_read_b128 a[252:255], v193 offset:18432\n\t"
    "v_fma_f32 v77, v77, s62, v212\n\t"
    "v_mfma_f32_16x16x32_bf16 v[4:7], a[228:231], a[128:131], v[224:227]\n\t"
    "s_waitcnt lgkmcnt(4)\n\t"
    "v_exp_f32 v75, v75\n\t"
    "v_mfma_f32_16x16x32_bf16 v[8:11], a[232:235], a[128:131], v[224:227]\n\t"
    "v_exp_f32 v76, v76\n\t"
    "v_mfma_f32_16x16x32_bf16 v[24:27], a[232:235], a[132:135], v[224:227]\n\t"
    "v_exp_f32 v77, v77\n\t"
    "v_mfma_f32_16x16x32_bf16 v[40:43], a[232:235], a[136:139], v[224:227]\n\t"
    "ds_read_b128 a[224:227], v192 offset:1024\n\t"
    "v_cvt_pk_bf16_f32 v73, v74, v75\n\t"
    "v_mfma_f32_16x16x32_bf16 v[56:59], a[232:235], a[140:143], v[224:227]\n\t"
    "v_cvt_pk_bf16_f32 v74, v76, v77\n\t"
    "v_fma_f32 v78, v78, s62, v212\n\t"
    "v_mfma_f32_16x16x32_bf16 v[60:63], a[236:239], a[140:143], v[224:227]\n\t"
    "v_exp_f32 v78, v78\n\t"
    "v_mfma_f32_16x16x32_bf16 v[44:47], a[236:239], a[136:139], v[224:227]\n\t"
    "v_fma_f32 v79, v79, s62, v212\n\t"
    "v_fma_f32 v88, v88, s62, v213\n\t"
    "v_mfma_f32_16x16x32_bf16 v[28:31], a[236:239], a[132:135], v[224:227]\n\t"
    "s_add_u32 m0, s57, 0\n\t"
    "ds_read_b128 a[228:231], v192 offset:7168\n\t"
    "v_fma_f32 v89, v89, s62, v213\n\t"
    "v_mfma_f32_16x16x32_bf16 v[12:15], a[236:239], a[128:131], v[224:227]\n\t"
    "s_waitcnt lgkmcnt(4)\n\t"
    "buffer_load_dwordx4 v202, s[40:43], s53 offen offset:3072 lds\n\t"
    "v_fma_f32 v90, v90, s62, v213\n\t"
    "v_mfma_f32_16x16x32_bf16 v[0:3], a[240:243], a[144:147], v[0:3]\n\t"
    "v_exp_f32 v79, v79\n\t"
    "v_mfma_f32_16x16x32_bf16 v[16:19], a[240:243], a[148:151], v[16:19]\n\t"
    "v_exp_f32 v88, v88\n\t"
    "v_mfma_f32_16x16x32_bf16 v[32:35], a[240:243], a[152:155], v[32:35]\n\t"
    "ds_read_b128 a[232:235], v192 offset:13312\n\t"
    "v_cvt_pk_bf16_f32 v75, v78, v79\n\t"
    "v_mfma_f32_16x16x32_bf16 v[48:51], a[240:243], a[156:159], v[48:51]\n\t"
    "v_exp_f32 v89, v89\n\t"
    "v_mfma_f32_16x16x32_bf16 v[52:55], a[244:247], a[156:159], v[52:55]\n\t"
    "v_exp_f32 v90, v90\n\t"
    "v_mfma_f32_16x16x32_bf16 v[36:39], a[244:247], a[152:155], v[36:39]\n\t"
    "v_cvt_pk_bf16_f32 v88, v88, v89\n\t"
    "v_fma_f32 v91, v91, s62, v213\n\t"
    "v_mfma_f32_16x16x32_bf16 v[20:23], a[244:247], a[148:151], v[20:23]\n\t"
    "ds_read_b128 a[236:239], v192 offset:19456\n\t"
    "v_fma_f32 v92, v92, s62, v213\n\t"
    "v_mfma_f32_16x16x32_bf16 v[4:7], a[244:247], a[144:147], v[4:7]\n\t"
    "s_waitcnt lgkmcnt(4)\n\t"
    "v_exp_f32 v91, v91\n\t"
    "v_mfma_f32_16x16x32_bf16 v[8:11], a[248:251], a[144:147], v[8:11]\n\t"
    "v_exp_f32 v92, v92\n\t"
    "v_mfma_f32_16x16x32_bf16 v[24:27], a[248:251], a[148:151], v[24:27]\n\t"
    "v_cvt_pk_bf16_f32 v89, v90, v91\n\t"
    "v_fma_f32 v93, v93, s62, v213\n\t"
    "v_mfma_f32_16x16x32_bf16 v[40:43], a[248:251], a[152:155], v[40:43]\n\t"
    "s_add_u32 m0, s57, 896\n\t"
    "ds_read_b128 a[240:243], v193 offset:1024\n\t"
    "v_fma_f32 v94, v94, s62, v213\n\t"
    "v_mfma_f32_16x16x32_bf16 v[56:59], a[248:251], a[156:159], v[56:59]\n\t"
    "buffer_load_dwordx4 v202, s[40:43], s53 offen offset:128 lds\n\t"
    "v_fma_f32 v95, v95, s62, v213\n\t"
    "v_mfma_f32_16x16x32_bf16 v[60:63], a[252:255], a[156:159], v[60:63]\n\t"
    "v_exp_f32 v93, v93\n\t"
    "v_mfma_f32_16x16x32_bf16 v[44:47], a[252:255], a[152:155], v[44:47]\n\t"
    "v_exp_f32 v94, v94\n\t"
    "v_mfma_f32_16x16x32_bf16 v[28:31], a[252:255], a[148:151], v[28:31]\n\t"
    "ds_read_b128 a[244:247], v193 offset:7168\n\t"
    "v_cvt_pk_bf16_f32 v90, v92, v93\n\t"
    "v_mfma_f32_16x16x32_bf16 v[12:15], a[252:255], a[144:147], v[12:15]\n\t"
    "s_waitcnt lgkmcnt(4)\n\t"
    "v_exp_f32 v95, v95\n\t"
    "v_mfma_f32_16x16x32_bf16 v[0:3], a[224:227], a[160:163], v[0:3]\n\t"
    "v_cvt_pk_bf16_f32 v91, v94, v95\n\t"
    "v_fma_f32 v104, v104, s62, v214\n\t"
    "v_mfma_f32_16x16x32_bf16 v[16:19], a[224:227], a[164:167], v[16:19]\n\t"
    "v_exp_f32 v104, v104\n\t"
    "v_mfma_f32_16x16x32_bf16 v[32:35], a[224:227], a[168:171], v[32:35]\n\t"
    "ds_read_b128 a[248:251], v193 offset:13312\n\t"
    "v_fma_f32 v105, v105, s62, v214\n\t"
    "v_mfma_f32_16x16x32_bf16 v[48:51], a[224:227], a[172:175], v[48:51]\n\t"
    "v_exp_f32 v105, v105\n\t"
    "v_mfma_f32_16x16x32_bf16 v[52:55], a[228:231], a[172:175], v[52:55]\n\t"
    "v_cvt_pk_bf16_f32 v104, v104, v105\n\t"
    "v_fma_f32 v106, v106, s62, v214\n\t"
    "v_mfma_f32_16x16x32_bf16 v[36:39], a[228:231], a[168:171], v[36:39]\n\t"
    "v_exp_f32 v106, v106\n\t"
    "v_mfma_f32_16x16x32_bf16 v[20:23], a[228:231], a[164:167], v[20:23]\n\t"
    "s_add_u32 m0, s57, 896\n\t"
    "ds_read_b128 a[252:255], v193 offset:19456\n\t"
    "v_fma_f32 v107, v107, s62, v214\n\t"
    "v_mfma_f32_16x16x32_bf16 v[4:7], a[228:231], a[160:163], v[4:7]\n\t"
    "s_waitcnt lgkmcnt(4)\n\t"
    "buffer_load_dwordx4 v202, s[40:43], s53 offen offset:3200 lds\n\t"
    "v_fma_f32 v108, v108, s62, v214\n\t"
    "v_mfma_f32_16x16x32_bf16 v[8:11], a[232:235], a[160:163], v[8:11]\n\t"
    "s_waitcnt vmcnt(9)\n\t"
    "v_exp_f32 v107, v107\n\t"
    "v_mfma_f32_16x16x32_bf16 v[24:27], a[232:235], a[164:167], v[24:27]\n\t"
    "v_exp_f32 v108, v108\n\t"
    "v_mfma_f32_16x16x32_bf16 v[40:43], a[232:235], a[168:171], v[40:43]\n\t"
    "ds_read_b128 a[224:227], v192 offset:2048\n\t"
    "v_cvt_pk_bf16_f32 v105, v106, v107\n\t"
    "v_mfma_f32_16x16x32_bf16 v[56:59], a[232:235], a[172:175], v[56:59]\n\t"
    "ds_read_b128 a[192:195], v204 offset:0\n\t"
    "v_fma_f32 v109, v109, s62, v214\n\t"
    "v_mfma_f32_16x16x32_bf16 v[60:63], a[236:239], a[172:175], v[60:63]\n\t"
    "ds_read_b128 a[196:199], v204 offset:2048\n\t"
    "v_fma_f32 v110, v110, s62, v214\n\t"
    "v_mfma_f32_16x16x32_bf16 v[44:47], a[236:239], a[168:171], v[44:47]\n\t"
    "v_exp_f32 v109, v109\n\t"
    "v_mfma_f32_16x16x32_bf16 v[28:31], a[236:239], a[164:167], v[28:31]\n\t"
    "ds_read_b128 a[228:231], v192 offset:8192\n\t"
    "v_cvt_pk_bf16_f32 v106, v108, v109\n\t"
    "v_mfma_f32_16x16x32_bf16 v[12:15], a[236:239], a[160:163], v[12:15]\n\t"
    "s_waitcnt lgkmcnt(6)\n\t"
    "ds_read_b128 a[200:203], v204 offset:4096\n\t"
    "v_fma_f32 v111, v111, s62, v214\n\t"
    "v_mfma_f32_16x16x32_bf16 v[0:3], a[240:243], a[176:179], v[0:3]\n\t"
    "ds_read_b128 a[204:207], v204 offset:6144\n\t"
    "v_fma_f32 v120, v120, s62, v215\n\t"
    "v_mfma_f32_16x16x32_bf16 v[16:19], a[240:243], a[180:183], v[16:19]\n\t"
    "v_exp_f32 v110, v110\n\t"
    "v_mfma_f32_16x16x32_bf16 v[32:35], a[240:243], a[184:187], v[32:35]\n\t"
    "s_add_u32 m0, s57, 1792\n\t"
    "ds_read_b128 a[232:235], v192 offset:14336\n\t"
    "v_fma_f32 v121, v121, s62, v215\n\t"
    "v_mfma_f32_16x16x32_bf16 v[48:51], a[240:243], a[188:191], v[48:51]\n\t"
    "buffer_load_dwordx4 v202, s[40:43], s53 offen offset:256 lds\n\t"
    "v_fma_f32 v122, v122, s62, v215\n\t"
    "v_mfma_f32_16x16x32_bf16 v[52:55], a[244:247], a[188:191], v[52:55]\n\t"
    "ds_read_b128 a[208:211], v205 offset:0\n\t"
    "v_fma_f32 v123, v123, s62, v215\n\t"
    "v_mfma_f32_16x16x32_bf16 v[36:39], a[244:247], a[184:187], v[36:39]\n\t"
    "ds_read_b128 a[212:215], v205 offset:2048\n\t"
    "v_fma_f32 v124, v124, s62, v215\n\t"
    "v_mfma_f32_16x16x32_bf16 v[20:23], a[244:247], a[180:183], v[20:23]\n\t"
    "ds_read_b128 a[236:239], v192 offset:20480\n\t"
    "v_mfma_f32_16x16x32_bf16 v[4:7], a[244:247], a[176:179], v[4:7]\n\t"
    "s_waitcnt lgkmcnt(10)\n\t"
    "ds_read_b128 a[216:219], v205 offset:4096\n\t"
    "v_mfma_f32_16x16x32_bf16 v[8:11], a[248:251], a[176:179], v[8:11]\n\t"
    "ds_read_b128 a[220:223], v205 offset:6144\n\t"
    "v_mfma_f32_16x16x32_bf16 v[24:27], a[248:251], a[180:183], v[24:27]\n\t"
    "v_exp_f32 v111, v111\n\t"
    "v_mfma_f32_16x16x32_bf16 v[40:43], a[248:251], a[184:187], v[40:43]\n\t"
    "ds_read_b128 a[240:243], v193 offset:2048\n\t"
    "v_cvt_pk_bf16_f32 v107, v110, v111\n\t"
    "v_mfma_f32_16x16x32_bf16 v[56:59], a[248:251], a[188:191], v[56:59]\n\t"
    "v_exp_f32 v120, v120\n\t"
    "v_mfma_f32_16x16x32_bf16 v[60:63], a[252:255], a[188:191], v[60:63]\n\t"
    "v_exp_f32 v121, v121\n\t"
    "v_mfma_f32_16x16x32_bf16 v[44:47], a[252:255], a[184:187], v[44:47]\n\t"
    "v_exp_f32 v122, v122\n\t"
    "v_mfma_f32_16x16x32_bf16 v[28:31], a[252:255], a[180:183], v[28:31]\n\t"
    "s_add_u32 m0, s57, 1792\n\t"
    "ds_read_b128 a[244:247], v193 offset:8192\n\t"
    "v_cvt_pk_bf16_f32 v120, v120, v121\n\t"
    "v_mfma_f32_16x16x32_bf16 v[12:15], a[252:255], a[176:179], v[12:15]\n\t"
    "s_waitcnt lgkmcnt(10)\n\t"
    "s_waitcnt lgkmcnt(2)\n\t"
    "buffer_load_dwordx4 v202, s[40:43], s53 offen offset:3328 lds\n\t"
    "v_fma_f32 v125, v125, s62, v215\n\t"
    "v_mfma_f32_16x16x32_bf16 v[0:3], a[224:227], a[192:195], v[0:3]\n\t"
    "v_exp_f32 v123, v123\n\t"
    "v_mfma_f32_16x16x32_bf16 v[16:19], a[224:227], a[196:199], v[16:19]\n\t"
    "v_exp_f32 v124, v124\n\t"
    "v_mfma_f32_16x16x32_bf16 v[32:35], a[224:227], a[200:203], v[32:35]\n\t"
    "ds_read_b128 a[248:251], v193 offset:14336\n\t"
    "v_cvt_pk_bf16_f32 v121, v122, v123\n\t"
    "v_mfma_f32_16x16x32_bf16 v[48:51], a[224:227], a[204:207], v[48:51]\n\t"
    "v_exp_f32 v125, v125\n\t"
    "v_mfma_f32_16x16x32_bf16 v[52:55], a[228:231], a[204:207], v[52:55]\n\t"
    "v_cvt_pk_bf16_f32 v122, v124, v125\n\t"
    "v_fma_f32 v126, v126, s62, v215\n\t"
    "v_mfma_f32_16x16x32_bf16 v[36:39], a[228:231], a[200:203], v[36:39]\n\t"
    "v_exp_f32 v126, v126\n\t"
    "v_mfma_f32_16x16x32_bf16 v[20:23], a[228:231], a[196:199], v[20:23]\n\t"
    "ds_read_b128 a[252:255], v193 offset:20480\n\t"
    "v_fma_f32 v127, v127, s62, v215\n\t"
    "v_mfma_f32_16x16x32_bf16 v[4:7], a[228:231], a[192:195], v[4:7]\n\t"
    "v_exp_f32 v127, v127\n\t"
    "v_mfma_f32_16x16x32_bf16 v[8:11], a[232:235], a[192:195], v[8:11]\n\t"
    "v_cvt_pk_bf16_f32 v123, v126, v127\n\t"
    "v_mfma_f32_16x16x32_bf16 v[24:27], a[232:235], a[196:199], v[24:27]\n\t"
    "v_mfma_f32_16x16x32_bf16 v[40:43], a[232:235], a[200:203], v[40:43]\n\t"
    "s_add_u32 s53, s53, 24576\n\t"
    "v_mfma_f32_16x16x32_bf16 v[56:59], a[232:235], a[204:207], v[56:59]\n\t"
    "v_mfma_f32_16x16x32_bf16 v[60:63], a[236:239], a[204:207], v[60:63]\n\t"
    "v_mfma_f32_16x16x32_bf16 v[44:47], a[236:239], a[200:203], v[44:47]\n\t"
    "v_mfma_f32_16x16x32_bf16 v[28:31], a[236:239], a[196:199], v[28:31]\n\t"
    "s_waitcnt lgkmcnt(0)\n\t"
    "v_mfma_f32_16x16x32_bf16 v[12:15], a[236:239], a[192:195], v[12:15]\n\t"
    "v_mfma_f32_16x16x32_bf16 v[0:3], a[240:243], a[208:211], v[0:3]\n\t"
    "s_barrier\n\t"
    "ds_read_b64_tr_b16 a[224:225], v196 offset:24576\n\t"
    "v_mfma_f32_16x16x32_bf16 v[16:19], a[240:243], a[212:215], v[16:19]\n\t"
    "ds_read_b64_tr_b16 a[226:227], v196 offset:30720\n\t"
    "v_mfma_f32_16x16x32_bf16 v[32:35], a[240:243], a[216:219], v[32:35]\n\t"
    "ds_read_b64_tr_b16 a[228:229], v197 offset:24576\n\t"
    "v_mfma_f32_16x16x32_bf16 v[48:51], a[240:243], a[220:223], v[48:51]\n\t"
    "ds_read_b64_tr_b16 a[230:231], v197 offset:30720\n\t"
    "v_mfma_f32_16x16x32_bf16 v[52:55], a[244:247], a[220:223], v[52:55]\n\t"
    "ds_read_b64_tr_b16 a[232:233], v198 offset:24576\n\t"
    "v_mfma_f32_16x16x32_bf16 v[36:39], a[244:247], a[216:219], v[36:39]\n\t"
    "ds_read_b64_tr_b16 a[234:235], v198 offset:30720\n\t"
    "v_mfma_f32_16x16x32_bf16 v[20:23], a[244:247], a[212:215], v[20:23]\n\t"
    "ds_read_b64_tr_b16 a[236:237], v199 offset:24576\n\t"
    "v_mfma_f32_16x16x32_bf16 v[4:7], a[244:247], a[208:211], v[4:7]\n\t"
    "ds_read_b64_tr_b16 a[238:239], v199 offset:30720\n\t"
    "v_mfma_f32_16x16x32_bf16 v[8:11], a[248:251], a[208:211], v[8:11]\n\t"
    "ds_read_b64_tr_b16 a[240:241], v196 offset:36864\n\t"
    "v_mfma_f32_16x16x32_bf16 v[24:27], a[248:251], a[212:215], v[24:27]\n\t"
    "v_mfma_f32_16x16x32_bf16 v[40:43], a[248:251], a[216:219], v[40:43]\n\t"
    "ds_read_b64_tr_b16 a[242:243], v196 offset:43008\n\t"
    "v_mfma_f32_16x16x32_bf16 v[56:59], a[248:251], a[220:223], v[56:59]\n\t"
    "v_mfma_f32_16x16x32_bf16 v[60:63], a[252:255], a[220:223], v[60:63]\n\t"
    "ds_read_b64_tr_b16 a[244:245], v197 offset:36864\n\t"
    "v_mfma_f32_16x16x32_bf16 v[44:47], a[252:255], a[216:219], v[44:47]\n\t"
    "v_mfma_f32_16x16x32_bf16 v[28:31], a[252:255], a[212:215], v[28:31]\n\t"
    "ds_read_b64_tr_b16 a[246:247], v197 offset:43008\n\t"
    "v_mfma_f32_16x16x32_bf16 v[12:15], a[252:255], a[208:211], v[12:15]\n\t"
    "s_waitcnt lgkmcnt(8)\n\t"
    "v_mfma_f32_16x16x32_bf16 a[0:3], a[224:227], v[64:67], a[0:3]\n\t"
    "ds_read_b64_tr_b16 a[248:249], v198 offset:36864\n\t"
    "v_mfma_f32_16x16x32_bf16 a[4:7], a[224:227], v[80:83], a[4:7]\n\t"
    "v_mfma_f32_16x16x32_bf16 a[8:11], a[224:227], v[96:99], a[8:11]\n\t"
    "s_add_u32 m0, s56, 0\n\t"
    "ds_read_b64_tr_b16 a[250:251], v198 offset:43008\n\t"
    "v_mfma_f32_16x16x32_bf16 a[12:15], a[224:227], v[112:115], a[12:15]\n\t"
    "buffer_load_dwordx4 v200, s[36:39], s52 offen offset:0 lds\n\t"
    "v_mfma_f32_16x16x32_bf16 a[28:31], a[228:231], v[112:115], a[28:31]\n\t"
    "ds_read_b64_tr_b16 a[252:253], v199 offset:36864\n\t"
    "v_max3_f32 v220, v0, v1, v2\n\t"
    "v_mfma_f32_16x16x32_bf16 a[24:27], a[228:231], v[96:99], a[24:27]\n\t"
    "v_max3_f32 v221, v16, v17, v18\n\t"
    "v_max3_f32 v222, v32, v33, v34\n\t"
    "v_mfma_f32_16x16x32_bf16 a[20:23], a[228:231], v[80:83], a[20:23]\n\t"
    "ds_read_b64_tr_b16 a[254:255], v199 offset:43008\n\t"
    "v_max3_f32 v223, v48, v49, v50\n\t"
    "v_mfma_f32_16x16x32_bf16 a[16:19], a[228:231], v[64:67], a[16:19]\n\t"
    "s_waitcnt lgkmcnt(8)\n\t"
    "v_max3_f32 v220, v220, v3, v4\n\t"
    "v_max3_f32 v221, v221, v19, v20\n\t"
    "v_mfma_f32_16x16x32_bf16 a[32:35], a[232:235], v[64:67], a[32:35]\n\t"
    "ds_read_b64_tr_b16 a[224:225], v196 offset:25600\n\t"
    "v_max3_f32 v222, v222, v35, v36\n\t"
    "v_mfma_f32_16x16x32_bf16 a[36:39], a[232:235], v[80:83], a[36:39]\n\t"
    "v_max3_f32 v223, v223, v51, v52\n\t"
    "v_max3_f32 v220, v220, v5, v6\n\t"
    "v_mfma_f32_16x16x32_bf16 v[228:231], v[244:247], v[64:67], v[228:231]\n\t"
    "v_max3_f32 v221, v221, v21, v22\n\t"
    "v_max3_f32 v222, v222, v37, v38\n\t"
    "v_mfma_f32_16x16x32_bf16 a[40:43], a[232:235], v[96:99], a[40:43]\n\t"
    "ds_read_b64_tr_b16 a[226:227], v196 offset:31744\n\t"
    "v_max3_f32 v223, v223, v53, v54\n\t"
    "v_mfma_f32_16x16x32_bf16 a[44:47], a[232:235], v[112:115], a[44:47]\n\t"
    "v_max3_f32 v220, v220, v7, v8\n\t"
    "v_max3_f32 v221, v221, v23, v24\n\t"
    "v_mfma_f32_16x16x32_bf16 a[60:63], a[236:239], v[112:115], a[60:63]\n\t"
    "ds_read_b64_tr_b16 a[228:229], v197 offset:25600\n\t"
    "v_max3_f32 v222, v222, v39, v40\n\t"
    "v_mfma_f32_16x16x32_bf16 a[56:59], a[236:239], v[96:99], a[56:59]\n\t"
    "v_max3_f32 v223, v223, v55, v56\n\t"
    "v_max3_f32 v220, v220, v9, v10\n\t"
    "v_mfma_f32_16x16x32_bf16 v[232:235], v[244:247], v[80:83], v[232:235]\n\t"
    "v_max3_f32 v221, v221, v25, v26\n\t"
    "v_max3_f32 v222, v222, v41, v42\n\t"
    "v_mfma_f32_16x16x32_bf16 a[52:55], a[236:239], v[80:83], a[52:55]\n\t"
    "s_add_u32 m0, s56, 0\n\t"
    "ds_read_b64_tr_b16 a[230:231], v197 offset:31744\n\t"
    "v_max3_f32 v223, v223, v57, v58\n\t"
    "v_mfma_f32_16x16x32_bf16 a[48:51], a[236:239], v[64:67], a[48:51]\n\t"
    "s_waitcnt lgkmcnt(8)\n\t"
    "buffer_load_dwordx4 v201, s[36:39], s52 offen offset:3072 lds\n\t"
    "v_max3_f32 v220, v220, v11, v12\n\t"
    "v_mfma_f32_16x16x32_bf16 a[0:3], a[240:243], v[72:75], a[0:3]\n\t"
    "ds_read_b64_tr_b16 a[232:233], v198 offset:25600\n\t"
    "v_max3_f32 v221, v221, v27, v28\n\t"
    "v_mfma_f32_16x16x32_bf16 a[4:7], a[240:243], v[88:91], a[4:7]\n\t"
    "v_max3_f32 v222, v222, v43, v44\n\t"
    "v_max3_f32 v223, v223, v59, v60\n\t"
    "v_mfma_f32_16x16x32_bf16 v[236:239], v[244:247], v[96:99], v[236:239]\n\t"
    "v_max3_f32 v220, v220, v13, v14\n\t"
    "v_max3_f32 v221, v221, v29, v30\n\t"
    "v_mfma_f32_16x16x32_bf16 a[8:11], a[240:243], v[104:107], a[8:11]\n\t"
    "ds_read_b64_tr_b16 a[234:235], v198 offset:31744\n\t"
    "v_max3_f32 v222, v222, v45, v46\n\t"
    "v_mfma_f32_16x16x32_bf16 a[12:15], a[240:243], v[120:123], a[12:15]\n\t"
    "v_max3_f32 v223, v223, v61, v62\n\t"
    "v_max_f32 v220, v220, v15\n\t"
    "v_mfma_f32_16x16x32_bf16 a[28:31], a[244:247], v[120:123], a[28:31]\n\t"
    "ds_read_b64_tr_b16 a[236:237], v199 offset:25600\n\t"
    "v_max_f32 v221, v221, v31\n\t"
    "v_mfma_f32_16x16x32_bf16 a[24:27], a[244:247], v[104:107], a[24:27]\n\t"
    "v_max_f32 v222, v222, v47\n\t"
    "v_max_f32 v223, v223, v63\n\t"
    "v_mfma_f32_16x16x32_bf16 v[240:243], v[244:247], v[112:115], v[240:243]\n\t"
    "v_mov_b32 v252, v220\n\t"
    "v_mov_b32 v253, v221\n\t"
    "v_mfma_f32_16x16x32_bf16 a[20:23], a[244:247], v[88:91], a[20:23]\n\t"
    "ds_read_b64_tr_b16 a[238:239], v199 offset:31744\n\t"
    "v_mov_b32 v254, v222\n\t"
    "v_mfma_f32_16x16x32_bf16 a[16:19], a[244:247], v[72:75], a[16:19]\n\t"
    "s_waitcnt lgkmcnt(8)\n\t"
    "v_mov_b32 v255, v223\n\t"
    "v_permlane32_swap_b32 v220, v252\n\t"
    "v_mfma_f32_16x16x32_bf16 a[32:35], a[248:251], v[72:75], a[32:35]\n\t"
    "ds_read_b64_tr_b16 a[240:241], v196 offset:37888\n\t"
    "v_permlane32_swap_b32 v221, v253\n\t"
    "v_mfma_f32_16x16x32_bf16 a[36:39], a[248:251], v[88:91], a[36:39]\n\t"
    "v_permlane32_swap_b32 v222, v254\n\t"
    "v_permlane32_swap_b32 v223, v255\n\t"
    "v_mfma_f32_16x16x32_bf16 a[40:43], a[248:251], v[104:107], a[40:43]\n\t"
    "s_add_u32 m0, s56, 896\n\t"
    "ds_read_b64_tr_b16 a[242:243], v196 offset:44032\n\t"
    "v_max_f32 v220, v220, v252\n\t"
    "v_mfma_f32_16x16x32_bf16 a[44:47], a[248:251], v[120:123], a[44:47]\n\t"
    "buffer_load_dwordx4 v200, s[36:39], s52 offen offset:128 lds\n\t"
    "v_max_f32 v221, v221, v253\n\t"
    "v_mfma_f32_16x16x32_bf16 a[60:63], a[252:255], v[120:123], a[60:63]\n\t"
    "ds_read_b64_tr_b16 a[244:245], v197 offset:37888\n\t"
    "v_max_f32 v222, v222, v254\n\t"
    "v_mfma_f32_16x16x32_bf16 a[56:59], a[252:255], v[104:107], a[56:59]\n\t"
    "v_max_f32 v223, v223, v255\n\t"
    "v_mov_b32 v252, v220\n\t"
    "v_mfma_f32_16x16x32_bf16 v[228:231], v[244:247], v[72:75], v[228:231]\n\t"
    "v_mov_b32 v253, v221\n\t"
    "v_mov_b32 v254, v222\n\t"
    "v_mfma_f32_16x16x32_bf16 a[52:55], a[252:255], v[88:91], a[52:55]\n\t"
    "ds_read_b64_tr_b16 a[246:247], v197 offset:44032\n\t"
    "v_mov_b32 v255, v223\n\t"
    "v_mfma_f32_16x16x32_bf16 a[48:51], a[252:255], v[72:75], a[48:51]\n\t"
    "s_waitcnt lgkmcnt(8)\n\t"
    "v_permlane16_swap_b32 v220, v252\n\t"
    "v_permlane16_swap_b32 v221, v253\n\t"
    "v_mfma_f32_16x16x32_bf16 a[64:67], a[224:227], v[64:67], a[64:67]\n\t"
    "ds_read_b64_tr_b16 a[248:249], v198 offset:37888\n\t"
    "v_permlane16_swap_b32 v222, v254\n\t"
    "v_mfma_f32_16x16x32_bf16 a[68:71], a[224:227], v[80:83], a[68:71]\n\t"
    "v_permlane16_swap_b32 v223, v255\n\t"
    "v_max_f32 v220, v220, v252\n\t"
    "v_mfma_f32_16x16x32_bf16 v[232:235], v[244:247], v[88:91], v[232:235]\n\t"
    "v_max_f32 v221, v221, v253\n\t"
    "v_max_f32 v222, v222, v254\n\t"
    "v_mfma_f32_16x16x32_bf16 a[72:75], a[224:227], v[96:99], a[72:75]\n\t"
    "ds_read_b64_tr_b16 a[250:251], v198 offset:44032\n\t"
    "v_max_f32 v223, v223, v255\n\t"
    "v_mfma_f32_16x16x32_bf16 a[76:79], a[224:227], v[112:115], a[76:79]\n\t"
    "v_add_f32 v216, s63, v220\n\t"
    "v_add_f32 v217, s63, v221\n\t"
    "v_mfma_f32_16x16x32_bf16 a[92:95], a[228:231], v[112:115], a[92:95]\n\t"
    "ds_read_b64_tr_b16 a[252:253], v199 offset:37888\n\t"
    "v_add_f32 v218, s63, v222\n\t"
    "v_mfma_f32_16x16x32_bf16 a[88:91], a[228:231], v[96:99], a[88:91]\n\t"
    "v_add_f32 v219, s63, v223\n\t"
    "v_mul_f32 v212, s62, v216\n\t"
    "v_mfma_f32_16x16x32_bf16 v[236:239], v[244:247], v[104:107], v[236:239]\n\t"
    "v_mul_f32 v213, s62, v217\n\t"
    "v_mul_f32 v214, s62, v218\n\t"
    "v_mfma_f32_16x16x32_bf16 a[84:87], a[228:231], v[80:83], a[84:87]\n\t"
    "s_add_u32 m0, s56, 896\n\t"
    "ds_read_b64_tr_b16 a[254:255], v199 offset:44032\n\t"
    "v_mul_f32 v215, s62, v219\n\t"
    "v_mfma_f32_16x16x32_bf16 a[80:83], a[228:231], v[64:67], a[80:83]\n\t"
    "s_waitcnt lgkmcnt(8)\n\t"
    "buffer_load_dwordx4 v201, s[36:39], s52 offen offset:3200 lds\n\t"
    "v_sub_f32 v212, 0x41000000, v212\n\t"
    "v_mfma_f32_16x16x32_bf16 a[96:99], a[232:235], v[64:67], a[96:99]\n\t"
    "ds_read_b64_tr_b16 a[224:225], v196 offset:26624\n\t"
    "v_sub_f32 v213, 0x41000000, v213\n\t"
    "v_mfma_f32_16x16x32_bf16 a[100:103], a[232:235], v[80:83], a[100:103]\n\t"
    "v_sub_f32 v214, 0x41000000, v214\n\t"
    "v_sub_f32 v215, 0x41000000, v215\n\t"
    "v_mfma_f32_16x16x32_bf16 v[240:243], v[244:247], v[120:123], v[240:243]\n\t"
    "v_fma_f32 v0, v0, s62, v212\n\t"
    "v_fma_f32 v1, v1, s62, v212\n\t"
    "v_mfma_f32_16x16x32_bf16 a[104:107], a[232:235], v[96:99], a[104:107]\n\t"
    "ds_read_b64_tr_b16 a[226:227], v196 offset:32768\n\t"
    "v_fma_f32 v2, v2, s62, v212\n\t"
    "v_mfma_f32_16x16x32_bf16 a[108:111], a[232:235], v[112:115], a[108:111]\n\t"
    "v_exp_f32 v0, v0\n\t"
    "v_mfma_f32_16x16x32_bf16 a[124:127], a[236:239], v[112:115], a[124:127]\n\t"
    "ds_read_b64_tr_b16 a[228:229], v197 offset:26624\n\t"
    "v_fma_f32 v3, v3, s62, v212\n\t"
    "v_mfma_f32_16x16x32_bf16 a[120:123], a[236:239], v[96:99], a[120:123]\n\t"
    "v_exp_f32 v1, v1\n\t"
    "v_mfma_f32_16x16x32_bf16 a[116:119], a[236:239], v[80:83], a[116:119]\n\t"
    "ds_read_b64_tr_b16 a[230:231], v197 offset:32768\n\t"
    "v_cvt_pk_bf16_f32 v0, v0, v1\n\t"
    "v_mfma_f32_16x16x32_bf16 a[112:115], a[236:239], v[64:67], a[112:115]\n\t"
    "s_waitcnt lgkmcnt(8)\n\t"
    "v_exp_f32 v2, v2\n\t"
    "v_mfma_f32_16x16x32_bf16 a[64:67], a[240:243], v[72:75], a[64:67]\n\t"
    "ds_read_b64_tr_b16 a[232:233], v198 offset:26624\n\t"
    "v_fma_f32 v4, v4, s62, v212\n\t"
    "v_mfma_f32_16x16x32_bf16 a[68:71], a[240:243], v[88:91], a[68:71]\n\t"
    "v_exp_f32 v3, v3\n\t"
    "v_mfma_f32_16x16x32_bf16 a[72:75], a[240:243], v[104:107], a[72:75]\n\t"
    "s_add_u32 m0, s56, 1792\n\t"
    "ds_read_b64_tr_b16 a[234:235], v198 offset:32768\n\t"
    "v_cvt_pk_bf16_f32 v1, v2, v3\n\t"
    "v_mfma_f32_16x16x32_bf16 a[76:79], a[240:243], v[120:123], a[76:79]\n\t"
    "buffer_load_dwordx4 v200, s[36:39], s52 offen offset:256 lds\n\t"
    "v_fma_f32 v5, v5, s62, v212\n\t"
    "v_mfma_f32_16x16x32_bf16 a[92:95], a[244:247], v[120:123], a[92:95]\n\t"
    "ds_read_b64_tr_b16 a[236:237], v199 offset:26624\n\t"
    "v_fma_f32 v6, v6, s62, v212\n\t"
    "v_mfma_f32_16x16x32_bf16 a[88:91], a[244:247], v[104:107], a[88:91]\n\t"
    "v_exp_f32 v4, v4\n\t"
    "v_mfma_f32_16x16x32_bf16 a[84:87], a[244:247], v[88:91], a[84:87]\n\t"
    "ds_read_b64_tr_b16 a[238:239], v199 offset:32768\n\t"
    "v_fma_f32 v7, v7, s62, v212\n\t"
    "v_mfma_f32_16x16x32_bf16 a[80:83], a[244:247], v[72:75], a[80:83]\n\t"
    "s_waitcnt lgkmcnt(8)\n\t"
    "v_exp_f32 v5, v5\n\t"
    "v_mfma_f32_16x16x32_bf16 a[96:99], a[248:251], v[72:75], a[96:99]\n\t"
    "ds_read_b64_tr_b16 a[240:241], v196 offset:38912\n\t"
    "v_cvt_pk_bf16_f32 v2, v4, v5\n\t"
    "v_mfma_f32_16x16x32_bf16 a[100:103], a[248:251], v[88:91], a[100:103]\n\t"
    "v_exp_f32 v6, v6\n\t"
    "v_mfma_f32_16x16x32_bf16 a[104:107], a[248:251], v[104:107], a[104:107]\n\t"
    "ds_read_b64_tr_b16 a[242:243], v196 offset:45056\n\t"
    "v_fma_f32 v16, v16, s62, v213\n\t"
    "v_mfma_f32_16x16x32_bf16 a[108:111], a[248:251], v[120:123], a[108:111]\n\t"
    "v_exp_f32 v7, v7\n\t"
    "v_mfma_f32_16x16x32_bf16 a[124:127], a[252:255], v[120:123], a[124:127]\n\t"
    "ds_read_b64_tr_b16 a[244:245], v197 offset:38912\n\t"
    "v_cvt_pk_bf16_f32 v3, v6, v7\n\t"
    "v_mfma_f32_16x16x32_bf16 a[120:123], a[252:255], v[104:107], a[120:123]\n\t"
    "v_exp_f32 v16, v16\n\t"
    "v_mfma_f32_16x16x32_bf16 a[116:119], a[252:255], v[88:91], a[116:119]\n\t"
    "s_add_u32 m0, s56, 1792\n\t"
    "ds_read_b64_tr_b16 a[246:247], v197 offset:45056\n\t"
    "v_fma_f32 v17, v17, s62, v213\n\t"
    "v_mfma_f32_16x16x32_bf16 a[112:115], a[252:255], v[72:75], a[112:115]\n\t"
    "s_waitcnt lgkmcnt(8)\n\t"
    "buffer_load_dwordx4 v201, s[36:39], s52 offen offset:3328 lds\n\t"
    "v_fma_f32 v18, v18, s62, v213\n\t"
    "v_mfma_f32_16x16x32_bf16 v[128:131], a[224:227], v[64:67], v[128:131]\n\t"
    "ds_read_b64_tr_b16 a[248:249], v198 offset:38912\n\t"
    "v_fma_f32 v19, v19, s62, v213\n\t"
    "v_mfma_f32_16x16x32_bf16 v[132:135], a[224:227], v[80:83], v[132:135]\n\t"
    "v_exp_f32 v17, v17\n\t"
    "v_mfma_f32_16x16x32_bf16 v[136:139], a[224:227], v[96:99], v[136:139]\n\t"
    "ds_read_b64_tr_b16 a[250:251], v198 offset:45056\n\t"
    "v_cvt_pk_bf16_f32 v16, v16, v17\n\t"
    "v_mfma_f32_16x16x32_bf16 v[140:143], a[224:227], v[112:115], v[140:143]\n\t"
    "v_exp_f32 v18, v18\n\t"
    "v_mfma_f32_16x16x32_bf16 v[156:159], a[228:231], v[112:115], v[156:159]\n\t"
    "ds_read_b64_tr_b16 a[252:253], v199 offset:38912\n\t"
    "v_fma_f32 v20, v20, s62, v213\n\t"
    "v_mfma_f32_16x16x32_bf16 v[152:155], a[228:231], v[96:99], v[152:155]\n\t"
    "v_exp_f32 v19, v19\n\t"
    "v_mfma_f32_16x16x32_bf16 v[148:151], a[228:231], v[80:83], v[148:151]\n\t"
    "ds_read_b64_tr_b16 a[254:255], v199 offset:45056\n\t"
    "v_cvt_pk_bf16_f32 v17, v18, v19\n\t"
    "v_mfma_f32_16x16x32_bf16 v[144:147], a[228:231], v[64:67], v[144:147]\n\t"
    "s_waitcnt lgkmcnt(8)\n\t"
    "v_exp_f32 v20, v20\n\t"
    "v_mfma_f32_16x16x32_bf16 v[160:163], a[232:235], v[64:67], v[160:163]\n\t"
    "v_fma_f32 v21, v21, s62, v213\n\t"
    "v_fma_f32 v22, v22, s62, v213\n\t"
    "v_mfma_f32_16x16x32_bf16 v[164:167], a[232:235], v[80:83], v[164:167]\n\t"
    "v_exp_f32 v21, v21\n\t"
    "v_mfma_f32_16x16x32_bf16 v[168:171], a[232:235], v[96:99], v[168:171]\n\t"
    "s_add_u32 s52, s52, 24576\n\t"
    "v_exp_f32 v22, v22\n\t"
    "v_mfma_f32_16x16x32_bf16 v[172:175], a[232:235], v[112:115], v[172:175]\n\t"
    "v_cvt_pk_bf16_f32 v18, v20, v21\n\t"
    "v_fma_f32 v23, v23, s62, v213\n\t"
    "v_mfma_f32_16x16x32_bf16 v[188:191], a[236:239], v[112:115], v[188:191]\n\t"
    "v_exp_f32 v23, v23\n\t"
    "v_mfma_f32_16x16x32_bf16 v[184:187], a[236:239], v[96:99], v[184:187]\n\t"
    "v_cvt_pk_bf16_f32 v19, v22, v23\n\t"
    "v_fma_f32 v32, v32, s62, v214\n\t"
    "v_mfma_f32_16x16x32_bf16 v[180:183], a[236:239], v[80:83], v[180:183]\n\t"
    "s_waitcnt lgkmcnt(0) vmcnt(12)\n\t"
    "v_exp_f32 v32, v32\n\t"
    "v_mfma_f32_16x16x32_bf16 v[176:179], a[236:239], v[64:67], v[176:179]\n\t"
    "v_fma_f32 v33, v33, s62, v214\n\t"
    "v_fma_f32 v34, v34, s62, v214\n\t"
    "v_mfma_f32_16x16x32_bf16 v[128:131], a[240:243], v[72:75], v[128:131]\n\t"
    "s_barrier\n\t"
    "ds_read_b128 a[224:227], v192 offset:24576\n\t"
    "v_fma_f32 v35, v35, s62, v214\n\t"
    "v_mfma_f32_16x16x32_bf16 v[132:135], a[240:243], v[88:91], v[132:135]\n\t"
    "ds_read_b128 a[228:231], v192 offset:30720\n\t"
    "v_fma_f32 v36, v36, s62, v214\n\t"
    "v_mfma_f32_16x16x32_bf16 v[136:139], a[240:243], v[104:107], v[136:139]\n\t"
    "ds_read_b128 a[232:235], v192 offset:36864\n\t"
    "v_fma_f32 v37, v37, s62, v214\n\t"
    "v_mfma_f32_16x16x32_bf16 v[140:143], a[240:243], v[120:123], v[140:143]\n\t"
    "v_exp_f32 v33, v33\n\t"
    "v_mfma_f32_16x16x32_bf16 v[156:159], a[244:247], v[120:123], v[156:159]\n\t"
    "v_exp_f32 v34, v34\n\t"
    "v_mfma_f32_16x16x32_bf16 v[152:155], a[244:247], v[104:107], v[152:155]\n\t"
    "v_exp_f32 v35, v35\n\t"
    "v_mfma_f32_16x16x32_bf16 v[148:151], a[244:247], v[88:91], v[148:151]\n\t"
    "ds_read_b128 a[236:239], v192 offset:43008\n\t"
    "v_cvt_pk_bf16_f32 v32, v32, v33\n\t"
    "v_mfma_f32_16x16x32_bf16 v[144:147], a[244:247], v[72:75], v[144:147]\n\t"
    "v_exp_f32 v36, v36\n\t"
    "v_mfma_f32_16x16x32_bf16 v[160:163], a[248:251], v[72:75], v[160:163]\n\t"
    "v_exp_f32 v37, v37\n\t"
    "v_mfma_f32_16x16x32_bf16 v[164:167], a[248:251], v[88:91], v[164:167]\n\t"
    "v_cvt_pk_bf16_f32 v33, v34, v35\n\t"
    "v_cvt_pk_bf16_f32 v34, v36, v37\n\t"
    "v_mfma_f32_16x16x32_bf16 v[168:171], a[248:251], v[104:107], v[168:171]\n\t"
    "ds_read_b128 a[240:243], v193 offset:24576\n\t"
    "v_fma_f32 v38, v38, s62, v214\n\t"
    "v_mfma_f32_16x16x32_bf16 v[172:175], a[248:251], v[120:123], v[172:175]\n\t"
    "v_exp_f32 v38, v38\n\t"
    "v_mfma_f32_16x16x32_bf16 v[188:191], a[252:255], v[120:123], v[188:191]\n\t"
    "v_fma_f32 v39, v39, s62, v214\n\t"
    "v_fma_f32 v48, v48, s62, v215\n\t"
    "v_mfma_f32_16x16x32_bf16 v[184:187], a[252:255], v[104:107], v[184:187]\n\t"
    "v_exp_f32 v39, v39\n\t"
    "v_mfma_f32_16x16x32_bf16 v[180:183], a[252:255], v[88:91], v[180:183]\n\t"
    "ds_read_b128 a[244:247], v193 offset:30720\n\t"
    "v_cvt_pk_bf16_f32 v35, v38, v39\n\t"
    "v_mfma_f32_16x16x32_bf16 v[176:179], a[252:255], v[72:75], v[176:179]\n\t"
    "s_waitcnt lgkmcnt(4)\n\t"
    "v_exp_f32 v48, v48\n\t"
    "s_add_u32 s55, s55, 25165824\n\t"
    "s_mov_b32 s50, 100663296\n\t"
    "s_add_u32 s69, s69, 1\n\t"
    "s_add_u32 s70, s55, s96\n\t"
    "s_add_u32 s78, s55, s97\n\t"
    "s_add_u32 s79, s55, s98\n\t"
    "s_add_u32 s80, s55, s99\n\t"
    "v_rcp_f32 v224, v228\n\t"
    "v_rcp_f32 v225, v232\n\t"
    "v_rcp_f32 v226, v236\n\t"
    "v_rcp_f32 v227, v240\n\t"
    "s_nop 7\n\t"
    "v_accvgpr_read_b32 v64, a0\n\t"
    "v_accvgpr_read_b32 v65, a1\n\t"
    "v_accvgpr_read_b32 v66, a2\n\t"
    "v_accvgpr_read_b32 v67, a3\n\t"
    "v_accvgpr_read_b32 v68, a4\n\t"
    "v_accvgpr_read_b32 v69, a5\n\t"
    "v_accvgpr_read_b32 v70, a6\n\t"
    "v_accvgpr_read_b32 v71, a7\n\t"
    "v_pk_mul_f32 v[64:65], v[64:65], v[224:225] op_sel:[0,0] op_sel_hi:[1,0]\n\t"
    "v_pk_mul_f32 v[66:67], v[66:67], v[224:225] op_sel:[0,0] op_sel_hi:[1,0]\n\t"
    "v_pk_mul_f32 v[68:69], v[68:69], v[224:225] op_sel:[0,1] op_sel_hi:[1,1]\n\t"
    "v_pk_mul_f32 v[70:71], v[70:71], v[224:225] op_sel:[0,1] op_sel_hi:[1,1]\n\t"
    "v_cvt_pk_bf16_f32 v64, v64, v65\n\t"
    "v_cvt_pk_bf16_f32 v65, v66, v67\n\t"
    "v_cvt_pk_bf16_f32 v66, v68, v69\n\t"
    "v_cvt_pk_bf16_f32 v67, v70, v71\n\t"
    "ds_write2st64_b64 v208, v[64:65], v[66:67] offset0:16 offset1:20\n\t"
    "v_accvgpr_read_b32 v72, a16\n\t"
    "v_accvgpr_read_b32 v73, a17\n\t"
    "v_accvgpr_read_b32 v74, a18\n\t"
    "v_accvgpr_read_b32 v75, a19\n\t"
    "v_accvgpr_read_b32 v76, a20\n\t"
    "v_accvgpr_read_b32 v77, a21\n\t"
    "v_accvgpr_read_b32 v78, a22\n\t"
    "v_accvgpr_read_b32 v79, a23\n\t"
    "v_pk_mul_f32 v[72:73], v[72:73], v[224:225] op_sel:[0,0] op_sel_hi:[1,0]\n\t"
    "v_pk_mul_f32 v[74:75], v[74:75], v[224:225] op_sel:[0,0] op_sel_hi:[1,0]\n\t"
    "v_pk_mul_f32 v[76:77], v[76:77], v[224:225] op_sel:[0,1] op_sel_hi:[1,1]\n\t"
    "v_pk_mul_f32 v[78:79], v[78:79], v[224:225] op_sel:[0,1] op_sel_hi:[1,1]\n\t"
    "v_cvt_pk_bf16_f32 v72, v72, v73\n\t"
    "v_cvt_pk_bf16_f32 v73, v74, v75\n\t"
    "v_cvt_pk_bf16_f32 v74, v76, v77\n\t"
    "v_cvt_pk_bf16_f32 v75, v78, v79\n\t"
    "ds_write2st64_b64 v209, v[72:73], v[74:75] offset0:16 offset1:20\n\t"
    "v_accvgpr_read_b32 v80, a32\n\t"
    "v_accvgpr_read_b32 v81, a33\n\t"
    "v_accvgpr_read_b32 v82, a34\n\t"
    "v_accvgpr_read_b32 v83, a35\n\t"
    "v_accvgpr_read_b32 v84, a36\n\t"
    "v_accvgpr_read_b32 v85, a37\n\t"
    "v_accvgpr_read_b32 v86, a38\n\t"
    "v_accvgpr_read_b32 v87, a39\n\t"
    "v_pk_mul_f32 v[80:81], v[80:81], v[224:225] op_sel:[0,0] op_sel_hi:[1,0]\n\t"
    "v_pk_mul_f32 v[82:83], v[82:83], v[224:225] op_sel:[0,0] op_sel_hi:[1,0]\n\t"
    "v_pk_mul_f32 v[84:85], v[84:85], v[224:225] op_sel:[0,1] op_sel_hi:[1,1]\n\t"
    "v_pk_mul_f32 v[86:87], v[86:87], v[224:225] op_sel:[0,1] op_sel_hi:[1,1]\n\t"
    "v_cvt_pk_bf16_f32 v80, v80, v81\n\t"
    "v_cvt_pk_bf16_f32 v81, v82, v83\n\t"
    "v_cvt_pk_bf16_f32 v82, v84, v85\n\t"
    "v_cvt_pk_bf16_f32 v83, v86, v87\n\t"
    "ds_write2st64_b64 v210, v[80:81], v[82:83] offset0:16 offset1:20\n\t"
    "v_accvgpr_read_b32 v88, a48\n\t"
    "v_accvgpr_read_b32 v89, a49\n\t"
    "v_accvgpr_read_b32 v90, a50\n\t"
    "v_accvgpr_read_b32 v91, a51\n\t"
    "v_accvgpr_read_b32 v92, a52\n\t"
    "v_accvgpr_read_b32 v93, a53\n\t"
    "v_accvgpr_read_b32 v94, a54\n\t"
    "v_accvgpr_read_b32 v95, a55\n\t"
    "v_pk_mul_f32 v[88:89], v[88:89], v[224:225] op_sel:[0,0] op_sel_hi:[1,0]\n\t"
    "v_pk_mul_f32 v[90:91], v[90:91], v[224:225] op_sel:[0,0] op_sel_hi:[1,0]\n\t"
    "v_pk_mul_f32 v[92:93], v[92:93], v[224:225] op_sel:[0,1] op_sel_hi:[1,1]\n\t"
    "v_pk_mul_f32 v[94:95], v[94:95], v[224:225] op_sel:[0,1] op_sel_hi:[1,1]\n\t"
    "v_cvt_pk_bf16_f32 v88, v88, v89\n\t"
    "v_cvt_pk_bf16_f32 v89, v90, v91\n\t"
    "v_cvt_pk_bf16_f32 v90, v92, v93\n\t"
    "v_cvt_pk_bf16_f32 v91, v94, v95\n\t"
    "ds_write2st64_b64 v211, v[88:89], v[90:91] offset0:16 offset1:20\n\t"
    "ds_read_b128 v[96:99], v203 offset:8192\n\t"
    "ds_read_b128 v[100:103], v203 offset:9216\n\t"
    "ds_read_b128 v[104:107], v203 offset:10240\n\t"
    "ds_read_b128 v[108:111], v203 offset:11264\n\t"
    "v_accvgpr_read_b32 v64, a8\n\t"
    "v_accvgpr_read_b32 v65, a9\n\t"
    "v_accvgpr_read_b32 v66, a10\n\t"
    "v_accvgpr_read_b32 v67, a11\n\t"
    "v_accvgpr_read_b32 v68, a12\n\t"
    "v_accvgpr_read_b32 v69, a13\n\t"
    "v_accvgpr_read_b32 v70, a14\n\t"
    "v_accvgpr_read_b32 v71, a15\n\t"
    "v_pk_mul_f32 v[64:65], v[64:65], v[226:227] op_sel:[0,0] op_sel_hi:[1,0]\n\t"
    "v_pk_mul_f32 v[66:67], v[66:67], v[226:227] op_sel:[0,0] op_sel_hi:[1,0]\n\t"
    "v_pk_mul_f32 v[68:69], v[68:69], v[226:227] op_sel:[0,1] op_sel_hi:[1,1]\n\t"
    "v_pk_mul_f32 v[70:71], v[70:71], v[226:227] op_sel:[0,1] op_sel_hi:[1,1]\n\t"
    "v_cvt_pk_bf16_f32 v64, v64, v65\n\t"
    "v_cvt_pk_bf16_f32 v65, v66, v67\n\t"
    "v_cvt_pk_bf16_f32 v66, v68, v69\n\t"
    "v_cvt_pk_bf16_f32 v67, v70, v71\n\t"
    "ds_write2st64_b64 v208, v[64:65], v[66:67] offset0:24 offset1:28\n\t"
    "s_waitcnt lgkmcnt(4)\n\t"
    "buffer_store_dwordx4 v[96:99], v200, s[48:51], s70 offen offset:0 nt\n\t"
    "v_accvgpr_read_b32 v72, a24\n\t"
    "v_accvgpr_read_b32 v73, a25\n\t"
    "v_accvgpr_read_b32 v74, a26\n\t"
    "v_accvgpr_read_b32 v75, a27\n\t"
    "v_accvgpr_read_b32 v76, a28\n\t"
    "v_accvgpr_read_b32 v77, a29\n\t"
    "v_accvgpr_read_b32 v78, a30\n\t"
    "v_accvgpr_read_b32 v79, a31\n\t"
    "v_pk_mul_f32 v[72:73], v[72:73], v[226:227] op_sel:[0,0] op_sel_hi:[1,0]\n\t"
    "v_pk_mul_f32 v[74:75], v[74:75], v[226:227] op_sel:[0,0] op_sel_hi:[1,0]\n\t"
    "v_pk_mul_f32 v[76:77], v[76:77], v[226:227] op_sel:[0,1] op_sel_hi:[1,1]\n\t"
    "v_pk_mul_f32 v[78:79], v[78:79], v[226:227] op_sel:[0,1] op_sel_hi:[1,1]\n\t"
    "v_cvt_pk_bf16_f32 v72, v72, v73\n\t"
    "v_cvt_pk_bf16_f32 v73, v74, v75\n\t"
    "v_cvt_pk_bf16_f32 v74, v76, v77\n\t"
    "v_cvt_pk_bf16_f32 v75, v78, v79\n\t"
    "ds_write2st64_b64 v209, v[72:73], v[74:75] offset0:24 offset1:28\n\t"
    "s_waitcnt lgkmcnt(4)\n\t"
    "buffer_store_dwordx4 v[100:103], v201, s[48:51], s70 offen offset:3072 nt\n\t"
    "v_accvgpr_read_b32 v80, a40\n\t"
    "v_accvgpr_read_b32 v81, a41\n\t"
    "v_accvgpr_read_b32 v82, a42\n\t"
    "v_accvgpr_read_b32 v83, a43\n\t"
    "v_accvgpr_read_b32 v84, a44\n\t"
    "v_accvgpr_read_b32 v85, a45\n\t"
    "v_accvgpr_read_b32 v86, a46\n\t"
    "v_accvgpr_read_b32 v87, a47\n\t"
    "v_pk_mul_f32 v[80:81], v[80:81], v[226:227] op_sel:[0,0] op_sel_hi:[1,0]\n\t"
    "v_pk_mul_f32 v[82:83], v[82:83], v[226:227] op_sel:[0,0] op_sel_hi:[1,0]\n\t"
    "v_pk_mul_f32 v[84:85], v[84:85], v[226:227] op_sel:[0,1] op_sel_hi:[1,1]\n\t"
    "v_pk_mul_f32 v[86:87], v[86:87], v[226:227] op_sel:[0,1] op_sel_hi:[1,1]\n\t"
    "v_cvt_pk_bf16_f32 v80, v80, v81\n\t"
    "v_cvt_pk_bf16_f32 v81, v82, v83\n\t"
    "v_cvt_pk_bf16_f32 v82, v84, v85\n\t"
    "v_cvt_pk_bf16_f32 v83, v86, v87\n\t"
    "ds_write2st64_b64 v210, v[80:81], v[82:83] offset0:24 offset1:28\n\t"
    "s_waitcnt lgkmcnt(4)\n\t"
    "buffer_store_dwordx4 v[104:107], v200, s[48:51], s78 offen offset:0 nt\n\t"
    "v_accvgpr_read_b32 v88, a56\n\t"
    "v_accvgpr_read_b32 v89, a57\n\t"
    "v_accvgpr_read_b32 v90, a58\n\t"
    "v_accvgpr_read_b32 v91, a59\n\t"
    "v_accvgpr_read_b32 v92, a60\n\t"
    "v_accvgpr_read_b32 v93, a61\n\t"
    "v_accvgpr_read_b32 v94, a62\n\t"
    "v_accvgpr_read_b32 v95, a63\n\t"
    "v_pk_mul_f32 v[88:89], v[88:89], v[226:227] op_sel:[0,0] op_sel_hi:[1,0]\n\t"
    "v_pk_mul_f32 v[90:91], v[90:91], v[226:227] op_sel:[0,0] op_sel_hi:[1,0]\n\t"
    "v_pk_mul_f32 v[92:93], v[92:93], v[226:227] op_sel:[0,1] op_sel_hi:[1,1]\n\t"
    "v_pk_mul_f32 v[94:95], v[94:95], v[226:227] op_sel:[0,1] op_sel_hi:[1,1]\n\t"
    "v_cvt_pk_bf16_f32 v88, v88, v89\n\t"
    "v_cvt_pk_bf16_f32 v89, v90, v91\n\t"
    "v_cvt_pk_bf16_f32 v90, v92, v93\n\t"
    "v_cvt_pk_bf16_f32 v91, v94, v95\n\t"
    "ds_write2st64_b64 v211, v[88:89], v[90:91] offset0:24 offset1:28\n\t"
    "s_waitcnt lgkmcnt(4)\n\t"
    "buffer_store_dwordx4 v[108:111], v201, s[48:51], s78 offen offset:3072 nt\n\t"
    "ds_read_b128 v[112:115], v203 offset:12288\n\t"
    "ds_read_b128 v[116:119], v203 offset:13312\n\t"
    "ds_read_b128 v[120:123], v203 offset:14336\n\t"
    "ds_read_b128 v[124:127], v203 offset:15360\n\t"
    "v_accvgpr_read_b32 v64, a64\n\t"
    "v_accvgpr_read_b32 v65, a65\n\t"
    "v_accvgpr_read_b32 v66, a66\n\t"
    "v_accvgpr_read_b32 v67, a67\n\t"
    "v_accvgpr_read_b32 v68, a68\n\t"
    "v_accvgpr_read_b32 v69, a69\n\t"
    "v_accvgpr_read_b32 v70, a70\n\t"
    "v_accvgpr_read_b32 v71, a71\n\t"
    "v_pk_mul_f32 v[64:65], v[64:65], v[224:225] op_sel:[0,0] op_sel_hi:[1,0]\n\t"
    "v_pk_mul_f32 v[66:67], v[66:67], v[224:225] op_sel:[0,0] op_sel_hi:[1,0]\n\t"
    "v_pk_mul_f32 v[68:69], v[68:69], v[224:225] op_sel:[0,1] op_sel_hi:[1,1]\n\t"
    "v_pk_mul_f32 v[70:71], v[70:71], v[224:225] op_sel:[0,1] op_sel_hi:[1,1]\n\t"
    "v_cvt_pk_bf16_f32 v64, v64, v65\n\t"
    "v_cvt_pk_bf16_f32 v65, v66, v67\n\t"
    "v_cvt_pk_bf16_f32 v66, v68, v69\n\t"
    "v_cvt_pk_bf16_f32 v67, v70, v71\n\t"
    "ds_write2st64_b64 v208, v[64:65], v[66:67] offset0:0 offset1:4\n\t"
    "s_waitcnt lgkmcnt(4)\n\t"
    "buffer_store_dwordx4 v[112:115], v200, s[48:51], s79 offen offset:0 nt\n\t"
    "v_accvgpr_read_b32 v72, a80\n\t"
    "v_accvgpr_read_b32 v73, a81\n\t"
    "v_accvgpr_read_b32 v74, a82\n\t"
    "v_accvgpr_read_b32 v75, a83\n\t"
    "v_accvgpr_read_b32 v76, a84\n\t"
    "v_accvgpr_read_b32 v77, a85\n\t"
    "v_accvgpr_read_b32 v78, a86\n\t"
    "v_accvgpr_read_b32 v79, a87\n\t"
    "v_pk_mul_f32 v[72:73], v[72:73], v[224:225] op_sel:[0,0] op_sel_hi:[1,0]\n\t"
    "v_pk_mul_f32 v[74:75], v[74:75], v[224:225] op_sel:[0,0] op_sel_hi:[1,0]\n\t"
    "v_pk_mul_f32 v[76:77], v[76:77], v[224:225] op_sel:[0,1] op_sel_hi:[1,1]\n\t"
    "v_pk_mul_f32 v[78:79], v[78:79], v[224:225] op_sel:[0,1] op_sel_hi:[1,1]\n\t"
    "v_cvt_pk_bf16_f32 v72, v72, v73\n\t"
    "v_cvt_pk_bf16_f32 v73, v74, v75\n\t"
    "v_cvt_pk_bf16_f32 v74, v76, v77\n\t"
    "v_cvt_pk_bf16_f32 v75, v78, v79\n\t"
    "ds_write2st64_b64 v209, v[72:73], v[74:75] offset0:0 offset1:4\n\t"
    "s_waitcnt lgkmcnt(4)\n\t"
    "buffer_store_dwordx4 v[116:119], v201, s[48:51], s79 offen offset:3072 nt\n\t"
    "v_accvgpr_read_b32 v80, a96\n\t"
    "v_accvgpr_read_b32 v81, a97\n\t"
    "v_accvgpr_read_b32 v82, a98\n\t"
    "v_accvgpr_read_b32 v83, a99\n\t"
    "v_accvgpr_read_b32 v84, a100\n\t"
    "v_accvgpr_read_b32 v85, a101\n\t"
    "v_accvgpr_read_b32 v86, a102\n\t"
    "v_accvgpr_read_b32 v87, a103\n\t"
    "v_pk_mul_f32 v[80:81], v[80:81], v[224:225] op_sel:[0,0] op_sel_hi:[1,0]\n\t"
    "v_pk_mul_f32 v[82:83], v[82:83], v[224:225] op_sel:[0,0] op_sel_hi:[1,0]\n\t"
    "v_pk_mul_f32 v[84:85], v[84:85], v[224:225] op_sel:[0,1] op_sel_hi:[1,1]\n\t"
    "v_pk_mul_f32 v[86:87], v[86:87], v[224:225] op_sel:[0,1] op_sel_hi:[1,1]\n\t"
    "v_cvt_pk_bf16_f32 v80, v80, v81\n\t"
    "v_cvt_pk_bf16_f32 v81, v82, v83\n\t"
    "v_cvt_pk_bf16_f32 v82, v84, v85\n\t"
    "v_cvt_pk_bf16_f32 v83, v86, v87\n\t"
    "ds_write2st64_b64 v210, v[80:81], v[82:83] offset0:0 offset1:4\n\t"
    "s_waitcnt lgkmcnt(4)\n\t"
    "buffer_store_dwordx4 v[120:123], v200, s[48:51], s80 offen offset:0 nt\n\t"
    "v_accvgpr_read_b32 v88, a112\n\t"
    "v_accvgpr_read_b32 v89, a113\n\t"
    "v_accvgpr_read_b32 v90, a114\n\t"
    "v_accvgpr_read_b32 v91, a115\n\t"
    "v_accvgpr_read_b32 v92, a116\n\t"
    "v_accvgpr_read_b32 v93, a117\n\t"
    "v_accvgpr_read_b32 v94, a118\n\t"
    "v_accvgpr_read_b32 v95, a119\n\t"
    "v_pk_mul_f32 v[88:89], v[88:89], v[224:225] op_sel:[0,0] op_sel_hi:[1,0]\n\t"
    "v_pk_mul_f32 v[90:91], v[90:91], v[224:225] op_sel:[0,0] op_sel_hi:[1,0]\n\t"
    "v_pk_mul_f32 v[92:93], v[92:93], v[224:225] op_sel:[0,1] op_sel_hi:[1,1]\n\t"
    "v_pk_mul_f32 v[94:95], v[94:95], v[224:225] op_sel:[0,1] op_sel_hi:[1,1]\n\t"
    "v_cvt_pk_bf16_f32 v88, v88, v89\n\t"
    "v_cvt_pk_bf16_f32 v89, v90, v91\n\t"
    "v_cvt_pk_bf16_f32 v90, v92, v93\n\t"
    "v_cvt_pk_bf16_f32 v91, v94, v95\n\t"
    "ds_write2st64_b64 v211, v[88:89], v[90:91] offset0:0 offset1:4\n\t"
    "s_waitcnt lgkmcnt(4)\n\t"
    "buffer_store_dwordx4 v[124:127], v201, s[48:51], s80 offen offset:3072 nt\n\t"
    "v_accvgpr_read_b32 v64, a72\n\t"
    "v_accvgpr_read_b32 v65, a73\n\t"
    "v_accvgpr_read_b32 v66, a74\n\t"
    "v_accvgpr_read_b32 v67, a75\n\t"
    "v_accvgpr_read_b32 v68, a76\n\t"
    "v_accvgpr_read_b32 v69, a77\n\t"
    "v_accvgpr_read_b32 v70, a78\n\t"
    "v_accvgpr_read_b32 v71, a79\n\t"
    "v_pk_mul_f32 v[64:65], v[64:65], v[226:227] op_sel:[0,0] op_sel_hi:[1,0]\n\t"
    "v_pk_mul_f32 v[66:67], v[66:67], v[226:227] op_sel:[0,0] op_sel_hi:[1,0]\n\t"
    "v_pk_mul_f32 v[68:69], v[68:69], v[226:227] op_sel:[0,1] op_sel_hi:[1,1]\n\t"
    "v_pk_mul_f32 v[70:71], v[70:71], v[226:227] op_sel:[0,1] op_sel_hi:[1,1]\n\t"
    "v_cvt_pk_bf16_f32 v64, v64, v65\n\t"
    "v_cvt_pk_bf16_f32 v65, v66, v67\n\t"
    "v_cvt_pk_bf16_f32 v66, v68, v69\n\t"
    "v_cvt_pk_bf16_f32 v67, v70, v71\n\t"
    "ds_write2st64_b64 v208, v[64:65], v[66:67] offset0:8 offset1:12\n\t"
    "v_accvgpr_read_b32 v72, a88\n\t"
    "v_accvgpr_read_b32 v73, a89\n\t"
    "v_accvgpr_read_b32 v74, a90\n\t"
    "v_accvgpr_read_b32 v75, a91\n\t"
    "v_accvgpr_read_b32 v76, a92\n\t"
    "v_accvgpr_read_b32 v77, a93\n\t"
    "v_accvgpr_read_b32 v78, a94\n\t"
    "v_accvgpr_read_b32 v79, a95\n\t"
    "v_pk_mul_f32 v[72:73], v[72:73], v[226:227] op_sel:[0,0] op_sel_hi:[1,0]\n\t"
    "v_pk_mul_f32 v[74:75], v[74:75], v[226:227] op_sel:[0,0] op_sel_hi:[1,0]\n\t"
    "v_pk_mul_f32 v[76:77], v[76:77], v[226:227] op_sel:[0,1] op_sel_hi:[1,1]\n\t"
    "v_pk_mul_f32 v[78:79], v[78:79], v[226:227] op_sel:[0,1] op_sel_hi:[1,1]\n\t"
    "v_cvt_pk_bf16_f32 v72, v72, v73\n\t"
    "v_cvt_pk_bf16_f32 v73, v74, v75\n\t"
    "v_cvt_pk_bf16_f32 v74, v76, v77\n\t"
    "v_cvt_pk_bf16_f32 v75, v78, v79\n\t"
    "ds_write2st64_b64 v209, v[72:73], v[74:75] offset0:8 offset1:12\n\t"
    "v_accvgpr_read_b32 v80, a104\n\t"
    "v_accvgpr_read_b32 v81, a105\n\t"
    "v_accvgpr_read_b32 v82, a106\n\t"
    "v_accvgpr_read_b32 v83, a107\n\t"
    "v_accvgpr_read_b32 v84, a108\n\t"
    "v_accvgpr_read_b32 v85, a109\n\t"
    "v_accvgpr_read_b32 v86, a110\n\t"
    "v_accvgpr_read_b32 v87, a111\n\t"
    "v_pk_mul_f32 v[80:81], v[80:81], v[226:227] op_sel:[0,0] op_sel_hi:[1,0]\n\t"
    "v_pk_mul_f32 v[82:83], v[82:83], v[226:227] op_sel:[0,0] op_sel_hi:[1,0]\n\t"
    "v_pk_mul_f32 v[84:85], v[84:85], v[226:227] op_sel:[0,1] op_sel_hi:[1,1]\n\t"
    "v_pk_mul_f32 v[86:87], v[86:87], v[226:227] op_sel:[0,1] op_sel_hi:[1,1]\n\t"
    "v_cvt_pk_bf16_f32 v80, v80, v81\n\t"
    "v_cvt_pk_bf16_f32 v81, v82, v83\n\t"
    "v_cvt_pk_bf16_f32 v82, v84, v85\n\t"
    "v_cvt_pk_bf16_f32 v83, v86, v87\n\t"
    "ds_write2st64_b64 v210, v[80:81], v[82:83] offset0:8 offset1:12\n\t"
    "s_waitcnt lgkmcnt(6)\n\t"
    "v_accvgpr_read_b32 v88, a120\n\t"
    "v_accvgpr_read_b32 v89, a121\n\t"
    "v_accvgpr_read_b32 v90, a122\n\t"
    "v_accvgpr_read_b32 v91, a123\n\t"
    "v_accvgpr_read_b32 v92, a124\n\t"
    "v_accvgpr_read_b32 v93, a125\n\t"
    "v_accvgpr_read_b32 v94, a126\n\t"
    "v_accvgpr_read_b32 v95, a127\n\t"
    "v_pk_mul_f32 v[88:89], v[88:89], v[226:227] op_sel:[0,0] op_sel_hi:[1,0]\n\t"
    "v_pk_mul_f32 v[90:91], v[90:91], v[226:227] op_sel:[0,0] op_sel_hi:[1,0]\n\t"
    "v_pk_mul_f32 v[92:93], v[92:93], v[226:227] op_sel:[0,1] op_sel_hi:[1,1]\n\t"
    "v_pk_mul_f32 v[94:95], v[94:95], v[226:227] op_sel:[0,1] op_sel_hi:[1,1]\n\t"
    "v_cvt_pk_bf16_f32 v88, v88, v89\n\t"
    "v_cvt_pk_bf16_f32 v89, v90, v91\n\t"
    "v_cvt_pk_bf16_f32 v90, v92, v93\n\t"
    "v_cvt_pk_bf16_f32 v91, v94, v95\n\t"
    "ds_write2st64_b64 v211, v[88:89], v[90:91] offset0:8 offset1:12\n\t"
    "s_waitcnt lgkmcnt(6)\n\t"
    "v_pk_mul_f32 v[64:65], v[128:129], v[224:225] op_sel:[0,0] op_sel_hi:[1,0]\n\t"
    "v_pk_mul_f32 v[66:67], v[130:131], v[224:225] op_sel:[0,0] op_sel_hi:[1,0]\n\t"
    "v_pk_mul_f32 v[68:69], v[132:133], v[224:225] op_sel:[0,1] op_sel_hi:[1,1]\n\t"
    "v_pk_mul_f32 v[70:71], v[134:135], v[224:225] op_sel:[0,1] op_sel_hi:[1,1]\n\t"
    "v_cvt_pk_bf16_f32 v64, v64, v65\n\t"
    "v_cvt_pk_bf16_f32 v65, v66, v67\n\t"
    "v_cvt_pk_bf16_f32 v66, v68, v69\n\t"
    "v_cvt_pk_bf16_f32 v67, v70, v71\n\t"
    "ds_write2st64_b64 v208, v[64:65], v[66:67] offset0:16 offset1:20\n\t"
    "s_waitcnt lgkmcnt(6)\n\t"
    "v_pk_mul_f32 v[72:73], v[144:145], v[224:225] op_sel:[0,0] op_sel_hi:[1,0]\n\t"
    "v_pk_mul_f32 v[74:75], v[146:147], v[224:225] op_sel:[0,0] op_sel_hi:[1,0]\n\t"
    "v_pk_mul_f32 v[76:77], v[148:149], v[224:225] op_sel:[0,1] op_sel_hi:[1,1]\n\t"
    "v_pk_mul_f32 v[78:79], v[150:151], v[224:225] op_sel:[0,1] op_sel_hi:[1,1]\n\t"
    "v_cvt_pk_bf16_f32 v72, v72, v73\n\t"
    "v_cvt_pk_bf16_f32 v73, v74, v75\n\t"
    "v_cvt_pk_bf16_f32 v74, v76, v77\n\t"
    "v_cvt_pk_bf16_f32 v75, v78, v79\n\t"
    "ds_write2st64_b64 v209, v[72:73], v[74:75] offset0:16 offset1:20\n\t"
    "s_waitcnt lgkmcnt(6)\n\t"
    "v_pk_mul_f32 v[80:81], v[160:161], v[224:225] op_sel:[0,0] op_sel_hi:[1,0]\n\t"
    "v_pk_mul_f32 v[82:83], v[162:163], v[224:225] op_sel:[0,0] op_sel_hi:[1,0]\n\t"
    "v_pk_mul_f32 v[84:85], v[164:165], v[224:225] op_sel:[0,1] op_sel_hi:[1,1]\n\t"
    "v_pk_mul_f32 v[86:87], v[166:167], v[224:225] op_sel:[0,1] op_sel_hi:[1,1]\n\t"
    "v_cvt_pk_bf16_f32 v80, v80, v81\n\t"
    "v_cvt_pk_bf16_f32 v81, v82, v83\n\t"
    "v_cvt_pk_bf16_f32 v82, v84, v85\n\t"
    "v_cvt_pk_bf16_f32 v83, v86, v87\n\t"
    "ds_write2st64_b64 v210, v[80:81], v[82:83] offset0:16 offset1:20\n\t"
    "s_waitcnt lgkmcnt(6)\n\t"
    "v_pk_mul_f32 v[88:89], v[176:177], v[224:225] op_sel:[0,0] op_sel_hi:[1,0]\n\t"
    "v_pk_mul_f32 v[90:91], v[178:179], v[224:225] op_sel:[0,0] op_sel_hi:[1,0]\n\t"
    "v_pk_mul_f32 v[92:93], v[180:181], v[224:225] op_sel:[0,1] op_sel_hi:[1,1]\n\t"
    "v_pk_mul_f32 v[94:95], v[182:183], v[224:225] op_sel:[0,1] op_sel_hi:[1,1]\n\t"
    "v_cvt_pk_bf16_f32 v88, v88, v89\n\t"
    "v_cvt_pk_bf16_f32 v89, v90, v91\n\t"
    "v_cvt_pk_bf16_f32 v90, v92, v93\n\t"
    "v_cvt_pk_bf16_f32 v91, v94, v95\n\t"
    "ds_write2st64_b64 v211, v[88:89], v[90:91] offset0:16 offset1:20\n\t"
    "s_waitcnt lgkmcnt(6)\n\t"
    "v_pk_mul_f32 v[64:65], v[136:137], v[226:227] op_sel:[0,0] op_sel_hi:[1,0]\n\t"
    "v_pk_mul_f32 v[66:67], v[138:139], v[226:227] op_sel:[0,0] op_sel_hi:[1,0]\n\t"
    "v_pk_mul_f32 v[68:69], v[140:141], v[226:227] op_sel:[0,1] op_sel_hi:[1,1]\n\t"
    "v_pk_mul_f32 v[70:71], v[142:143], v[226:227] op_sel:[0,1] op_sel_hi:[1,1]\n\t"
    "v_cvt_pk_bf16_f32 v64, v64, v65\n\t"
    "v_cvt_pk_bf16_f32 v65, v66, v67\n\t"
    "v_cvt_pk_bf16_f32 v66, v68, v69\n\t"
    "v_cvt_pk_bf16_f32 v67, v70, v71\n\t"
    "ds_write2st64_b64 v208, v[64:65], v[66:67] offset0:24 offset1:28\n\t"
    "s_waitcnt lgkmcnt(6)\n\t"
    "v_pk_mul_f32 v[72:73], v[152:153], v[226:227] op_sel:[0,0] op_sel_hi:[1,0]\n\t"
    "v_pk_mul_f32 v[74:75], v[154:155], v[226:227] op_sel:[0,0] op_sel_hi:[1,0]\n\t"
    "v_pk_mul_f32 v[76:77], v[156:157], v[226:227] op_sel:[0,1] op_sel_hi:[1,1]\n\t"
    "v_pk_mul_f32 v[78:79], v[158:159], v[226:227] op_sel:[0,1] op_sel_hi:[1,1]\n\t"
    "v_cvt_pk_bf16_f32 v72, v72, v73\n\t"
    "v_cvt_pk_bf16_f32 v73, v74, v75\n\t"
    "v_cvt_pk_bf16_f32 v74, v76, v77\n\t"
    "v_cvt_pk_bf16_f32 v75, v78, v79\n\t"
    "ds_write2st64_b64 v209, v[72:73], v[74:75] offset0:24 offset1:28\n\t"
    "s_waitcnt lgkmcnt(6)\n\t"
    "v_pk_mul_f32 v[80:81], v[168:169], v[226:227] op_sel:[0,0] op_sel_hi:[1,0]\n\t"
    "v_pk_mul_f32 v[82:83], v[170:171], v[226:227] op_sel:[0,0] op_sel_hi:[1,0]\n\t"
    "v_pk_mul_f32 v[84:85], v[172:173], v[226:227] op_sel:[0,1] op_sel_hi:[1,1]\n\t"
    "v_pk_mul_f32 v[86:87], v[174:175], v[226:227] op_sel:[0,1] op_sel_hi:[1,1]\n\t"
    "v_cvt_pk_bf16_f32 v80, v80, v81\n\t"
    "v_cvt_pk_bf16_f32 v81, v82, v83\n\t"
    "v_cvt_pk_bf16_f32 v82, v84, v85\n\t"
    "v_cvt_pk_bf16_f32 v83, v86, v87\n\t"
    "ds_write2st64_b64 v210, v[80:81], v[82:83] offset0:24 offset1:28\n\t"
    "s_waitcnt lgkmcnt(6)\n\t"
    "v_pk_mul_f32 v[88:89], v[184:185], v[226:227] op_sel:[0,0] op_sel_hi:[1,0]\n\t"
    "v_pk_mul_f32 v[90:91], v[186:187], v[226:227] op_sel:[0,0] op_sel_hi:[1,0]\n\t"
    "v_pk_mul_f32 v[92:93], v[188:189], v[226:227] op_sel:[0,1] op_sel_hi:[1,1]\n\t"
    "v_pk_mul_f32 v[94:95], v[190:191], v[226:227] op_sel:[0,1] op_sel_hi:[1,1]\n\t"
    "v_cvt_pk_bf16_f32 v88, v88, v89\n\t"
    "v_cvt_pk_bf16_f32 v89, v90, v91\n\t"
    "v_cvt_pk_bf16_f32 v90, v92, v93\n\t"
    "v_cvt_pk_bf16_f32 v91, v94, v95\n\t"
    "ds_write2st64_b64 v211, v[88:89], v[90:91] offset0:24 offset1:28\n\t"
    "s_waitcnt lgkmcnt(6)\n\t"
    "v_mov_b32 v224, 0x44400000\n\t"
    "v_mov_b32 v225, 0x44400000\n\t"
    "v_mov_b32 v226, 0x44400000\n\t"
    "v_mov_b32 v227, 0x44400000\n\t"
    "s_nop 1\n\t"
    "s_branch LOOP\n\t"
    "LAST7:\n\t"
    "s_cmp_lg_u32 s72, 0\n\t"
    "s_cbranch_scc1 SB7L\n\t"
    "NSB7L:\n\t"
    "v_fma_f32 v73, v73, s62, v212\n\t"
    "v_exp_f32 v73, v73\n\t"
    "s_nop 0\n\t"
    "v_cvt_pk_bf16_f32 v72, v72, v73\n\t"
    "v_fma_f32 v74, v74, s62, v212\n\t"
    "v_exp_f32 v74, v74\n\t"
    "v_fma_f32 v75, v75, s62, v212\n\t"
    "v_fma_f32 v76, v76, s62, v212\n\t"
    "v_fma_f32 v77, v77, s62, v212\n\t"
    "v_exp_f32 v75, v75\n\t"
    "v_exp_f32 v76, v76\n\t"
    "v_exp_f32 v77, v77\n\t"
    "v_cvt_pk_bf16_f32 v73, v74, v75\n\t"
    "v_cvt_pk_bf16_f32 v74, v76, v77\n\t"
    "v_fma_f32 v78, v78, s62, v212\n\t"
    "v_exp_f32 v78, v78\n\t"
    "v_fma_f32 v79, v79, s62, v212\n\t"
    "v_fma_f32 v88, v88, s62, v213\n\t"
    "v_fma_f32 v89, v89, s62, v213\n\t"
    "v_fma_f32 v90, v90, s62, v213\n\t"
    "v_exp_f32 v79, v79\n\t"
    "v_exp_f32 v88, v88\n\t"
    "v_cvt_pk_bf16_f32 v75, v78, v79\n\t"
    "v_exp_f32 v89, v89\n\t"
    "v_exp_f32 v90, v90\n\t"
    "v_cvt_pk_bf16_f32 v88, v88, v89\n\t"
    "v_fma_f32 v91, v91, s62, v213\n\t"
    "v_fma_f32 v92, v92, s62, v213\n\t"
    "v_exp_f32 v91, v91\n\t"
    "v_exp_f32 v92, v92\n\t"
    "v_cvt_pk_bf16_f32 v89, v90, v91\n\t"
    "v_fma_f32 v93, v93, s62, v213\n\t"
    "v_fma_f32 v94, v94, s62, v213\n\t"
    "v_fma_f32 v95, v95, s62, v213\n\t"
    "v_exp_f32 v93, v93\n\t"
    "v_exp_f32 v94, v94\n\t"
    "v_cvt_pk_bf16_f32 v90, v92, v93\n\t"
    "v_exp_f32 v95, v95\n\t"
    "s_nop 0\n\t"
    "v_cvt_pk_bf16_f32 v91, v94, v95\n\t"
    "v_fma_f32 v104, v104, s62, v214\n\t"
    "v_exp_f32 v104, v104\n\t"
    "v_fma_f32 v105, v105, s62, v214\n\t"
    "v_exp_f32 v105, v105\n\t"
    "s_nop 0\n\t"
    "v_cvt_pk_bf16_f32 v104, v104, v105\n\t"
    "v_fma_f32 v106, v106, s62, v214\n\t"
    "v_exp_f32 v106, v106\n\t"
    "v_fma_f32 v107, v107, s62, v214\n\t"
    "v_fma_f32 v108, v108, s62, v214\n\t"
    "v_exp_f32 v107, v107\n\t"
    "v_exp_f32 v108, v108\n\t"
    "v_cvt_pk_bf16_f32 v105, v106, v107\n\t"
    "v_fma_f32 v109, v109, s62, v214\n\t"
    "v_fma_f32 v110, v110, s62, v214\n\t"
    "v_exp_f32 v109, v109\n\t"
    "s_nop 0\n\t"
    "v_cvt_pk_bf16_f32 v106, v108, v109\n\t"
    "v_fma_f32 v111, v111, s62, v214\n\t"
    "v_fma_f32 v120, v120, s62, v215\n\t"
    "v_exp_f32 v110, v110\n\t"
    "v_fma_f32 v121, v121, s62, v215\n\t"
    "v_fma_f32 v122, v122, s62, v215\n\t"
    "v_fma_f32 v123, v123, s62, v215\n\t"
    "v_fma_f32 v124, v124, s62, v215\n\t"
    "v_exp_f32 v111, v111\n\t"
    "s_nop 0\n\t"
    "v_cvt_pk_bf16_f32 v107, v110, v111\n\t"
    "v_exp_f32 v120, v120\n\t"
    "v_exp_f32 v121, v121\n\t"
    "v_exp_f32 v122, v122\n\t"
    "v_cvt_pk_bf16_f32 v120, v120, v121\n\t"
    "v_fma_f32 v125, v125, s62, v215\n\t"
    "v_exp_f32 v123, v123\n\t"
    "v_exp_f32 v124, v124\n\t"
    "v_cvt_pk_bf16_f32 v121, v122, v123\n\t"
    "v_exp_f32 v125, v125\n\t"
    "s_nop 0\n\t"
    "v_cvt_pk_bf16_f32 v122, v124, v125\n\t"
    "v_fma_f32 v126, v126, s62, v215\n\t"
    "v_exp_f32 v126, v126\n\t"
    "v_fma_f32 v127, v127, s62, v215\n\t"
    "v_exp_f32 v127, v127\n\t"
    "s_nop 0\n\t"
    "v_cvt_pk_bf16_f32 v123, v126, v127\n\t"
    "s_waitcnt lgkmcnt(15) vmcnt(12)\n\t"
    "s_barrier\n\t"
    "ds_read_b64_tr_b16 a[224:225], v196 offset:24576\n\t"
    "ds_read_b64_tr_b16 a[226:227], v196 offset:30720\n\t"
    "ds_read_b64_tr_b16 a[228:229], v197 offset:24576\n\t"
    "ds_read_b64_tr_b16 a[230:231], v197 offset:30720\n\t"
    "ds_read_b64_tr_b16 a[232:233], v198 offset:24576\n\t"
    "ds_read_b64_tr_b16 a[234:235], v198 offset:30720\n\t"
    "ds_read_b64_tr_b16 a[236:237], v199 offset:24576\n\t"
    "ds_read_b64_tr_b16 a[238:239], v199 offset:30720\n\t"
    "ds_read_b64_tr_b16 a[240:241], v196 offset:36864\n\t"
    "ds_read_b64_tr_b16 a[242:243], v196 offset:43008\n\t"
    "ds_read_b64_tr_b16 a[244:245], v197 offset:36864\n\t"
    "ds_read_b64_tr_b16 a[246:247], v197 offset:43008\n\t"
    "s_waitcnt lgkmcnt(8)\n\t"
    "v_mfma_f32_16x16x32_bf16 a[0:3], a[224:227], v[64:67], a[0:3]\n\t"
    "ds_read_b64_tr_b16 a[248:249], v198 offset:36864\n\t"
    "v_mfma_f32_16x16x32_bf16 a[4:7], a[224:227], v[80:83], a[4:7]\n\t"
    "v_mfma_f32_16x16x32_bf16 a[8:11], a[224:227], v[96:99], a[8:11]\n\t"
    "ds_read_b64_tr_b16 a[250:251], v198 offset:43008\n\t"
    "v_mfma_f32_16x16x32_bf16 a[12:15], a[224:227], v[112:115], a[12:15]\n\t"
    "v_mfma_f32_16x16x32_bf16 a[28:31], a[228:231], v[112:115], a[28:31]\n\t"
    "ds_read_b64_tr_b16 a[252:253], v199 offset:36864\n\t"
    "v_mfma_f32_16x16x32_bf16 a[24:27], a[228:231], v[96:99], a[24:27]\n\t"
    "v_mfma_f32_16x16x32_bf16 a[20:23], a[228:231], v[80:83], a[20:23]\n\t"
    "ds_read_b64_tr_b16 a[254:255], v199 offset:43008\n\t"
    "v_mfma_f32_16x16x32_bf16 a[16:19], a[228:231], v[64:67], a[16:19]\n\t"
    "s_waitcnt lgkmcnt(8)\n\t"
    "v_mfma_f32_16x16x32_bf16 a[32:35], a[232:235], v[64:67], a[32:35]\n\t"
    "ds_read_b64_tr_b16 a[224:225], v196 offset:25600\n\t"
    "v_mfma_f32_16x16x32_bf16 a[36:39], a[232:235], v[80:83], a[36:39]\n\t"
    "v_mfma_f32_16x16x32_bf16 v[228:231], v[244:247], v[64:67], v[228:231]\n\t"
    "v_mfma_f32_16x16x32_bf16 a[40:43], a[232:235], v[96:99], a[40:43]\n\t"
    "ds_read_b64_tr_b16 a[226:227], v196 offset:31744\n\t"
    "v_mfma_f32_16x16x32_bf16 a[44:47], a[232:235], v[112:115], a[44:47]\n\t"
    "v_mfma_f32_16x16x32_bf16 a[60:63], a[236:239], v[112:115], a[60:63]\n\t"
    "ds_read_b64_tr_b16 a[228:229], v197 offset:25600\n\t"
    "v_mfma_f32_16x16x32_bf16 a[56:59], a[236:239], v[96:99], a[56:59]\n\t"
    "v_mfma_f32_16x16x32_bf16 v[232:235], v[244:247], v[80:83], v[232:235]\n\t"
    "v_mfma_f32_16x16x32_bf16 a[52:55], a[236:239], v[80:83], a[52:55]\n\t"
    "ds_read_b64_tr_b16 a[230:231], v197 offset:31744\n\t"
    "v_mfma_f32_16x16x32_bf16 a[48:51], a[236:239], v[64:67], a[48:51]\n\t"
    "s_waitcnt lgkmcnt(8)\n\t"
    "v_mfma_f32_16x16x32_bf16 a[0:3], a[240:243], v[72:75], a[0:3]\n\t"
    "ds_read_b64_tr_b16 a[232:233], v198 offset:25600\n\t"
    "v_mfma_f32_16x16x32_bf16 a[4:7], a[240:243], v[88:91], a[4:7]\n\t"
    "v_mfma_f32_16x16x32_bf16 v[236:239], v[244:247], v[96:99], v[236:239]\n\t"
    "v_mfma_f32_16x16x32_bf16 a[8:11], a[240:243], v[104:107], a[8:11]\n\t"
    "ds_read_b64_tr_b16 a[234:235], v198 offset:31744\n\t"
    "v_mfma_f32_16x16x32_bf16 a[12:15], a[240:243], v[120:123], a[12:15]\n\t"
    "v_mfma_f32_16x16x32_bf16 a[28:31], a[244:247], v[120:123], a[28:31]\n\t"
    "ds_read_b64_tr_b16 a[236:237], v199 offset:25600\n\t"
    "v_mfma_f32_16x16x32_bf16 a[24:27], a[244:247], v[104:107], a[24:27]\n\t"
    "v_mfma_f32_16x16x32_bf16 v[240:243], v[244:247], v[112:115], v[240:243]\n\t"
    "v_mfma_f32_16x16x32_bf16 a[20:23], a[244:247], v[88:91], a[20:23]\n\t"
    "ds_read_b64_tr_b16 a[238:239], v199 offset:31744\n\t"
    "v_mfma_f32_16x16x32_bf16 a[16:19], a[244:247], v[72:75], a[16:19]\n\t"
    "s_waitcnt lgkmcnt(8)\n\t"
    "v_mfma_f32_16x16x32_bf16 a[32:35], a[248:251], v[72:75], a[32:35]\n\t"
    "ds_read_b64_tr_b16 a[240:241], v196 offset:37888\n\t"
    "v_mfma_f32_16x16x32_bf16 a[36:39], a[248:251], v[88:91], a[36:39]\n\t"
    "v_mfma_f32_16x16x32_bf16 a[40:43], a[248:251], v[104:107], a[40:43]\n\t"
    "ds_read_b64_tr_b16 a[242:243], v196 offset:44032\n\t"
    "v_mfma_f32_16x16x32_bf16 a[44:47], a[248:251], v[120:123], a[44:47]\n\t"
    "v_mfma_f32_16x16x32_bf16 a[60:63], a[252:255], v[120:123], a[60:63]\n\t"
    "ds_read_b64_tr_b16 a[244:245], v197 offset:37888\n\t"
    "v_mfma_f32_16x16x32_bf16 a[56:59], a[252:255], v[104:107], a[56:59]\n\t"
    "v_mfma_f32_16x16x32_bf16 v[228:231], v[244:247], v[72:75], v[228:231]\n\t"
    "v_mfma_f32_16x16x32_bf16 a[52:55], a[252:255], v[88:91], a[52:55]\n\t"
    "ds_read_b64_tr_b16 a[246:247], v197 offset:44032\n\t"
    "v_mfma_f32_16x16x32_bf16 a[48:51], a[252:255], v[72:75], a[48:51]\n\t"
    "s_waitcnt lgkmcnt(8)\n\t"
    "v_mfma_f32_16x16x32_bf16 a[64:67], a[224:227], v[64:67], a[64:67]\n\t"
    "ds_read_b64_tr_b16 a[248:249], v198 offset:37888\n\t"
    "v_mfma_f32_16x16x32_bf16 a[68:71], a[224:227], v[80:83], a[68:71]\n\t"
    "v_mfma_f32_16x16x32_bf16 v[232:235], v[244:247], v[88:91], v[232:235]\n\t"
    "v_mfma_f32_16x16x32_bf16 a[72:75], a[224:227], v[96:99], a[72:75]\n\t"
    "ds_read_b64_tr_b16 a[250:251], v198 offset:44032\n\t"
    "v_mfma_f32_16x16x32_bf16 a[76:79], a[224:227], v[112:115], a[76:79]\n\t"
    "v_mfma_f32_16x16x32_bf16 a[92:95], a[228:231], v[112:115], a[92:95]\n\t"
    "ds_read_b64_tr_b16 a[252:253], v199 offset:37888\n\t"
    "v_mfma_f32_16x16x32_bf16 a[88:91], a[228:231], v[96:99], a[88:91]\n\t"
    "v_mfma_f32_16x16x32_bf16 v[236:239], v[244:247], v[104:107], v[236:239]\n\t"
    "v_mfma_f32_16x16x32_bf16 a[84:87], a[228:231], v[80:83], a[84:87]\n\t"
    "ds_read_b64_tr_b16 a[254:255], v199 offset:44032\n\t"
    "v_mfma_f32_16x16x32_bf16 a[80:83], a[228:231], v[64:67], a[80:83]\n\t"
    "s_waitcnt lgkmcnt(8)\n\t"
    "v_mfma_f32_16x16x32_bf16 a[96:99], a[232:235], v[64:67], a[96:99]\n\t"
    "ds_read_b64_tr_b16 a[224:225], v196 offset:26624\n\t"
    "v_mfma_f32_16x16x32_bf16 a[100:103], a[232:235], v[80:83], a[100:103]\n\t"
    "v_mfma_f32_16x16x32_bf16 v[240:243], v[244:247], v[120:123], v[240:243]\n\t"
    "v_mfma_f32_16x16x32_bf16 a[104:107], a[232:235], v[96:99], a[104:107]\n\t"
    "ds_read_b64_tr_b16 a[226:227], v196 offset:32768\n\t"
    "v_mfma_f32_16x16x32_bf16 a[108:111], a[232:235], v[112:115], a[108:111]\n\t"
    "v_mfma_f32_16x16x32_bf16 a[124:127], a[236:239], v[112:115], a[124:127]\n\t"
    "ds_read_b64_tr_b16 a[228:229], v197 offset:26624\n\t"
    "v_mfma_f32_16x16x32_bf16 a[120:123], a[236:239], v[96:99], a[120:123]\n\t"
    "v_mfma_f32_16x16x32_bf16 a[116:119], a[236:239], v[80:83], a[116:119]\n\t"
    "ds_read_b64_tr_b16 a[230:231], v197 offset:32768\n\t"
    "v_mfma_f32_16x16x32_bf16 a[112:115], a[236:239], v[64:67], a[112:115]\n\t"
    "s_waitcnt lgkmcnt(8)\n\t"
    "v_mfma_f32_16x16x32_bf16 a[64:67], a[240:243], v[72:75], a[64:67]\n\t"
    "ds_read_b64_tr_b16 a[232:233], v198 offset:26624\n\t"
    "v_mfma_f32_16x16x32_bf16 a[68:71], a[240:243], v[88:91], a[68:71]\n\t"
    "v_mfma_f32_16x16x32_bf16 a[72:75], a[240:243], v[104:107], a[72:75]\n\t"
    "ds_read_b64_tr_b16 a[234:235], v198 offset:32768\n\t"
    "v_mfma_f32_16x16x32_bf16 a[76:79], a[240:243], v[120:123], a[76:79]\n\t"
    "v_mfma_f32_16x16x32_bf16 a[92:95], a[244:247], v[120:123], a[92:95]\n\t"
    "ds_read_b64_tr_b16 a[236:237], v199 offset:26624\n\t"
    "v_mfma_f32_16x16x32_bf16 a[88:91], a[244:247], v[104:107], a[88:91]\n\t"
    "v_mfma_f32_16x16x32_bf16 a[84:87], a[244:247], v[88:91], a[84:87]\n\t"
    "ds_read_b64_tr_b16 a[238:239], v199 offset:32768\n\t"
    "v_mfma_f32_16x16x32_bf16 a[80:83], a[244:247], v[72:75], a[80:83]\n\t"
    "s_waitcnt lgkmcnt(8)\n\t"
    "v_mfma_f32_16x16x32_bf16 a[96:99], a[248:251], v[72:75], a[96:99]\n\t"
    "ds_read_b64_tr_b16 a[240:241], v196 offset:38912\n\t"
    "v_mfma_f32_16x16x32_bf16 a[100:103], a[248:251], v[88:91], a[100:103]\n\t"
    "v_mfma_f32_16x16x32_bf16 a[104:107], a[248:251], v[104:107], a[104:107]\n\t"
    "ds_read_b64_tr_b16 a[242:243], v196 offset:45056\n\t"
    "v_mfma_f32_16x16x32_bf16 a[108:111], a[248:251], v[120:123], a[108:111]\n\t"
    "v_mfma_f32_16x16x32_bf16 a[124:127], a[252:255], v[120:123], a[124:127]\n\t"
    "ds_read_b64_tr_b16 a[244:245], v197 offset:38912\n\t"
    "v_mfma_f32_16x16x32_bf16 a[120:123], a[252:255], v[104:107], a[120:123]\n\t"
    "v_mfma_f32_16x16x32_bf16 a[116:119], a[252:255], v[88:91], a[116:119]\n\t"
    "ds_read_b64_tr_b16 a[246:247], v197 offset:45056\n\t"
    "v_mfma_f32_16x16x32_bf16 a[112:115], a[252:255], v[72:75], a[112:115]\n\t"
    "s_waitcnt lgkmcnt(8)\n\t"
    "v_mfma_f32_16x16x32_bf16 v[128:131], a[224:227], v[64:67], v[128:131]\n\t"
    "ds_read_b64_tr_b16 a[248:249], v198 offset:38912\n\t"
    "v_mfma_f32_16x16x32_bf16 v[132:135], a[224:227], v[80:83], v[132:135]\n\t"
    "v_mfma_f32_16x16x32_bf16 v[136:139], a[224:227], v[96:99], v[136:139]\n\t"
    "ds_read_b64_tr_b16 a[250:251], v198 offset:45056\n\t"
    "v_mfma_f32_16x16x32_bf16 v[140:143], a[224:227], v[112:115], v[140:143]\n\t"
    "v_mfma_f32_16x16x32_bf16 v[156:159], a[228:231], v[112:115], v[156:159]\n\t"
    "ds_read_b64_tr_b16 a[252:253], v199 offset:38912\n\t"
    "v_mfma_f32_16x16x32_bf16 v[152:155], a[228:231], v[96:99], v[152:155]\n\t"
    "v_mfma_f32_16x16x32_bf16 v[148:151], a[228:231], v[80:83], v[148:151]\n\t"
    "ds_read_b64_tr_b16 a[254:255], v199 offset:45056\n\t"
    "v_mfma_f32_16x16x32_bf16 v[144:147], a[228:231], v[64:67], v[144:147]\n\t"
    "s_waitcnt lgkmcnt(8)\n\t"
    "v_mfma_f32_16x16x32_bf16 v[160:163], a[232:235], v[64:67], v[160:163]\n\t"
    "v_mfma_f32_16x16x32_bf16 v[164:167], a[232:235], v[80:83], v[164:167]\n\t"
    "v_mfma_f32_16x16x32_bf16 v[168:171], a[232:235], v[96:99], v[168:171]\n\t"
    "v_mfma_f32_16x16x32_bf16 v[172:175], a[232:235], v[112:115], v[172:175]\n\t"
    "v_mfma_f32_16x16x32_bf16 v[188:191], a[236:239], v[112:115], v[188:191]\n\t"
    "v_mfma_f32_16x16x32_bf16 v[184:187], a[236:239], v[96:99], v[184:187]\n\t"
    "v_mfma_f32_16x16x32_bf16 v[180:183], a[236:239], v[80:83], v[180:183]\n\t"
    "v_mfma_f32_16x16x32_bf16 v[176:179], a[236:239], v[64:67], v[176:179]\n\t"
    "s_waitcnt lgkmcnt(4)\n\t"
    "v_mfma_f32_16x16x32_bf16 v[128:131], a[240:243], v[72:75], v[128:131]\n\t"
    "v_mfma_f32_16x16x32_bf16 v[132:135], a[240:243], v[88:91], v[132:135]\n\t"
    "v_mfma_f32_16x16x32_bf16 v[136:139], a[240:243], v[104:107], v[136:139]\n\t"
    "v_mfma_f32_16x16x32_bf16 v[140:143], a[240:243], v[120:123], v[140:143]\n\t"
    "v_mfma_f32_16x16x32_bf16 v[156:159], a[244:247], v[120:123], v[156:159]\n\t"
    "v_mfma_f32_16x16x32_bf16 v[152:155], a[244:247], v[104:107], v[152:155]\n\t"
    "v_mfma_f32_16x16x32_bf16 v[148:151], a[244:247], v[88:91], v[148:151]\n\t"
    "v_mfma_f32_16x16x32_bf16 v[144:147], a[244:247], v[72:75], v[144:147]\n\t"
    "s_waitcnt lgkmcnt(0)\n\t"
    "v_mfma_f32_16x16x32_bf16 v[160:163], a[248:251], v[72:75], v[160:163]\n\t"
    "v_mfma_f32_16x16x32_bf16 v[164:167], a[248:251], v[88:91], v[164:167]\n\t"
    "v_mfma_f32_16x16x32_bf16 v[168:171], a[248:251], v[104:107], v[168:171]\n\t"
    "v_mfma_f32_16x16x32_bf16 v[172:175], a[248:251], v[120:123], v[172:175]\n\t"
    "v_mfma_f32_16x16x32_bf16 v[188:191], a[252:255], v[120:123], v[188:191]\n\t"
    "v_mfma_f32_16x16x32_bf16 v[184:187], a[252:255], v[104:107], v[184:187]\n\t"
    "v_mfma_f32_16x16x32_bf16 v[180:183], a[252:255], v[88:91], v[180:183]\n\t"
    "v_mfma_f32_16x16x32_bf16 v[176:179], a[252:255], v[72:75], v[176:179]\n\t"
    "s_add_u32 s55, s55, 25165824\n\t"
    "s_mov_b32 s50, 100663296\n\t"
    "s_add_u32 s69, s69, 1\n\t"
    "s_add_u32 s70, s55, s96\n\t"
    "s_add_u32 s78, s55, s97\n\t"
    "s_add_u32 s79, s55, s98\n\t"
    "s_add_u32 s80, s55, s99\n\t"
    "v_rcp_f32 v224, v228\n\t"
    "v_rcp_f32 v225, v232\n\t"
    "v_rcp_f32 v226, v236\n\t"
    "v_rcp_f32 v227, v240\n\t"
    "s_nop 7\n\t"
    "v_accvgpr_read_b32 v64, a0\n\t"
    "v_accvgpr_read_b32 v65, a1\n\t"
    "v_accvgpr_read_b32 v66, a2\n\t"
    "v_accvgpr_read_b32 v67, a3\n\t"
    "v_accvgpr_read_b32 v68, a4\n\t"
    "v_accvgpr_read_b32 v69, a5\n\t"
    "v_accvgpr_read_b32 v70, a6\n\t"
    "v_accvgpr_read_b32 v71, a7\n\t"
    "v_pk_mul_f32 v[64:65], v[64:65], v[224:225] op_sel:[0,0] op_sel_hi:[1,0]\n\t"
    "v_pk_mul_f32 v[66:67], v[66:67], v[224:225] op_sel:[0,0] op_sel_hi:[1,0]\n\t"
    "v_pk_mul_f32 v[68:69], v[68:69], v[224:225] op_sel:[0,1] op_sel_hi:[1,1]\n\t"
    "v_pk_mul_f32 v[70:71], v[70:71], v[224:225] op_sel:[0,1] op_sel_hi:[1,1]\n\t"
    "v_cvt_pk_bf16_f32 v64, v64, v65\n\t"
    "v_cvt_pk_bf16_f32 v65, v66, v67\n\t"
    "v_cvt_pk_bf16_f32 v66, v68, v69\n\t"
    "v_cvt_pk_bf16_f32 v67, v70, v71\n\t"
    "ds_write2st64_b64 v208, v[64:65], v[66:67] offset0:16 offset1:20\n\t"
    "v_accvgpr_read_b32 v72, a16\n\t"
    "v_accvgpr_read_b32 v73, a17\n\t"
    "v_accvgpr_read_b32 v74, a18\n\t"
    "v_accvgpr_read_b32 v75, a19\n\t"
    "v_accvgpr_read_b32 v76, a20\n\t"
    "v_accvgpr_read_b32 v77, a21\n\t"
    "v_accvgpr_read_b32 v78, a22\n\t"
    "v_accvgpr_read_b32 v79, a23\n\t"
    "v_pk_mul_f32 v[72:73], v[72:73], v[224:225] op_sel:[0,0] op_sel_hi:[1,0]\n\t"
    "v_pk_mul_f32 v[74:75], v[74:75], v[224:225] op_sel:[0,0] op_sel_hi:[1,0]\n\t"
    "v_pk_mul_f32 v[76:77], v[76:77], v[224:225] op_sel:[0,1] op_sel_hi:[1,1]\n\t"
    "v_pk_mul_f32 v[78:79], v[78:79], v[224:225] op_sel:[0,1] op_sel_hi:[1,1]\n\t"
    "v_cvt_pk_bf16_f32 v72, v72, v73\n\t"
    "v_cvt_pk_bf16_f32 v73, v74, v75\n\t"
    "v_cvt_pk_bf16_f32 v74, v76, v77\n\t"
    "v_cvt_pk_bf16_f32 v75, v78, v79\n\t"
    "ds_write2st64_b64 v209, v[72:73], v[74:75] offset0:16 offset1:20\n\t"
    "v_accvgpr_read_b32 v80, a32\n\t"
    "v_accvgpr_read_b32 v81, a33\n\t"
    "v_accvgpr_read_b32 v82, a34\n\t"
    "v_accvgpr_read_b32 v83, a35\n\t"
    "v_accvgpr_read_b32 v84, a36\n\t"
    "v_accvgpr_read_b32 v85, a37\n\t"
    "v_accvgpr_read_b32 v86, a38\n\t"
    "v_accvgpr_read_b32 v87, a39\n\t"
    "v_pk_mul_f32 v[80:81], v[80:81], v[224:225] op_sel:[0,0] op_sel_hi:[1,0]\n\t"
    "v_pk_mul_f32 v[82:83], v[82:83], v[224:225] op_sel:[0,0] op_sel_hi:[1,0]\n\t"
    "v_pk_mul_f32 v[84:85], v[84:85], v[224:225] op_sel:[0,1] op_sel_hi:[1,1]\n\t"
    "v_pk_mul_f32 v[86:87], v[86:87], v[224:225] op_sel:[0,1] op_sel_hi:[1,1]\n\t"
    "v_cvt_pk_bf16_f32 v80, v80, v81\n\t"
    "v_cvt_pk_bf16_f32 v81, v82, v83\n\t"
    "v_cvt_pk_bf16_f32 v82, v84, v85\n\t"
    "v_cvt_pk_bf16_f32 v83, v86, v87\n\t"
    "ds_write2st64_b64 v210, v[80:81], v[82:83] offset0:16 offset1:20\n\t"
    "v_accvgpr_read_b32 v88, a48\n\t"
    "v_accvgpr_read_b32 v89, a49\n\t"
    "v_accvgpr_read_b32 v90, a50\n\t"
    "v_accvgpr_read_b32 v91, a51\n\t"
    "v_accvgpr_read_b32 v92, a52\n\t"
    "v_accvgpr_read_b32 v93, a53\n\t"
    "v_accvgpr_read_b32 v94, a54\n\t"
    "v_accvgpr_read_b32 v95, a55\n\t"
    "v_pk_mul_f32 v[88:89], v[88:89], v[224:225] op_sel:[0,0] op_sel_hi:[1,0]\n\t"
    "v_pk_mul_f32 v[90:91], v[90:91], v[224:225] op_sel:[0,0] op_sel_hi:[1,0]\n\t"
    "v_pk_mul_f32 v[92:93], v[92:93], v[224:225] op_sel:[0,1] op_sel_hi:[1,1]\n\t"
    "v_pk_mul_f32 v[94:95], v[94:95], v[224:225] op_sel:[0,1] op_sel_hi:[1,1]\n\t"
    "v_cvt_pk_bf16_f32 v88, v88, v89\n\t"
    "v_cvt_pk_bf16_f32 v89, v90, v91\n\t"
    "v_cvt_pk_bf16_f32 v90, v92, v93\n\t"
    "v_cvt_pk_bf16_f32 v91, v94, v95\n\t"
    "ds_write2st64_b64 v211, v[88:89], v[90:91] offset0:16 offset1:20\n\t"
    "ds_read_b128 v[96:99], v203 offset:8192\n\t"
    "ds_read_b128 v[100:103], v203 offset:9216\n\t"
    "ds_read_b128 v[104:107], v203 offset:10240\n\t"
    "ds_read_b128 v[108:111], v203 offset:11264\n\t"
    "v_accvgpr_read_b32 v64, a8\n\t"
    "v_accvgpr_read_b32 v65, a9\n\t"
    "v_accvgpr_read_b32 v66, a10\n\t"
    "v_accvgpr_read_b32 v67, a11\n\t"
    "v_accvgpr_read_b32 v68, a12\n\t"
    "v_accvgpr_read_b32 v69, a13\n\t"
    "v_accvgpr_read_b32 v70, a14\n\t"
    "v_accvgpr_read_b32 v71, a15\n\t"
    "v_pk_mul_f32 v[64:65], v[64:65], v[226:227] op_sel:[0,0] op_sel_hi:[1,0]\n\t"
    "v_pk_mul_f32 v[66:67], v[66:67], v[226:227] op_sel:[0,0] op_sel_hi:[1,0]\n\t"
    "v_pk_mul_f32 v[68:69], v[68:69], v[226:227] op_sel:[0,1] op_sel_hi:[1,1]\n\t"
    "v_pk_mul_f32 v[70:71], v[70:71], v[226:227] op_sel:[0,1] op_sel_hi:[1,1]\n\t"
    "v_cvt_pk_bf16_f32 v64, v64, v65\n\t"
    "v_cvt_pk_bf16_f32 v65, v66, v67\n\t"
    "v_cvt_pk_bf16_f32 v66, v68, v69\n\t"
    "v_cvt_pk_bf16_f32 v67, v70, v71\n\t"
    "ds_write2st64_b64 v208, v[64:65], v[66:67] offset0:24 offset1:28\n\t"
    "s_waitcnt lgkmcnt(4)\n\t"
    "buffer_store_dwordx4 v[96:99], v200, s[48:51], s70 offen offset:0 nt\n\t"
    "v_accvgpr_read_b32 v72, a24\n\t"
    "v_accvgpr_read_b32 v73, a25\n\t"
    "v_accvgpr_read_b32 v74, a26\n\t"
    "v_accvgpr_read_b32 v75, a27\n\t"
    "v_accvgpr_read_b32 v76, a28\n\t"
    "v_accvgpr_read_b32 v77, a29\n\t"
    "v_accvgpr_read_b32 v78, a30\n\t"
    "v_accvgpr_read_b32 v79, a31\n\t"
    "v_pk_mul_f32 v[72:73], v[72:73], v[226:227] op_sel:[0,0] op_sel_hi:[1,0]\n\t"
    "v_pk_mul_f32 v[74:75], v[74:75], v[226:227] op_sel:[0,0] op_sel_hi:[1,0]\n\t"
    "v_pk_mul_f32 v[76:77], v[76:77], v[226:227] op_sel:[0,1] op_sel_hi:[1,1]\n\t"
    "v_pk_mul_f32 v[78:79], v[78:79], v[226:227] op_sel:[0,1] op_sel_hi:[1,1]\n\t"
    "v_cvt_pk_bf16_f32 v72, v72, v73\n\t"
    "v_cvt_pk_bf16_f32 v73, v74, v75\n\t"
    "v_cvt_pk_bf16_f32 v74, v76, v77\n\t"
    "v_cvt_pk_bf16_f32 v75, v78, v79\n\t"
    "ds_write2st64_b64 v209, v[72:73], v[74:75] offset0:24 offset1:28\n\t"
    "s_waitcnt lgkmcnt(4)\n\t"
    "buffer_store_dwordx4 v[100:103], v201, s[48:51], s70 offen offset:3072 nt\n\t"
    "v_accvgpr_read_b32 v80, a40\n\t"
    "v_accvgpr_read_b32 v81, a41\n\t"
    "v_accvgpr_read_b32 v82, a42\n\t"
    "v_accvgpr_read_b32 v83, a43\n\t"
    "v_accvgpr_read_b32 v84, a44\n\t"
    "v_accvgpr_read_b32 v85, a45\n\t"
    "v_accvgpr_read_b32 v86, a46\n\t"
    "v_accvgpr_read_b32 v87, a47\n\t"
    "v_pk_mul_f32 v[80:81], v[80:81], v[226:227] op_sel:[0,0] op_sel_hi:[1,0]\n\t"
    "v_pk_mul_f32 v[82:83], v[82:83], v[226:227] op_sel:[0,0] op_sel_hi:[1,0]\n\t"
    "v_pk_mul_f32 v[84:85], v[84:85], v[226:227] op_sel:[0,1] op_sel_hi:[1,1]\n\t"
    "v_pk_mul_f32 v[86:87], v[86:87], v[226:227] op_sel:[0,1] op_sel_hi:[1,1]\n\t"
    "v_cvt_pk_bf16_f32 v80, v80, v81\n\t"
    "v_cvt_pk_bf16_f32 v81, v82, v83\n\t"
    "v_cvt_pk_bf16_f32 v82, v84, v85\n\t"
    "v_cvt_pk_bf16_f32 v83, v86, v87\n\t"
    "ds_write2st64_b64 v210, v[80:81], v[82:83] offset0:24 offset1:28\n\t"
    "s_waitcnt lgkmcnt(4)\n\t"
    "buffer_store_dwordx4 v[104:107], v200, s[48:51], s78 offen offset:0 nt\n\t"
    "v_accvgpr_read_b32 v88, a56\n\t"
    "v_accvgpr_read_b32 v89, a57\n\t"
    "v_accvgpr_read_b32 v90, a58\n\t"
    "v_accvgpr_read_b32 v91, a59\n\t"
    "v_accvgpr_read_b32 v92, a60\n\t"
    "v_accvgpr_read_b32 v93, a61\n\t"
    "v_accvgpr_read_b32 v94, a62\n\t"
    "v_accvgpr_read_b32 v95, a63\n\t"
    "v_pk_mul_f32 v[88:89], v[88:89], v[226:227] op_sel:[0,0] op_sel_hi:[1,0]\n\t"
    "v_pk_mul_f32 v[90:91], v[90:91], v[226:227] op_sel:[0,0] op_sel_hi:[1,0]\n\t"
    "v_pk_mul_f32 v[92:93], v[92:93], v[226:227] op_sel:[0,1] op_sel_hi:[1,1]\n\t"
    "v_pk_mul_f32 v[94:95], v[94:95], v[226:227] op_sel:[0,1] op_sel_hi:[1,1]\n\t"
    "v_cvt_pk_bf16_f32 v88, v88, v89\n\t"
    "v_cvt_pk_bf16_f32 v89, v90, v91\n\t"
    "v_cvt_pk_bf16_f32 v90, v92, v93\n\t"
    "v_cvt_pk_bf16_f32 v91, v94, v95\n\t"
    "ds_write2st64_b64 v211, v[88:89], v[90:91] offset0:24 offset1:28\n\t"
    "s_waitcnt lgkmcnt(4)\n\t"
    "buffer_store_dwordx4 v[108:111], v201, s[48:51], s78 offen offset:3072 nt\n\t"
    "ds_read_b128 v[112:115], v203 offset:12288\n\t"
    "ds_read_b128 v[116:119], v203 offset:13312\n\t"
    "ds_read_b128 v[120:123], v203 offset:14336\n\t"
    "ds_read_b128 v[124:127], v203 offset:15360\n\t"
    "v_accvgpr_read_b32 v64, a64\n\t"
    "v_accvgpr_read_b32 v65, a65\n\t"
    "v_accvgpr_read_b32 v66, a66\n\t"
    "v_accvgpr_read_b32 v67, a67\n\t"
    "v_accvgpr_read_b32 v68, a68\n\t"
    "v_accvgpr_read_b32 v69, a69\n\t"
    "v_accvgpr_read_b32 v70, a70\n\t"
    "v_accvgpr_read_b32 v71, a71\n\t"
    "v_pk_mul_f32 v[64:65], v[64:65], v[224:225] op_sel:[0,0] op_sel_hi:[1,0]\n\t"
    "v_pk_mul_f32 v[66:67], v[66:67], v[224:225] op_sel:[0,0] op_sel_hi:[1,0]\n\t"
    "v_pk_mul_f32 v[68:69], v[68:69], v[224:225] op_sel:[0,1] op_sel_hi:[1,1]\n\t"
    "v_pk_mul_f32 v[70:71], v[70:71], v[224:225] op_sel:[0,1] op_sel_hi:[1,1]\n\t"
    "v_cvt_pk_bf16_f32 v64, v64, v65\n\t"
    "v_cvt_pk_bf16_f32 v65, v66, v67\n\t"
    "v_cvt_pk_bf16_f32 v66, v68, v69\n\t"
    "v_cvt_pk_bf16_f32 v67, v70, v71\n\t"
    "ds_write2st64_b64 v208, v[64:65], v[66:67] offset0:0 offset1:4\n\t"
    "s_waitcnt lgkmcnt(4)\n\t"
    "buffer_store_dwordx4 v[112:115], v200, s[48:51], s79 offen offset:0 nt\n\t"
    "v_accvgpr_read_b32 v72, a80\n\t"
    "v_accvgpr_read_b32 v73, a81\n\t"
    "v_accvgpr_read_b32 v74, a82\n\t"
    "v_accvgpr_read_b32 v75, a83\n\t"
    "v_accvgpr_read_b32 v76, a84\n\t"
    "v_accvgpr_read_b32 v77, a85\n\t"
    "v_accvgpr_read_b32 v78, a86\n\t"
    "v_accvgpr_read_b32 v79, a87\n\t"
    "v_pk_mul_f32 v[72:73], v[72:73], v[224:225] op_sel:[0,0] op_sel_hi:[1,0]\n\t"
    "v_pk_mul_f32 v[74:75], v[74:75], v[224:225] op_sel:[0,0] op_sel_hi:[1,0]\n\t"
    "v_pk_mul_f32 v[76:77], v[76:77], v[224:225] op_sel:[0,1] op_sel_hi:[1,1]\n\t"
    "v_pk_mul_f32 v[78:79], v[78:79], v[224:225] op_sel:[0,1] op_sel_hi:[1,1]\n\t"
    "v_cvt_pk_bf16_f32 v72, v72, v73\n\t"
    "v_cvt_pk_bf16_f32 v73, v74, v75\n\t"
    "v_cvt_pk_bf16_f32 v74, v76, v77\n\t"
    "v_cvt_pk_bf16_f32 v75, v78, v79\n\t"
    "ds_write2st64_b64 v209, v[72:73], v[74:75] offset0:0 offset1:4\n\t"
    "s_waitcnt lgkmcnt(4)\n\t"
    "buffer_store_dwordx4 v[116:119], v201, s[48:51], s79 offen offset:3072 nt\n\t"
    "v_accvgpr_read_b32 v80, a96\n\t"
    "v_accvgpr_read_b32 v81, a97\n\t"
    "v_accvgpr_read_b32 v82, a98\n\t"
    "v_accvgpr_read_b32 v83, a99\n\t"
    "v_accvgpr_read_b32 v84, a100\n\t"
    "v_accvgpr_read_b32 v85, a101\n\t"
    "v_accvgpr_read_b32 v86, a102\n\t"
    "v_accvgpr_read_b32 v87, a103\n\t"
    "v_pk_mul_f32 v[80:81], v[80:81], v[224:225] op_sel:[0,0] op_sel_hi:[1,0]\n\t"
    "v_pk_mul_f32 v[82:83], v[82:83], v[224:225] op_sel:[0,0] op_sel_hi:[1,0]\n\t"
    "v_pk_mul_f32 v[84:85], v[84:85], v[224:225] op_sel:[0,1] op_sel_hi:[1,1]\n\t"
    "v_pk_mul_f32 v[86:87], v[86:87], v[224:225] op_sel:[0,1] op_sel_hi:[1,1]\n\t"
    "v_cvt_pk_bf16_f32 v80, v80, v81\n\t"
    "v_cvt_pk_bf16_f32 v81, v82, v83\n\t"
    "v_cvt_pk_bf16_f32 v82, v84, v85\n\t"
    "v_cvt_pk_bf16_f32 v83, v86, v87\n\t"
    "ds_write2st64_b64 v210, v[80:81], v[82:83] offset0:0 offset1:4\n\t"
    "s_waitcnt lgkmcnt(4)\n\t"
    "buffer_store_dwordx4 v[120:123], v200, s[48:51], s80 offen offset:0 nt\n\t"
    "v_accvgpr_read_b32 v88, a112\n\t"
    "v_accvgpr_read_b32 v89, a113\n\t"
    "v_accvgpr_read_b32 v90, a114\n\t"
    "v_accvgpr_read_b32 v91, a115\n\t"
    "v_accvgpr_read_b32 v92, a116\n\t"
    "v_accvgpr_read_b32 v93, a117\n\t"
    "v_accvgpr_read_b32 v94, a118\n\t"
    "v_accvgpr_read_b32 v95, a119\n\t"
    "v_pk_mul_f32 v[88:89], v[88:89], v[224:225] op_sel:[0,0] op_sel_hi:[1,0]\n\t"
    "v_pk_mul_f32 v[90:91], v[90:91], v[224:225] op_sel:[0,0] op_sel_hi:[1,0]\n\t"
    "v_pk_mul_f32 v[92:93], v[92:93], v[224:225] op_sel:[0,1] op_sel_hi:[1,1]\n\t"
    "v_pk_mul_f32 v[94:95], v[94:95], v[224:225] op_sel:[0,1] op_sel_hi:[1,1]\n\t"
    "v_cvt_pk_bf16_f32 v88, v88, v89\n\t"
    "v_cvt_pk_bf16_f32 v89, v90, v91\n\t"
    "v_cvt_pk_bf16_f32 v90, v92, v93\n\t"
    "v_cvt_pk_bf16_f32 v91, v94, v95\n\t"
    "ds_write2st64_b64 v211, v[88:89], v[90:91] offset0:0 offset1:4\n\t"
    "s_waitcnt lgkmcnt(4)\n\t"
    "buffer_store_dwordx4 v[124:127], v201, s[48:51], s80 offen offset:3072 nt\n\t"
    "ds_read_b128 v[96:99], v203 offset:0\n\t"
    "ds_read_b128 v[100:103], v203 offset:1024\n\t"
    "ds_read_b128 v[104:107], v203 offset:2048\n\t"
    "ds_read_b128 v[108:111], v203 offset:3072\n\t"
    "v_accvgpr_read_b32 v64, a72\n\t"
    "v_accvgpr_read_b32 v65, a73\n\t"
    "v_accvgpr_read_b32 v66, a74\n\t"
    "v_accvgpr_read_b32 v67, a75\n\t"
    "v_accvgpr_read_b32 v68, a76\n\t"
    "v_accvgpr_read_b32 v69, a77\n\t"
    "v_accvgpr_read_b32 v70, a78\n\t"
    "v_accvgpr_read_b32 v71, a79\n\t"
    "v_pk_mul_f32 v[64:65], v[64:65], v[226:227] op_sel:[0,0] op_sel_hi:[1,0]\n\t"
    "v_pk_mul_f32 v[66:67], v[66:67], v[226:227] op_sel:[0,0] op_sel_hi:[1,0]\n\t"
    "v_pk_mul_f32 v[68:69], v[68:69], v[226:227] op_sel:[0,1] op_sel_hi:[1,1]\n\t"
    "v_pk_mul_f32 v[70:71], v[70:71], v[226:227] op_sel:[0,1] op_sel_hi:[1,1]\n\t"
    "v_cvt_pk_bf16_f32 v64, v64, v65\n\t"
    "v_cvt_pk_bf16_f32 v65, v66, v67\n\t"
    "v_cvt_pk_bf16_f32 v66, v68, v69\n\t"
    "v_cvt_pk_bf16_f32 v67, v70, v71\n\t"
    "ds_write2st64_b64 v208, v[64:65], v[66:67] offset0:8 offset1:12\n\t"
    "s_waitcnt lgkmcnt(4)\n\t"
    "buffer_store_dwordx4 v[96:99], v200, s[48:51], s70 offen offset:128 nt\n\t"
    "v_accvgpr_read_b32 v72, a88\n\t"
    "v_accvgpr_read_b32 v73, a89\n\t"
    "v_accvgpr_read_b32 v74, a90\n\t"
    "v_accvgpr_read_b32 v75, a91\n\t"
    "v_accvgpr_read_b32 v76, a92\n\t"
    "v_accvgpr_read_b32 v77, a93\n\t"
    "v_accvgpr_read_b32 v78, a94\n\t"
    "v_accvgpr_read_b32 v79, a95\n\t"
    "v_pk_mul_f32 v[72:73], v[72:73], v[226:227] op_sel:[0,0] op_sel_hi:[1,0]\n\t"
    "v_pk_mul_f32 v[74:75], v[74:75], v[226:227] op_sel:[0,0] op_sel_hi:[1,0]\n\t"
    "v_pk_mul_f32 v[76:77], v[76:77], v[226:227] op_sel:[0,1] op_sel_hi:[1,1]\n\t"
    "v_pk_mul_f32 v[78:79], v[78:79], v[226:227] op_sel:[0,1] op_sel_hi:[1,1]\n\t"
    "v_cvt_pk_bf16_f32 v72, v72, v73\n\t"
    "v_cvt_pk_bf16_f32 v73, v74, v75\n\t"
    "v_cvt_pk_bf16_f32 v74, v76, v77\n\t"
    "v_cvt_pk_bf16_f32 v75, v78, v79\n\t"
    "ds_write2st64_b64 v209, v[72:73], v[74:75] offset0:8 offset1:12\n\t"
    "s_waitcnt lgkmcnt(4)\n\t"
    "buffer_store_dwordx4 v[100:103], v201, s[48:51], s70 offen offset:3200 nt\n\t"
    "v_accvgpr_read_b32 v80, a104\n\t"
    "v_accvgpr_read_b32 v81, a105\n\t"
    "v_accvgpr_read_b32 v82, a106\n\t"
    "v_accvgpr_read_b32 v83, a107\n\t"
    "v_accvgpr_read_b32 v84, a108\n\t"
    "v_accvgpr_read_b32 v85, a109\n\t"
    "v_accvgpr_read_b32 v86, a110\n\t"
    "v_accvgpr_read_b32 v87, a111\n\t"
    "v_pk_mul_f32 v[80:81], v[80:81], v[226:227] op_sel:[0,0] op_sel_hi:[1,0]\n\t"
    "v_pk_mul_f32 v[82:83], v[82:83], v[226:227] op_sel:[0,0] op_sel_hi:[1,0]\n\t"
    "v_pk_mul_f32 v[84:85], v[84:85], v[226:227] op_sel:[0,1] op_sel_hi:[1,1]\n\t"
    "v_pk_mul_f32 v[86:87], v[86:87], v[226:227] op_sel:[0,1] op_sel_hi:[1,1]\n\t"
    "v_cvt_pk_bf16_f32 v80, v80, v81\n\t"
    "v_cvt_pk_bf16_f32 v81, v82, v83\n\t"
    "v_cvt_pk_bf16_f32 v82, v84, v85\n\t"
    "v_cvt_pk_bf16_f32 v83, v86, v87\n\t"
    "ds_write2st64_b64 v210, v[80:81], v[82:83] offset0:8 offset1:12\n\t"
    "s_waitcnt lgkmcnt(4)\n\t"
    "buffer_store_dwordx4 v[104:107], v200, s[48:51], s78 offen offset:128 nt\n\t"
    "v_accvgpr_read_b32 v88, a120\n\t"
    "v_accvgpr_read_b32 v89, a121\n\t"
    "v_accvgpr_read_b32 v90, a122\n\t"
    "v_accvgpr_read_b32 v91, a123\n\t"
    "v_accvgpr_read_b32 v92, a124\n\t"
    "v_accvgpr_read_b32 v93, a125\n\t"
    "v_accvgpr_read_b32 v94, a126\n\t"
    "v_accvgpr_read_b32 v95, a127\n\t"
    "v_pk_mul_f32 v[88:89], v[88:89], v[226:227] op_sel:[0,0] op_sel_hi:[1,0]\n\t"
    "v_pk_mul_f32 v[90:91], v[90:91], v[226:227] op_sel:[0,0] op_sel_hi:[1,0]\n\t"
    "v_pk_mul_f32 v[92:93], v[92:93], v[226:227] op_sel:[0,1] op_sel_hi:[1,1]\n\t"
    "v_pk_mul_f32 v[94:95], v[94:95], v[226:227] op_sel:[0,1] op_sel_hi:[1,1]\n\t"
    "v_cvt_pk_bf16_f32 v88, v88, v89\n\t"
    "v_cvt_pk_bf16_f32 v89, v90, v91\n\t"
    "v_cvt_pk_bf16_f32 v90, v92, v93\n\t"
    "v_cvt_pk_bf16_f32 v91, v94, v95\n\t"
    "ds_write2st64_b64 v211, v[88:89], v[90:91] offset0:8 offset1:12\n\t"
    "s_waitcnt lgkmcnt(4)\n\t"
    "buffer_store_dwordx4 v[108:111], v201, s[48:51], s78 offen offset:3200 nt\n\t"
    "ds_read_b128 v[112:115], v203 offset:4096\n\t"
    "ds_read_b128 v[116:119], v203 offset:5120\n\t"
    "ds_read_b128 v[120:123], v203 offset:6144\n\t"
    "ds_read_b128 v[124:127], v203 offset:7168\n\t"
    "v_pk_mul_f32 v[64:65], v[128:129], v[224:225] op_sel:[0,0] op_sel_hi:[1,0]\n\t"
    "v_pk_mul_f32 v[66:67], v[130:131], v[224:225] op_sel:[0,0] op_sel_hi:[1,0]\n\t"
    "v_pk_mul_f32 v[68:69], v[132:133], v[224:225] op_sel:[0,1] op_sel_hi:[1,1]\n\t"
    "v_pk_mul_f32 v[70:71], v[134:135], v[224:225] op_sel:[0,1] op_sel_hi:[1,1]\n\t"
    "v_cvt_pk_bf16_f32 v64, v64, v65\n\t"
    "v_cvt_pk_bf16_f32 v65, v66, v67\n\t"
    "v_cvt_pk_bf16_f32 v66, v68, v69\n\t"
    "v_cvt_pk_bf16_f32 v67, v70, v71\n\t"
    "ds_write2st64_b64 v208, v[64:65], v[66:67] offset0:16 offset1:20\n\t"
    "s_waitcnt lgkmcnt(4)\n\t"
    "buffer_store_dwordx4 v[112:115], v200, s[48:51], s79 offen offset:128 nt\n\t"
    "v_pk_mul_f32 v[72:73], v[144:145], v[224:225] op_sel:[0,0] op_sel_hi:[1,0]\n\t"
    "v_pk_mul_f32 v[74:75], v[146:147], v[224:225] op_sel:[0,0] op_sel_hi:[1,0]\n\t"
    "v_pk_mul_f32 v[76:77], v[148:149], v[224:225] op_sel:[0,1] op_sel_hi:[1,1]\n\t"
    "v_pk_mul_f32 v[78:79], v[150:151], v[224:225] op_sel:[0,1] op_sel_hi:[1,1]\n\t"
    "v_cvt_pk_bf16_f32 v72, v72, v73\n\t"
    "v_cvt_pk_bf16_f32 v73, v74, v75\n\t"
    "v_cvt_pk_bf16_f32 v74, v76, v77\n\t"
    "v_cvt_pk_bf16_f32 v75, v78, v79\n\t"
    "ds_write2st64_b64 v209, v[72:73], v[74:75] offset0:16 offset1:20\n\t"
    "s_waitcnt lgkmcnt(4)\n\t"
    "buffer_store_dwordx4 v[116:119], v201, s[48:51], s79 offen offset:3200 nt\n\t"
    "v_pk_mul_f32 v[80:81], v[160:161], v[224:225] op_sel:[0,0] op_sel_hi:[1,0]\n\t"
    "v_pk_mul_f32 v[82:83], v[162:163], v[224:225] op_sel:[0,0] op_sel_hi:[1,0]\n\t"
    "v_pk_mul_f32 v[84:85], v[164:165], v[224:225] op_sel:[0,1] op_sel_hi:[1,1]\n\t"
    "v_pk_mul_f32 v[86:87], v[166:167], v[224:225] op_sel:[0,1] op_sel_hi:[1,1]\n\t"
    "v_cvt_pk_bf16_f32 v80, v80, v81\n\t"
    "v_cvt_pk_bf16_f32 v81, v82, v83\n\t"
    "v_cvt_pk_bf16_f32 v82, v84, v85\n\t"
    "v_cvt_pk_bf16_f32 v83, v86, v87\n\t"
    "ds_write2st64_b64 v210, v[80:81], v[82:83] offset0:16 offset1:20\n\t"
    "s_waitcnt lgkmcnt(4)\n\t"
    "buffer_store_dwordx4 v[120:123], v200, s[48:51], s80 offen offset:128 nt\n\t"
    "v_pk_mul_f32 v[88:89], v[176:177], v[224:225] op_sel:[0,0] op_sel_hi:[1,0]\n\t"
    "v_pk_mul_f32 v[90:91], v[178:179], v[224:225] op_sel:[0,0] op_sel_hi:[1,0]\n\t"
    "v_pk_mul_f32 v[92:93], v[180:181], v[224:225] op_sel:[0,1] op_sel_hi:[1,1]\n\t"
    "v_pk_mul_f32 v[94:95], v[182:183], v[224:225] op_sel:[0,1] op_sel_hi:[1,1]\n\t"
    "v_cvt_pk_bf16_f32 v88, v88, v89\n\t"
    "v_cvt_pk_bf16_f32 v89, v90, v91\n\t"
    "v_cvt_pk_bf16_f32 v90, v92, v93\n\t"
    "v_cvt_pk_bf16_f32 v91, v94, v95\n\t"
    "ds_write2st64_b64 v211, v[88:89], v[90:91] offset0:16 offset1:20\n\t"
    "s_waitcnt lgkmcnt(4)\n\t"
    "buffer_store_dwordx4 v[124:127], v201, s[48:51], s80 offen offset:3200 nt\n\t"
    "ds_read_b128 v[96:99], v203 offset:8192\n\t"
    "ds_read_b128 v[100:103], v203 offset:9216\n\t"
    "ds_read_b128 v[104:107], v203 offset:10240\n\t"
    "ds_read_b128 v[108:111], v203 offset:11264\n\t"
    "v_pk_mul_f32 v[64:65], v[136:137], v[226:227] op_sel:[0,0] op_sel_hi:[1,0]\n\t"
    "v_pk_mul_f32 v[66:67], v[138:139], v[226:227] op_sel:[0,0] op_sel_hi:[1,0]\n\t"
    "v_pk_mul_f32 v[68:69], v[140:141], v[226:227] op_sel:[0,1] op_sel_hi:[1,1]\n\t"
    "v_pk_mul_f32 v[70:71], v[142:143], v[226:227] op_sel:[0,1] op_sel_hi:[1,1]\n\t"
    "v_cvt_pk_bf16_f32 v64, v64, v65\n\t"
    "v_cvt_pk_bf16_f32 v65, v66, v67\n\t"
    "v_cvt_pk_bf16_f32 v66, v68, v69\n\t"
    "v_cvt_pk_bf16_f32 v67, v70, v71\n\t"
    "ds_write2st64_b64 v208, v[64:65], v[66:67] offset0:24 offset1:28\n\t"
    "s_waitcnt lgkmcnt(4)\n\t"
    "buffer_store_dwordx4 v[96:99], v200, s[48:51], s70 offen offset:256 nt\n\t"
    "v_pk_mul_f32 v[72:73], v[152:153], v[226:227] op_sel:[0,0] op_sel_hi:[1,0]\n\t"
    "v_pk_mul_f32 v[74:75], v[154:155], v[226:227] op_sel:[0,0] op_sel_hi:[1,0]\n\t"
    "v_pk_mul_f32 v[76:77], v[156:157], v[226:227] op_sel:[0,1] op_sel_hi:[1,1]\n\t"
    "v_pk_mul_f32 v[78:79], v[158:159], v[226:227] op_sel:[0,1] op_sel_hi:[1,1]\n\t"
    "v_cvt_pk_bf16_f32 v72, v72, v73\n\t"
    "v_cvt_pk_bf16_f32 v73, v74, v75\n\t"
    "v_cvt_pk_bf16_f32 v74, v76, v77\n\t"
    "v_cvt_pk_bf16_f32 v75, v78, v79\n\t"
    "ds_write2st64_b64 v209, v[72:73], v[74:75] offset0:24 offset1:28\n\t"
    "s_waitcnt lgkmcnt(4)\n\t"
    "buffer_store_dwordx4 v[100:103], v201, s[48:51], s70 offen offset:3328 nt\n\t"
    "v_pk_mul_f32 v[80:81], v[168:169], v[226:227] op_sel:[0,0] op_sel_hi:[1,0]\n\t"
    "v_pk_mul_f32 v[82:83], v[170:171], v[226:227] op_sel:[0,0] op_sel_hi:[1,0]\n\t"
    "v_pk_mul_f32 v[84:85], v[172:173], v[226:227] op_sel:[0,1] op_sel_hi:[1,1]\n\t"
    "v_pk_mul_f32 v[86:87], v[174:175], v[226:227] op_sel:[0,1] op_sel_hi:[1,1]\n\t"
    "v_cvt_pk_bf16_f32 v80, v80, v81\n\t"
    "v_cvt_pk_bf16_f32 v81, v82, v83\n\t"
    "v_cvt_pk_bf16_f32 v82, v84, v85\n\t"
    "v_cvt_pk_bf16_f32 v83, v86, v87\n\t"
    "ds_write2st64_b64 v210, v[80:81], v[82:83] offset0:24 offset1:28\n\t"
    "s_waitcnt lgkmcnt(4)\n\t"
    "buffer_store_dwordx4 v[104:107], v200, s[48:51], s78 offen offset:256 nt\n\t"
    "v_pk_mul_f32 v[88:89], v[184:185], v[226:227] op_sel:[0,0] op_sel_hi:[1,0]\n\t"
    "v_pk_mul_f32 v[90:91], v[186:187], v[226:227] op_sel:[0,0] op_sel_hi:[1,0]\n\t"
    "v_pk_mul_f32 v[92:93], v[188:189], v[226:227] op_sel:[0,1] op_sel_hi:[1,1]\n\t"
    "v_pk_mul_f32 v[94:95], v[190:191], v[226:227] op_sel:[0,1] op_sel_hi:[1,1]\n\t"
    "v_cvt_pk_bf16_f32 v88, v88, v89\n\t"
    "v_cvt_pk_bf16_f32 v89, v90, v91\n\t"
    "v_cvt_pk_bf16_f32 v90, v92, v93\n\t"
    "v_cvt_pk_bf16_f32 v91, v94, v95\n\t"
    "ds_write2st64_b64 v211, v[88:89], v[90:91] offset0:24 offset1:28\n\t"
    "s_waitcnt lgkmcnt(4)\n\t"
    "buffer_store_dwordx4 v[108:111], v201, s[48:51], s78 offen offset:3328 nt\n\t"
    "ds_read_b128 v[112:115], v203 offset:12288\n\t"
    "ds_read_b128 v[116:119], v203 offset:13312\n\t"
    "ds_read_b128 v[120:123], v203 offset:14336\n\t"
    "ds_read_b128 v[124:127], v203 offset:15360\n\t"
    "s_waitcnt lgkmcnt(3)\n\t"
    "buffer_store_dwordx4 v[112:115], v200, s[48:51], s79 offen offset:256 nt\n\t"
    "s_waitcnt lgkmcnt(2)\n\t"
    "buffer_store_dwordx4 v[116:119], v201, s[48:51], s79 offen offset:3328 nt\n\t"
    "s_waitcnt lgkmcnt(1)\n\t"
    "buffer_store_dwordx4 v[120:123], v200, s[48:51], s80 offen offset:256 nt\n\t"
    "s_waitcnt lgkmcnt(0)\n\t"
    "buffer_store_dwordx4 v[124:127], v201, s[48:51], s80 offen offset:3328 nt\n\t"
    "v_mov_b32 v224, 0x44400000\n\t"
    "v_mov_b32 v225, 0x44400000\n\t"
    "v_mov_b32 v226, 0x44400000\n\t"
    "v_mov_b32 v227, 0x44400000\n\t"
    "s_nop 1\n\t"
    "s_waitcnt vmcnt(0) lgkmcnt(0)\n\t"
    "s_branch DONE\n\t"
    "SA1:\n\t"
    "s_getpc_b64 s[76:77]\n\t"
    "s_branch RARE_A\n\t"
    "s_branch NSA1\n\t"
    "SB1:\n\t"
    "s_getpc_b64 s[76:77]\n\t"
    "s_branch RARE_B\n\t"
    "s_branch NSB1\n\t"
    "SA2:\n\t"
    "s_getpc_b64 s[76:77]\n\t"
    "s_branch RARE_A\n\t"
    "s_branch NSA2\n\t"
    "SB2:\n\t"
    "s_getpc_b64 s[76:77]\n\t"
    "s_branch RARE_B\n\t"
    "s_branch NSB2\n\t"
    "SA3:\n\t"
    "s_getpc_b64 s[76:77]\n\t"
    "s_branch RARE_A\n\t"
    "s_branch NSA3\n\t"
    "SB3:\n\t"
    "s_getpc_b64 s[76:77]\n\t"
    "s_branch RARE_B\n\t"
    "s_branch NSB3\n\t"
    "SA4:\n\t"
    "s_getpc_b64 s[76:77]\n\t"
    "s_branch RARE_A\n\t"
    "s_branch NSA4\n\t"
    "SB4:\n\t"
    "s_getpc_b64 s[76:77]\n\t"
    "s_branch RARE_B\n\t"
    "s_branch NSB4\n\t"
    "SA5:\n\t"
    "s_getpc_b64 s[76:77]\n\t"
    "s_branch RARE_A\n\t"
    "s_branch NSA5\n\t"
    "SB5:\n\t"
    "s_getpc_b64 s[76:77]\n\t"
    "s_branch RARE_B\n\t"
    "s_branch NSB5\n\t"
    "SA6:\n\t"
    "s_getpc_b64 s[76:77]\n\t"
    "s_branch RARE_A\n\t"
    "s_branch NSA6\n\t"
    "SB6:\n\t"
    "s_getpc_b64 s[76:77]\n\t"
    "s_branch RARE_B\n\t"
    "s_branch NSB6\n\t"
    "SA7:\n\t"
    "s_getpc_b64 s[76:77]\n\t"
    "s_branch RARE_A\n\t"
    "s_branch NSA7\n\t"
    "SB7:\n\t"
    "s_getpc_b64 s[76:77]\n\t"
    "s_branch RARE_B\n\t"
    "s_branch NSB7\n\t"
    "SB7L:\n\t"
    "s_getpc_b64 s[76:77]\n\t"
    "s_branch RARE_B\n\t"
    "s_branch NSB7L\n\t"
    "RARE_A:\n\t"
    "v_mov_b32 v252, v220\n\t"
    "v_mov_b32 v253, v221\n\t"
    "v_mov_b32 v254, v222\n\t"
    "v_mov_b32 v255, v223\n\t"
    "v_permlane32_swap_b32 v220, v252\n\t"
    "v_permlane32_swap_b32 v221, v253\n\t"
    "v_permlane32_swap_b32 v222, v254\n\t"
    "v_permlane32_swap_b32 v223, v255\n\t"
    "v_max_f32 v220, v220, v252\n\t"
    "v_max_f32 v221, v221, v253\n\t"
    "v_max_f32 v222, v222, v254\n\t"
    "v_max_f32 v223, v223, v255\n\t"
    "v_mov_b32 v252, v220\n\t"
    "v_mov_b32 v253, v221\n\t"
    "v_mov_b32 v254, v222\n\t"
    "v_mov_b32 v255, v223\n\t"
    "v_permlane16_swap_b32 v220, v252\n\t"
    "v_permlane16_swap_b32 v221, v253\n\t"
    "v_permlane16_swap_b32 v222, v254\n\t"
    "v_permlane16_swap_b32 v223, v255\n\t"
    "v_max_f32 v220, v220, v252\n\t"
    "v_max_f32 v221, v221, v253\n\t"
    "v_max_f32 v222, v222, v254\n\t"
    "v_max_f32 v223, v223, v255\n\t"
    "v_add_f32 v220, s63, v220\n\t"
    "v_max_f32 v216, v216, v220\n\t"
    "v_mul_f32 v252, s62, v216\n\t"
    "v_sub_f32 v252, 0x41000000, v252\n\t"
    "v_sub_f32 v220, v252, v212\n\t"
    "v_mov_b32 v212, v252\n\t"
    "v_exp_f32 v220, v220\n\t"
    "s_nop 0\n\t"
    "v_add_f32 v221, s63, v221\n\t"
    "v_max_f32 v217, v217, v221\n\t"
    "v_mul_f32 v252, s62, v217\n\t"
    "v_sub_f32 v252, 0x41000000, v252\n\t"
    "v_sub_f32 v221, v252, v213\n\t"
    "v_mov_b32 v213, v252\n\t"
    "v_exp_f32 v221, v221\n\t"
    "s_nop 0\n\t"
    "v_add_f32 v222, s63, v222\n\t"
    "v_max_f32 v218, v218, v222\n\t"
    "v_mul_f32 v252, s62, v218\n\t"
    "v_sub_f32 v252, 0x41000000, v252\n\t"
    "v_sub_f32 v222, v252, v214\n\t"
    "v_mov_b32 v214, v252\n\t"
    "v_exp_f32 v222, v222\n\t"
    "s_nop 0\n\t"
    "v_add_f32 v223, s63, v223\n\t"
    "v_max_f32 v219, v219, v223\n\t"
    "v_mul_f32 v252, s62, v219\n\t"
    "v_sub_f32 v252, 0x41000000, v252\n\t"
    "v_sub_f32 v223, v252, v215\n\t"
    "v_mov_b32 v215, v252\n\t"
    "v_exp_f32 v223, v223\n\t"
    "s_nop 0\n\t"
    "s_mov_b32 s72, 1\n\t"
    "s_add_u32 s76, s76, 4\n\t"
    "s_addc_u32 s77, s77, 0\n\t"
    "s_nop 1\n\t"
    "s_setpc_b64 s[76:77]\n\t"
    "RARE_B:\n\t"
    "s_nop 7\n\t"
    "s_nop 7\n\t"
    "v_accvgpr_read_b32 v252, a0\n\t"
    "v_accvgpr_read_b32 v253, a1\n\t"
    "v_accvgpr_read_b32 v254, a2\n\t"
    "v_accvgpr_read_b32 v255, a3\n\t"
    "v_mul_f32 v252, v252, v220\n\t"
    "v_mul_f32 v253, v253, v220\n\t"
    "v_mul_f32 v254, v254, v220\n\t"
    "v_mul_f32 v255, v255, v220\n\t"
    "v_accvgpr_write_b32 a0, v252\n\t"
    "v_accvgpr_write_b32 a1, v253\n\t"
    "v_accvgpr_write_b32 a2, v254\n\t"
    "v_accvgpr_write_b32 a3, v255\n\t"
    "v_accvgpr_read_b32 v252, a4\n\t"
    "v_accvgpr_read_b32 v253, a5\n\t"
    "v_accvgpr_read_b32 v254, a6\n\t"
    "v_accvgpr_read_b32 v255, a7\n\t"
    "v_mul_f32 v252, v252, v221\n\t"
    "v_mul_f32 v253, v253, v221\n\t"
    "v_mul_f32 v254, v254, v221\n\t"
    "v_mul_f32 v255, v255, v221\n\t"
    "v_accvgpr_write_b32 a4, v252\n\t"
    "v_accvgpr_write_b32 a5, v253\n\t"
    "v_accvgpr_write_b32 a6, v254\n\t"
    "v_accvgpr_write_b32 a7, v255\n\t"
    "v_accvgpr_read_b32 v252, a8\n\t"
    "v_accvgpr_read_b32 v253, a9\n\t"
    "v_accvgpr_read_b32 v254, a10\n\t"
    "v_accvgpr_read_b32 v255, a11\n\t"
    "v_mul_f32 v252, v252, v222\n\t"
    "v_mul_f32 v253, v253, v222\n\t"
    "v_mul_f32 v254, v254, v222\n\t"
    "v_mul_f32 v255, v255, v222\n\t"
    "v_accvgpr_write_b32 a8, v252\n\t"
    "v_accvgpr_write_b32 a9, v253\n\t"
    "v_accvgpr_write_b32 a10, v254\n\t"
    "v_accvgpr_write_b32 a11, v255\n\t"
    "v_accvgpr_read_b32 v252, a12\n\t"
    "v_accvgpr_read_b32 v253, a13\n\t"
    "v_accvgpr_read_b32 v254, a14\n\t"
    "v_accvgpr_read_b32 v255, a15\n\t"
    "v_mul_f32 v252, v252, v223\n\t"
    "v_mul_f32 v253, v253, v223\n\t"
    "v_mul_f32 v254, v254, v223\n\t"
    "v_mul_f32 v255, v255, v223\n\t"
    "v_accvgpr_write_b32 a12, v252\n\t"
    "v_accvgpr_write_b32 a13, v253\n\t"
    "v_accvgpr_write_b32 a14, v254\n\t"
    "v_accvgpr_write_b32 a15, v255\n\t"
    "v_accvgpr_read_b32 v252, a16\n\t"
    "v_accvgpr_read_b32 v253, a17\n\t"
    "v_accvgpr_read_b32 v254, a18\n\t"
    "v_accvgpr_read_b32 v255, a19\n\t"
    "v_mul_f32 v252, v252, v220\n\t"
    "v_mul_f32 v253, v253, v220\n\t"
    "v_mul_f32 v254, v254, v220\n\t"
    "v_mul_f32 v255, v255, v220\n\t"
    "v_accvgpr_write_b32 a16, v252\n\t"
    "v_accvgpr_write_b32 a17, v253\n\t"
    "v_accvgpr_write_b32 a18, v254\n\t"
    "v_accvgpr_write_b32 a19, v255\n\t"
    "v_accvgpr_read_b32 v252, a20\n\t"
    "v_accvgpr_read_b32 v253, a21\n\t"
    "v_accvgpr_read_b32 v254, a22\n\t"
    "v_accvgpr_read_b32 v255, a23\n\t"
    "v_mul_f32 v252, v252, v221\n\t"
    "v_mul_f32 v253, v253, v221\n\t"
    "v_mul_f32 v254, v254, v221\n\t"
    "v_mul_f32 v255, v255, v221\n\t"
    "v_accvgpr_write_b32 a20, v252\n\t"
    "v_accvgpr_write_b32 a21, v253\n\t"
    "v_accvgpr_write_b32 a22, v254\n\t"
    "v_accvgpr_write_b32 a23, v255\n\t"
    "v_accvgpr_read_b32 v252, a24\n\t"
    "v_accvgpr_read_b32 v253, a25\n\t"
    "v_accvgpr_read_b32 v254, a26\n\t"
    "v_accvgpr_read_b32 v255, a27\n\t"
    "v_mul_f32 v252, v252, v222\n\t"
    "v_mul_f32 v253, v253, v222\n\t"
    "v_mul_f32 v254, v254, v222\n\t"
    "v_mul_f32 v255, v255, v222\n\t"
    "v_accvgpr_write_b32 a24, v252\n\t"
    "v_accvgpr_write_b32 a25, v253\n\t"
    "v_accvgpr_write_b32 a26, v254\n\t"
    "v_accvgpr_write_b32 a27, v255\n\t"
    "v_accvgpr_read_b32 v252, a28\n\t"
    "v_accvgpr_read_b32 v253, a29\n\t"
    "v_accvgpr_read_b32 v254, a30\n\t"
    "v_accvgpr_read_b32 v255, a31\n\t"
    "v_mul_f32 v252, v252, v223\n\t"
    "v_mul_f32 v253, v253, v223\n\t"
    "v_mul_f32 v254, v254, v223\n\t"
    "v_mul_f32 v255, v255, v223\n\t"
    "v_accvgpr_write_b32 a28, v252\n\t"
    "v_accvgpr_write_b32 a29, v253\n\t"
    "v_accvgpr_write_b32 a30, v254\n\t"
    "v_accvgpr_write_b32 a31, v255\n\t"
    "v_accvgpr_read_b32 v252, a32\n\t"
    "v_accvgpr_read_b32 v253, a33\n\t"
    "v_accvgpr_read_b32 v254, a34\n\t"
    "v_accvgpr_read_b32 v255, a35\n\t"
    "v_mul_f32 v252, v252, v220\n\t"
    "v_mul_f32 v253, v253, v220\n\t"
    "v_mul_f32 v254, v254, v220\n\t"
    "v_mul_f32 v255, v255, v220\n\t"
    "v_accvgpr_write_b32 a32, v252\n\t"
    "v_accvgpr_write_b32 a33, v253\n\t"
    "v_accvgpr_write_b32 a34, v254\n\t"
    "v_accvgpr_write_b32 a35, v255\n\t"
    "v_accvgpr_read_b32 v252, a36\n\t"
    "v_accvgpr_read_b32 v253, a37\n\t"
    "v_accvgpr_read_b32 v254, a38\n\t"
    "v_accvgpr_read_b32 v255, a39\n\t"
    "v_mul_f32 v252, v252, v221\n\t"
    "v_mul_f32 v253, v253, v221\n\t"
    "v_mul_f32 v254, v254, v221\n\t"
    "v_mul_f32 v255, v255, v221\n\t"
    "v_accvgpr_write_b32 a36, v252\n\t"
    "v_accvgpr_write_b32 a37, v253\n\t"
    "v_accvgpr_write_b32 a38, v254\n\t"
    "v_accvgpr_write_b32 a39, v255\n\t"
    "v_accvgpr_read_b32 v252, a40\n\t"
    "v_accvgpr_read_b32 v253, a41\n\t"
    "v_accvgpr_read_b32 v254, a42\n\t"
    "v_accvgpr_read_b32 v255, a43\n\t"
    "v_mul_f32 v252, v252, v222\n\t"
    "v_mul_f32 v253, v253, v222\n\t"
    "v_mul_f32 v254, v254, v222\n\t"
    "v_mul_f32 v255, v255, v222\n\t"
    "v_accvgpr_write_b32 a40, v252\n\t"
    "v_accvgpr_write_b32 a41, v253\n\t"
    "v_accvgpr_write_b32 a42, v254\n\t"
    "v_accvgpr_write_b32 a43, v255\n\t"
    "v_accvgpr_read_b32 v252, a44\n\t"
    "v_accvgpr_read_b32 v253, a45\n\t"
    "v_accvgpr_read_b32 v254, a46\n\t"
    "v_accvgpr_read_b32 v255, a47\n\t"
    "v_mul_f32 v252, v252, v223\n\t"
    "v_mul_f32 v253, v253, v223\n\t"
    "v_mul_f32 v254, v254, v223\n\t"
    "v_mul_f32 v255, v255, v223\n\t"
    "v_accvgpr_write_b32 a44, v252\n\t"
    "v_accvgpr_write_b32 a45, v253\n\t"
    "v_accvgpr_write_b32 a46, v254\n\t"
    "v_accvgpr_write_b32 a47, v255\n\t"
    "v_accvgpr_read_b32 v252, a48\n\t"
    "v_accvgpr_read_b32 v253, a49\n\t"
    "v_accvgpr_read_b32 v254, a50\n\t"
    "v_accvgpr_read_b32 v255, a51\n\t"
    "v_mul_f32 v252, v252, v220\n\t"
    "v_mul_f32 v253, v253, v220\n\t"
    "v_mul_f32 v254, v254, v220\n\t"
    "v_mul_f32 v255, v255, v220\n\t"
    "v_accvgpr_write_b32 a48, v252\n\t"
    "v_accvgpr_write_b32 a49, v253\n\t"
    "v_accvgpr_write_b32 a50, v254\n\t"
    "v_accvgpr_write_b32 a51, v255\n\t"
    "v_accvgpr_read_b32 v252, a52\n\t"
    "v_accvgpr_read_b32 v253, a53\n\t"
    "v_accvgpr_read_b32 v254, a54\n\t"
    "v_accvgpr_read_b32 v255, a55\n\t"
    "v_mul_f32 v252, v252, v221\n\t"
    "v_mul_f32 v253, v253, v221\n\t"
    "v_mul_f32 v254, v254, v221\n\t"
    "v_mul_f32 v255, v255, v221\n\t"
    "v_accvgpr_write_b32 a52, v252\n\t"
    "v_accvgpr_write_b32 a53, v253\n\t"
    "v_accvgpr_write_b32 a54, v254\n\t"
    "v_accvgpr_write_b32 a55, v255\n\t"
    "v_accvgpr_read_b32 v252, a56\n\t"
    "v_accvgpr_read_b32 v253, a57\n\t"
    "v_accvgpr_read_b32 v254, a58\n\t"
    "v_accvgpr_read_b32 v255, a59\n\t"
    "v_mul_f32 v252, v252, v222\n\t"
    "v_mul_f32 v253, v253, v222\n\t"
    "v_mul_f32 v254, v254, v222\n\t"
    "v_mul_f32 v255, v255, v222\n\t"
    "v_accvgpr_write_b32 a56, v252\n\t"
    "v_accvgpr_write_b32 a57, v253\n\t"
    "v_accvgpr_write_b32 a58, v254\n\t"
    "v_accvgpr_write_b32 a59, v255\n\t"
    "v_accvgpr_read_b32 v252, a60\n\t"
    "v_accvgpr_read_b32 v253, a61\n\t"
    "v_accvgpr_read_b32 v254, a62\n\t"
    "v_accvgpr_read_b32 v255, a63\n\t"
    "v_mul_f32 v252, v252, v223\n\t"
    "v_mul_f32 v253, v253, v223\n\t"
    "v_mul_f32 v254, v254, v223\n\t"
    "v_mul_f32 v255, v255, v223\n\t"
    "v_accvgpr_write_b32 a60, v252\n\t"
    "v_accvgpr_write_b32 a61, v253\n\t"
    "v_accvgpr_write_b32 a62, v254\n\t"
    "v_accvgpr_write_b32 a63, v255\n\t"
    "v_accvgpr_read_b32 v252, a64\n\t"
    "v_accvgpr_read_b32 v253, a65\n\t"
    "v_accvgpr_read_b32 v254, a66\n\t"
    "v_accvgpr_read_b32 v255, a67\n\t"
    "v_mul_f32 v252, v252, v220\n\t"
    "v_mul_f32 v253, v253, v220\n\t"
    "v_mul_f32 v254, v254, v220\n\t"
    "v_mul_f32 v255, v255, v220\n\t"
    "v_accvgpr_write_b32 a64, v252\n\t"
    "v_accvgpr_write_b32 a65, v253\n\t"
    "v_accvgpr_write_b32 a66, v254\n\t"
    "v_accvgpr_write_b32 a67, v255\n\t"
    "v_accvgpr_read_b32 v252, a68\n\t"
    "v_accvgpr_read_b32 v253, a69\n\t"
    "v_accvgpr_read_b32 v254, a70\n\t"
    "v_accvgpr_read_b32 v255, a71\n\t"
    "v_mul_f32 v252, v252, v221\n\t"
    "v_mul_f32 v253, v253, v221\n\t"
    "v_mul_f32 v254, v254, v221\n\t"
    "v_mul_f32 v255, v255, v221\n\t"
    "v_accvgpr_write_b32 a68, v252\n\t"
    "v_accvgpr_write_b32 a69, v253\n\t"
    "v_accvgpr_write_b32 a70, v254\n\t"
    "v_accvgpr_write_b32 a71, v255\n\t"
    "v_accvgpr_read_b32 v252, a72\n\t"
    "v_accvgpr_read_b32 v253, a73\n\t"
    "v_accvgpr_read_b32 v254, a74\n\t"
    "v_accvgpr_read_b32 v255, a75\n\t"
    "v_mul_f32 v252, v252, v222\n\t"
    "v_mul_f32 v253, v253, v222\n\t"
    "v_mul_f32 v254, v254, v222\n\t"
    "v_mul_f32 v255, v255, v222\n\t"
    "v_accvgpr_write_b32 a72, v252\n\t"
    "v_accvgpr_write_b32 a73, v253\n\t"
    "v_accvgpr_write_b32 a74, v254\n\t"
    "v_accvgpr_write_b32 a75, v255\n\t"
    "v_accvgpr_read_b32 v252, a76\n\t"
    "v_accvgpr_read_b32 v253, a77\n\t"
    "v_accvgpr_read_b32 v254, a78\n\t"
    "v_accvgpr_read_b32 v255, a79\n\t"
    "v_mul_f32 v252, v252, v223\n\t"
    "v_mul_f32 v253, v253, v223\n\t"
    "v_mul_f32 v254, v254, v223\n\t"
    "v_mul_f32 v255, v255, v223\n\t"
    "v_accvgpr_write_b32 a76, v252\n\t"
    "v_accvgpr_write_b32 a77, v253\n\t"
    "v_accvgpr_write_b32 a78, v254\n\t"
    "v_accvgpr_write_b32 a79, v255\n\t"
    "v_accvgpr_read_b32 v252, a80\n\t"
    "v_accvgpr_read_b32 v253, a81\n\t"
    "v_accvgpr_read_b32 v254, a82\n\t"
    "v_accvgpr_read_b32 v255, a83\n\t"
    "v_mul_f32 v252, v252, v220\n\t"
    "v_mul_f32 v253, v253, v220\n\t"
    "v_mul_f32 v254, v254, v220\n\t"
    "v_mul_f32 v255, v255, v220\n\t"
    "v_accvgpr_write_b32 a80, v252\n\t"
    "v_accvgpr_write_b32 a81, v253\n\t"
    "v_accvgpr_write_b32 a82, v254\n\t"
    "v_accvgpr_write_b32 a83, v255\n\t"
    "v_accvgpr_read_b32 v252, a84\n\t"
    "v_accvgpr_read_b32 v253, a85\n\t"
    "v_accvgpr_read_b32 v254, a86\n\t"
    "v_accvgpr_read_b32 v255, a87\n\t"
    "v_mul_f32 v252, v252, v221\n\t"
    "v_mul_f32 v253, v253, v221\n\t"
    "v_mul_f32 v254, v254, v221\n\t"
    "v_mul_f32 v255, v255, v221\n\t"
    "v_accvgpr_write_b32 a84, v252\n\t"
    "v_accvgpr_write_b32 a85, v253\n\t"
    "v_accvgpr_write_b32 a86, v254\n\t"
    "v_accvgpr_write_b32 a87, v255\n\t"
    "v_accvgpr_read_b32 v252, a88\n\t"
    "v_accvgpr_read_b32 v253, a89\n\t"
    "v_accvgpr_read_b32 v254, a90\n\t"
    "v_accvgpr_read_b32 v255, a91\n\t"
    "v_mul_f32 v252, v252, v222\n\t"
    "v_mul_f32 v253, v253, v222\n\t"
    "v_mul_f32 v254, v254, v222\n\t"
    "v_mul_f32 v255, v255, v222\n\t"
    "v_accvgpr_write_b32 a88, v252\n\t"
    "v_accvgpr_write_b32 a89, v253\n\t"
    "v_accvgpr_write_b32 a90, v254\n\t"
    "v_accvgpr_write_b32 a91, v255\n\t"
    "v_accvgpr_read_b32 v252, a92\n\t"
    "v_accvgpr_read_b32 v253, a93\n\t"
    "v_accvgpr_read_b32 v254, a94\n\t"
    "v_accvgpr_read_b32 v255, a95\n\t"
    "v_mul_f32 v252, v252, v223\n\t"
    "v_mul_f32 v253, v253, v223\n\t"
    "v_mul_f32 v254, v254, v223\n\t"
    "v_mul_f32 v255, v255, v223\n\t"
    "v_accvgpr_write_b32 a92, v252\n\t"
    "v_accvgpr_write_b32 a93, v253\n\t"
    "v_accvgpr_write_b32 a94, v254\n\t"
    "v_accvgpr_write_b32 a95, v255\n\t"
    "v_accvgpr_read_b32 v252, a96\n\t"
    "v_accvgpr_read_b32 v253, a97\n\t"
    "v_accvgpr_read_b32 v254, a98\n\t"
    "v_accvgpr_read_b32 v255, a99\n\t"
    "v_mul_f32 v252, v252, v220\n\t"
    "v_mul_f32 v253, v253, v220\n\t"
    "v_mul_f32 v254, v254, v220\n\t"
    "v_mul_f32 v255, v255, v220\n\t"
    "v_accvgpr_write_b32 a96, v252\n\t"
    "v_accvgpr_write_b32 a97, v253\n\t"
    "v_accvgpr_write_b32 a98, v254\n\t"
    "v_accvgpr_write_b32 a99, v255\n\t"
    "v_accvgpr_read_b32 v252, a100\n\t"
    "v_accvgpr_read_b32 v253, a101\n\t"
    "v_accvgpr_read_b32 v254, a102\n\t"
    "v_accvgpr_read_b32 v255, a103\n\t"
    "v_mul_f32 v252, v252, v221\n\t"
    "v_mul_f32 v253, v253, v221\n\t"
    "v_mul_f32 v254, v254, v221\n\t"
    "v_mul_f32 v255, v255, v221\n\t"
    "v_accvgpr_write_b32 a100, v252\n\t"
    "v_accvgpr_write_b32 a101, v253\n\t"
    "v_accvgpr_write_b32 a102, v254\n\t"
    "v_accvgpr_write_b32 a103, v255\n\t"
    "v_accvgpr_read_b32 v252, a104\n\t"
    "v_accvgpr_read_b32 v253, a105\n\t"
    "v_accvgpr_read_b32 v254, a106\n\t"
    "v_accvgpr_read_b32 v255, a107\n\t"
    "v_mul_f32 v252, v252, v222\n\t"
    "v_mul_f32 v253, v253, v222\n\t"
    "v_mul_f32 v254, v254, v222\n\t"
    "v_mul_f32 v255, v255, v222\n\t"
    "v_accvgpr_write_b32 a104, v252\n\t"
    "v_accvgpr_write_b32 a105, v253\n\t"
    "v_accvgpr_write_b32 a106, v254\n\t"
    "v_accvgpr_write_b32 a107, v255\n\t"
    "v_accvgpr_read_b32 v252, a108\n\t"
    "v_accvgpr_read_b32 v253, a109\n\t"
    "v_accvgpr_read_b32 v254, a110\n\t"
    "v_accvgpr_read_b32 v255, a111\n\t"
    "v_mul_f32 v252, v252, v223\n\t"
    "v_mul_f32 v253, v253, v223\n\t"
    "v_mul_f32 v254, v254, v223\n\t"
    "v_mul_f32 v255, v255, v223\n\t"
    "v_accvgpr_write_b32 a108, v252\n\t"
    "v_accvgpr_write_b32 a109, v253\n\t"
    "v_accvgpr_write_b32 a110, v254\n\t"
    "v_accvgpr_write_b32 a111, v255\n\t"
    "v_accvgpr_read_b32 v252, a112\n\t"
    "v_accvgpr_read_b32 v253, a113\n\t"
    "v_accvgpr_read_b32 v254, a114\n\t"
    "v_accvgpr_read_b32 v255, a115\n\t"
    "v_mul_f32 v252, v252, v220\n\t"
    "v_mul_f32 v253, v253, v220\n\t"
    "v_mul_f32 v254, v254, v220\n\t"
    "v_mul_f32 v255, v255, v220\n\t"
    "v_accvgpr_write_b32 a112, v252\n\t"
    "v_accvgpr_write_b32 a113, v253\n\t"
    "v_accvgpr_write_b32 a114, v254\n\t"
    "v_accvgpr_write_b32 a115, v255\n\t"
    "v_accvgpr_read_b32 v252, a116\n\t"
    "v_accvgpr_read_b32 v253, a117\n\t"
    "v_accvgpr_read_b32 v254, a118\n\t"
    "v_accvgpr_read_b32 v255, a119\n\t"
    "v_mul_f32 v252, v252, v221\n\t"
    "v_mul_f32 v253, v253, v221\n\t"
    "v_mul_f32 v254, v254, v221\n\t"
    "v_mul_f32 v255, v255, v221\n\t"
    "v_accvgpr_write_b32 a116, v252\n\t"
    "v_accvgpr_write_b32 a117, v253\n\t"
    "v_accvgpr_write_b32 a118, v254\n\t"
    "v_accvgpr_write_b32 a119, v255\n\t"
    "v_accvgpr_read_b32 v252, a120\n\t"
    "v_accvgpr_read_b32 v253, a121\n\t"
    "v_accvgpr_read_b32 v254, a122\n\t"
    "v_accvgpr_read_b32 v255, a123\n\t"
    "v_mul_f32 v252, v252, v222\n\t"
    "v_mul_f32 v253, v253, v222\n\t"
    "v_mul_f32 v254, v254, v222\n\t"
    "v_mul_f32 v255, v255, v222\n\t"
    "v_accvgpr_write_b32 a120, v252\n\t"
    "v_accvgpr_write_b32 a121, v253\n\t"
    "v_accvgpr_write_b32 a122, v254\n\t"
    "v_accvgpr_write_b32 a123, v255\n\t"
    "v_accvgpr_read_b32 v252, a124\n\t"
    "v_accvgpr_read_b32 v253, a125\n\t"
    "v_accvgpr_read_b32 v254, a126\n\t"
    "v_accvgpr_read_b32 v255, a127\n\t"
    "v_mul_f32 v252, v252, v223\n\t"
    "v_mul_f32 v253, v253, v223\n\t"
    "v_mul_f32 v254, v254, v223\n\t"
    "v_mul_f32 v255, v255, v223\n\t"
    "v_accvgpr_write_b32 a124, v252\n\t"
    "v_accvgpr_write_b32 a125, v253\n\t"
    "v_accvgpr_write_b32 a126, v254\n\t"
    "v_accvgpr_write_b32 a127, v255\n\t"
    "v_mul_f32 v128, v128, v220\n\t"
    "v_mul_f32 v129, v129, v220\n\t"
    "v_mul_f32 v130, v130, v220\n\t"
    "v_mul_f32 v131, v131, v220\n\t"
    "v_mul_f32 v132, v132, v221\n\t"
    "v_mul_f32 v133, v133, v221\n\t"
    "v_mul_f32 v134, v134, v221\n\t"
    "v_mul_f32 v135, v135, v221\n\t"
    "v_mul_f32 v136, v136, v222\n\t"
    "v_mul_f32 v137, v137, v222\n\t"
    "v_mul_f32 v138, v138, v222\n\t"
    "v_mul_f32 v139, v139, v222\n\t"
    "v_mul_f32 v140, v140, v223\n\t"
    "v_mul_f32 v141, v141, v223\n\t"
    "v_mul_f32 v142, v142, v223\n\t"
    "v_mul_f32 v143, v143, v223\n\t"
    "v_mul_f32 v144, v144, v220\n\t"
    "v_mul_f32 v145, v145, v220\n\t"
    "v_mul_f32 v146, v146, v220\n\t"
    "v_mul_f32 v147, v147, v220\n\t"
    "v_mul_f32 v148, v148, v221\n\t"
    "v_mul_f32 v149, v149, v221\n\t"
    "v_mul_f32 v150, v150, v221\n\t"
    "v_mul_f32 v151, v151, v221\n\t"
    "v_mul_f32 v152, v152, v222\n\t"
    "v_mul_f32 v153, v153, v222\n\t"
    "v_mul_f32 v154, v154, v222\n\t"
    "v_mul_f32 v155, v155, v222\n\t"
    "v_mul_f32 v156, v156, v223\n\t"
    "v_mul_f32 v157, v157, v223\n\t"
    "v_mul_f32 v158, v158, v223\n\t"
    "v_mul_f32 v159, v159, v223\n\t"
    "v_mul_f32 v160, v160, v220\n\t"
    "v_mul_f32 v161, v161, v220\n\t"
    "v_mul_f32 v162, v162, v220\n\t"
    "v_mul_f32 v163, v163, v220\n\t"
    "v_mul_f32 v164, v164, v221\n\t"
    "v_mul_f32 v165, v165, v221\n\t"
    "v_mul_f32 v166, v166, v221\n\t"
    "v_mul_f32 v167, v167, v221\n\t"
    "v_mul_f32 v168, v168, v222\n\t"
    "v_mul_f32 v169, v169, v222\n\t"
    "v_mul_f32 v170, v170, v222\n\t"
    "v_mul_f32 v171, v171, v222\n\t"
    "v_mul_f32 v172, v172, v223\n\t"
    "v_mul_f32 v173, v173, v223\n\t"
    "v_mul_f32 v174, v174, v223\n\t"
    "v_mul_f32 v175, v175, v223\n\t"
    "v_mul_f32 v176, v176, v220\n\t"
    "v_mul_f32 v177, v177, v220\n\t"
    "v_mul_f32 v178, v178, v220\n\t"
    "v_mul_f32 v179, v179, v220\n\t"
    "v_mul_f32 v180, v180, v221\n\t"
    "v_mul_f32 v181, v181, v221\n\t"
    "v_mul_f32 v182, v182, v221\n\t"
    "v_mul_f32 v183, v183, v221\n\t"
    "v_mul_f32 v184, v184, v222\n\t"
    "v_mul_f32 v185, v185, v222\n\t"
    "v_mul_f32 v186, v186, v222\n\t"
    "v_mul_f32 v187, v187, v222\n\t"
    "v_mul_f32 v188, v188, v223\n\t"
    "v_mul_f32 v189, v189, v223\n\t"
    "v_mul_f32 v190, v190, v223\n\t"
    "v_mul_f32 v191, v191, v223\n\t"
    "v_mul_f32 v228, v228, v220\n\t"
    "v_mul_f32 v229, v229, v220\n\t"
    "v_mul_f32 v230, v230, v220\n\t"
    "v_mul_f32 v231, v231, v220\n\t"
    "v_mul_f32 v232, v232, v221\n\t"
    "v_mul_f32 v233, v233, v221\n\t"
    "v_mul_f32 v234, v234, v221\n\t"
    "v_mul_f32 v235, v235, v221\n\t"
    "v_mul_f32 v236, v236, v222\n\t"
    "v_mul_f32 v237, v237, v222\n\t"
    "v_mul_f32 v238, v238, v222\n\t"
    "v_mul_f32 v239, v239, v222\n\t"
    "v_mul_f32 v240, v240, v223\n\t"
    "v_mul_f32 v241, v241, v223\n\t"
    "v_mul_f32 v242, v242, v223\n\t"
    "v_mul_f32 v243, v243, v223\n\t"
    "s_mov_b32 s72, 0\n\t"
    "s_add_u32 s76, s76, 4\n\t"
    "s_addc_u32 s77, s77, 0\n\t"
    "s_nop 1\n\t"
    "s_setpc_b64 s[76:77]\n\t"
    "DONE:\n\t"
    :
    : "{v200}"(ks0), "{v201}"(ks1), "{v202}"(vsrc), "{v203}"(stw), "{v220}"(qdir), "{v192}"((kb0 ^ 0u)), "{v204}"((stq ^ 0u)), "{v193}"((kb0 ^ 64u)), "{v205}"((stq ^ 64u)), "{v196}"((vb0 ^ 0u)), "{v197}"((vb0 ^ 32u)), "{v198}"((vb0 ^ 64u)), "{v199}"((vb0 ^ 96u)), "{v208}"((sto ^ 0u)), "{v209}"((sto ^ 32u)), "{v210}"((sto ^ 64u)), "{v211}"((sto ^ 96u)), "{v207}"(min(128u * tid, 43008u)), "{v206}"(min(128u * tid + 32768u, 43008u)), "{s[36:39]}"(srdK), "{s[40:43]}"(srdV), "{s[44:47]}"(srdQ), "{s[48:51]}"(srdO), "{s52}"(dsoff), "{s53}"(dsoff), "{s54}"(qoff), "{s55}"(qoff), "{s56}"(m0k), "{s57}"(m0v), "{s58}"(stage), "{s59}"(rot), "{s60}"(rot), "{s[96:99]}"(dtab), "{s81}"((pair & 3u))
    : "v0", "v1", "v2", "v3", "v4", "v5", "v6", "v7", "v8", "v9", "v10", "v11", "v12", "v13", "v14", "v15", "v16", "v17", "v18", "v19", "v20", "v21", "v22", "v23", "v24", "v25", "v26", "v27", "v28", "v29", "v30", "v31", "v32", "v33", "v34", "v35", "v36", "v37", "v38", "v39", "v40", "v41", "v42", "v43", "v44", "v45", "v46", "v47", "v48", "v49", "v50", "v51", "v52", "v53", "v54", "v55", "v56", "v57", "v58", "v59", "v60", "v61", "v62", "v63", "v64", "v65", "v66", "v67", "v68", "v69", "v70", "v71", "v72", "v73", "v74", "v75", "v76", "v77", "v78", "v79", "v80", "v81", "v82", "v83", "v84", "v85", "v86", "v87", "v88", "v89", "v90", "v91", "v92", "v93", "v94", "v95", "v96", "v97", "v98", "v99", "v100", "v101", "v102", "v103", "v104", "v105", "v106", "v107", "v108", "v109", "v110", "v111", "v112", "v113", "v114", "v115", "v116", "v117", "v118", "v119", "v120", "v121", "v122", "v123", "v124", "v125", "v126", "v127", "v128", "v129", "v130", "v131", "v132", "v133", "v134", "v135", "v136", "v137", "v138", "v139", "v140", "v141", "v142", "v143", "v144", "v145", "v146", "v147", "v148", "v149", "v150", "v151", "v152", "v153", "v154", "v155", "v156", "v157", "v158", "v159", "v160", "v161", "v162", "v163", "v164", "v165", "v166", "v167", "v168", "v169", "v170", "v171", "v172", "v173", "v174", "v175", "v176", "v177", "v178", "v179", "v180", "v181", "v182", "v183", "v184", "v185", "v186", "v187", "v188", "v189", "v190", "v191", "v194", "v195", "v212", "v213", "v214", "v215", "v216", "v217", "v218", "v219", "v221", "v222", "v223", "v224", "v225", "v226", "v227", "v228", "v229", "v230", "v231", "v232", "v233", "v234", "v235", "v236", "v237", "v238", "v239", "v240", "v241", "v242", "v243", "v244", "v245", "v246", "v247", "v248", "v249", "v250", "v251", "v252", "v253", "v254", "v255", "a0", "a1", "a2", "a3", "a4", "a5", "a6", "a7", "a8", "a9", "a10", "a11", "a12", "a13", "a14", "a15", "a16", "a17", "a18", "a19", "a20", "a21", "a22", "a23", "a24", "a25", "a26", "a27", "a28", "a29", "a30", "a31", "a32", "a33", "a34", "a35", "a36", "a37", "a38", "a39", "a40", "a41", "a42", "a43", "a44", "a45", "a46", "a47", "a48", "a49", "a50", "a51", "a52", "a53", "a54", "a55", "a56", "a57", "a58", "a59", "a60", "a61", "a62", "a63", "a64", "a65", "a66", "a67", "a68", "a69", "a70", "a71", "a72", "a73", "a74", "a75", "a76", "a77", "a78", "a79", "a80", "a81", "a82", "a83", "a84", "a85", "a86", "a87", "a88", "a89", "a90", "a91", "a92", "a93", "a94", "a95", "a96", "a97", "a98", "a99", "a100", "a101", "a102", "a103", "a104", "a105", "a106", "a107", "a108", "a109", "a110", "a111", "a112", "a113", "a114", "a115", "a116", "a117", "a118", "a119", "a120", "a121", "a122", "a123", "a124", "a125", "a126", "a127", "a128", "a129", "a130", "a131", "a132", "a133", "a134", "a135", "a136", "a137", "a138", "a139", "a140", "a141", "a142", "a143", "a144", "a145", "a146", "a147", "a148", "a149", "a150", "a151", "a152", "a153", "a154", "a155", "a156", "a157", "a158", "a159", "a160", "a161", "a162", "a163", "a164", "a165", "a166", "a167", "a168", "a169", "a170", "a171", "a172", "a173", "a174", "a175", "a176", "a177", "a178", "a179", "a180", "a181", "a182", "a183", "a184", "a185", "a186", "a187", "a188", "a189", "a190", "a191", "a192", "a193", "a194", "a195", "a196", "a197", "a198", "a199", "a200", "a201", "a202", "a203", "a204", "a205", "a206", "a207", "a208", "a209", "a210", "a211", "a212", "a213", "a214", "a215", "a216", "a217", "a218", "a219", "a220", "a221", "a222", "a223", "a224", "a225", "a226", "a227", "a228", "a229", "a230", "a231", "a232", "a233", "a234", "a235", "a236", "a237", "a238", "a239", "a240", "a241", "a242", "a243", "a244", "a245", "a246", "a247", "a248", "a249", "a250", "a251", "a252", "a253", "a254", "a255", "s61", "s62", "s63", "s64", "s65", "s66", "s67", "s68", "s69", "s70", "s71", "s72", "s73", "s74", "s75", "s76", "s77", "s78", "s79", "s80", "s82", "s83", "s84", "s85", "s86", "s87", "s88", "s89", "s90", "s91", "s92", "s93", "s94", "s95", "vcc", "scc", "memory");
}

extern "C" void kernel_launch(void* const* d_in, const int* in_sizes, int n_in, void* d_out, int out_size, void* d_ws, size_t ws_size,
                              hipStream_t stream) {
  mha_fwd_kernel<<<256, 256, 0, stream>>>(d_in[0], d_in[1], d_in[2], d_out, (unsigned*)d_ws);
}
```
